```python
import torch

def _softmax(x, axis=-1):
    return torch.softmax(x, dim=axis)
def _gelu(x):
    return torch.nn.functional.gelu(x, approximate="tanh")
def _silu(x):
    return torch.nn.functional.silu(x)
def _sigmoid(x):
    return torch.sigmoid(x)
def _layernorm(x, axis=-1, eps=1e-5):
    m = x.mean(dim=axis, keepdim=True)
    v = x.var(dim=axis, keepdim=True, unbiased=False)
    return (x - m) / torch.sqrt(v + eps)
def _scatter_add(tgt, idx, src, axis=0):
    return tgt.clone().index_add_(axis, idx, src)
def _segment_sum(src, seg, n):
    out = torch.zeros((n,) + src.shape[1:], dtype=src.dtype)
    return out.index_add_(0, seg, src)
def _pad(x, pw):
    flat = tuple(v for pair in reversed(pw) for v in pair)
    return torch.nn.functional.pad(x, flat)

kernel_name = 'gated_mix__mha_block_cached__geglu_ffn__decode'

def setup_inputs():

    _w = (lambda a: (lambda t: t.to(torch.bfloat16) if t.is_floating_point() else t)(torch.as_tensor(a)))
    def _A_setup():
        B, Sq, D = 8, 1, 1024
        H, Sk, Dh = 32, 4096, 128
        return {
            "x": _w((lambda *s: torch.randn(*s, dtype=torch.bfloat16) * 0.05)(B, Sq, D)),
            "k_cache": _w((lambda *s: torch.randn(*s, dtype=torch.bfloat16) * 6.4)(B, H, Sk, Dh)),
            "v_cache": _w((lambda *s: torch.randn(*s, dtype=torch.bfloat16) * 0.4)(B, H, Sk, Dh)),
            "wq": _w((lambda *s: torch.randn(*s, dtype=torch.bfloat16) * 6.4)(D, H * Dh) * (D ** -0.5)),
            "wo": _w((lambda *s: torch.randn(*s, dtype=torch.bfloat16) * 0.4)(H * Dh, D) * ((H * Dh) ** -0.5)),
        }
    _A_in = _A_setup()

    def _B_setup():
        B, S, D, F = 8, 1, 1024, 3584
        return {
            "x": _w((lambda *s: torch.randn(*s, dtype=torch.bfloat16) * 0.05)(B, S, D)),
            "w_gate": _w((lambda *s: torch.randn(*s, dtype=torch.bfloat16) * 0.05)(D, F)),
            "w_up": _w((lambda *s: torch.randn(*s, dtype=torch.bfloat16) * 0.05)(D, F)),
            "w_down": _w((lambda *s: torch.randn(*s, dtype=torch.bfloat16) * 0.05)(F, D)),
        }
    _B_in = _B_setup()

    return {
        "A_x": _A_in["x"],
        "A_k_cache": _A_in["k_cache"],
        "A_v_cache": _A_in["v_cache"],
        "A_wq": _A_in["wq"],
        "A_wo": _A_in["wo"],
        "B_x": _B_in["x"],
        "B_w_gate": _B_in["w_gate"],
        "B_w_up": _B_in["w_up"],
        "B_w_down": _B_in["w_down"],
        "gate_w": _w((lambda *s: torch.randn(*s, dtype=torch.bfloat16) * 0.05)(1024) * 16.0),
    }

def reference(A_x, A_k_cache, A_v_cache, A_wq, A_wo, B_x, B_w_gate, B_w_up, B_w_down, gate_w):
    def _A_ref(x, k_cache, v_cache, wq, wo):
        B, Sq, D = x.shape
        H, Dh = 32, 128
        q = (lambda x, *d: x.permute(*d))((x @ wq).reshape(B, Sq, H, Dh), 0, 2, 1, 3)
        scores = torch.einsum("bhqd,bhkd->bhqk", q, k_cache) * (Dh ** -0.5)
        p = _softmax(scores, axis=-1)
        o = torch.einsum("bhqk,bhkd->bhqd", p, v_cache)
        return (lambda x, *d: x.permute(*d))(o, 0, 2, 1, 3).reshape(B, Sq, H * Dh) @ wo

    def _B_ref(x, w_gate, w_up, w_down):
        return (_gelu(x @ w_gate) * (x @ w_up)) @ w_down

    x = A_x
    g = _sigmoid((x * gate_w).sum(-1, keepdims=True))
    a = _A_ref(x, A_k_cache, A_v_cache, A_wq, A_wo)
    b = _B_ref(x, B_w_gate, B_w_up, B_w_down)
    return g * a + (1 - g) * b
```

```cpp
#include <hip/hip_runtime.h>
#include <hip/hip_bf16.h>

#define GAS __attribute__((address_space(1)))
#define LAS __attribute__((address_space(3)))
typedef unsigned v4u __attribute__((ext_vector_type(4)));
typedef float f32x4 __attribute__((ext_vector_type(4)));
typedef float f32x2 __attribute__((ext_vector_type(2)));
typedef short bf16x8 __attribute__((ext_vector_type(8)));
typedef GAS unsigned gu32;
typedef GAS unsigned long long gu64;
#define RLX_AGENT __ATOMIC_RELAXED, __HIP_MEMORY_SCOPE_AGENT
#define RLX_WG __ATOMIC_RELAXED, __HIP_MEMORY_SCOPE_WORKGROUP

namespace {

constexpr int NB = 8, DM = 1024, NH = 32, SK = 4096, DH = 128, FF = 3584;
constexpr int FS = FF / NH;
constexpr int NCW = 8, NTHR = 64 * (NCW + 1);
constexpr int SLOT = 16384, NSLOT = 8;
constexpr int F_V = 96, F_W = 174, F_END = 182;
#define DEPTH 3
#define W_AUX 0

constexpr int SC_OFF = NSLOT * SLOT;
constexpr int X_OFF = SC_OFF + 16384;
constexpr int SCR_OFF = X_OFF + 2048;
constexpr int Q_OFF = SCR_OFF + 8192;
constexpr int O_OFF = Q_OFF + 256;
constexpr int HH_OFF = O_OFF + 256;
constexpr int CTL_OFF = HH_OFF + 256;
constexpr int LDS_BYTES = CTL_OFF + 256;
constexpr int C_LANDED = 0, C_BAR = 1, C_FREE = 8, C_WMAX = 16, C_WSUM = 24, C_GP = 32;
static_assert(LDS_BYTES <= 160 * 1024, "LDS");

constexpr size_t WS_EPOCH = 0, WS_GRAN = 4096;

constexpr unsigned SPIN_CAP = 1u << 22;

__device__ __forceinline__ f32x4 mfma(v4u a, v4u b, f32x4 c) {
    return __builtin_amdgcn_mfma_f32_16x16x32_bf16(__builtin_bit_cast(bf16x8, a), __builtin_bit_cast(bf16x8, b), c, 0, 0, 0);
}
__device__ __forceinline__ float bf_lo(unsigned u) { return __builtin_bit_cast(float, u << 16); }
__device__ __forceinline__ float bf_hi(unsigned u) { return __builtin_bit_cast(float, u & 0xffff0000u); }
__device__ __forceinline__ float rbf(float f) { return (float)(__bf16)f; }
__device__ __forceinline__ unsigned bf_bits(float f) { return (unsigned)__builtin_bit_cast(unsigned short, (__bf16)f); }
__device__ __forceinline__ unsigned pack2(float lo, float hi) { return bf_bits(lo) | (bf_bits(hi) << 16); }
__device__ __forceinline__ unsigned long long rt() { return __builtin_amdgcn_s_memrealtime(); }
__device__ __forceinline__ unsigned rfl(unsigned v) { return (unsigned)__builtin_amdgcn_readfirstlane((int)v); }
#define LGKM0() asm volatile("s_waitcnt lgkmcnt(0)" ::: "memory")
#define CBAR() asm volatile("" ::: "memory")

template <bool NT> __device__ __forceinline__ void glds(const void* sbase, unsigned voff, unsigned lds) {
    unsigned keep;
    if constexpr (NT)
        asm volatile("s_mov_b32 %0, m0\n\ts_mov_b32 m0, %3\n\ts_nop 0\n\tglobal_load_lds_dwordx4 %1, %2 nt\n\ts_mov_b32 m0, %0"
                     : "=&s"(keep) : "v"(voff), "s"(sbase), "s"(lds) : "memory");
    else
        asm volatile("s_mov_b32 %0, m0\n\ts_mov_b32 m0, %3\n\ts_nop 0\n\tglobal_load_lds_dwordx4 %1, %2\n\ts_mov_b32 m0, %0"
                     : "=&s"(keep) : "v"(voff), "s"(sbase), "s"(lds) : "memory");
}

__device__ __forceinline__ void wait_ge(volatile LAS unsigned* w, unsigned need) {
    for (unsigned s = 0; rfl(*w) < need && s < SPIN_CAP; ++s) __builtin_amdgcn_s_sleep(1);
    CBAR();
}
__device__ __forceinline__ void cons_barrier(LAS unsigned* ctl, unsigned& phase, int lane) {
    LGKM0();
    if (lane == 0) __hip_atomic_fetch_add(ctl + C_BAR, 1u, RLX_WG);
    phase += NCW;
    for (unsigned s = 0; rfl(*(volatile LAS unsigned*)(ctl + C_BAR)) < phase && s < SPIN_CAP; ++s) __builtin_amdgcn_s_sleep(0);
    CBAR();
}
__device__ __forceinline__ void release_slot(LAS unsigned* ctl, int slot, unsigned n, int lane) {
    LGKM0();
    if (lane == 0) __hip_atomic_fetch_add(ctl + C_FREE + slot, n, RLX_WG);
    CBAR();
}

struct Sel { unsigned s[2][4]; };
__device__ __forceinline__ v4u diagA(unsigned S, const unsigned (&sel)[4]) {
    return v4u{__builtin_amdgcn_perm(0u, S, sel[0]), __builtin_amdgcn_perm(0u, S, sel[1]), __builtin_amdgcn_perm(0u, S, sel[2]), __builtin_amdgcn_perm(0u, S, sel[3])};
}

__device__ __forceinline__ int fresh_lane() {
    int l;
    asm volatile("v_mbcnt_lo_u32_b32 %0, -1, 0\n\tv_mbcnt_hi_u32_b32 %0, -1, %0" : "=v"(l));
    return l;
}
__device__ __forceinline__ Sel make_sel(int c) {
    Sel sel;
    const int dm = (c & 7) >> 1, hf = c & 1;
#pragma unroll
    for (int par = 0; par < 2; ++par)
#pragma unroll
        for (int d = 0; d < 4; ++d) {
            const unsigned lohi = (unsigned)(2 * par) | ((unsigned)(2 * par + 1) << 8);
            const unsigned v = hf ? (0x0c0cu | (lohi << 16)) : (0x0c0c0000u | lohi);
            sel.s[par][d] = (c < 8 && d == dm) ? v : 0x0c0c0c0cu;
        }
    return sel;
}

__device__ __forceinline__ f32x4 slot_rows64(const LAS unsigned char* slot, const LAS unsigned char* vec, const Sel& sel, int c, int g, f32x4 acc) {
    const LAS unsigned char* base = slot + g * 4096 + c * 16;
    f32x4 a1 = {0.f, 0.f, 0.f, 0.f};
#pragma unroll
    for (int hf = 0; hf < 2; ++hf) {
        const v4u s = *(const LAS v4u*)(vec + g * 32 + hf * 16);
        v4u bb[8];
#pragma unroll
        for (int t = 0; t < 8; ++t) bb[t] = *(const LAS v4u*)(base + (8 * hf + t) * 256);
        __builtin_amdgcn_sched_barrier(0);
#pragma unroll
        for (int u = 0; u < 4; ++u) {
            acc = mfma(diagA(s[u], sel.s[0]), bb[2 * u], acc);
            a1 = mfma(diagA(s[u], sel.s[1]), bb[2 * u + 1], a1);
            __builtin_amdgcn_sched_barrier(0);
        }
    }
    return acc + a1;
}

__device__ __forceinline__ float wave_max(float v) {
#pragma unroll
    for (int o = 32; o >= 1; o >>= 1) v = fmaxf(v, __shfl_xor(v, o));
    return v;
}
__device__ __forceinline__ float wave_sum(float v) {
#pragma unroll
    for (int o = 32; o >= 1; o >>= 1) v += __shfl_xor(v, o);
    return v;
}

__global__ void __launch_bounds__(NTHR)
fused_decode_kernel(const unsigned char* __restrict__ xin, const unsigned char* __restrict__ kc, const unsigned char* __restrict__ vc,
                    const unsigned char* __restrict__ wq, const unsigned char* __restrict__ wo, const unsigned char* __restrict__ xb,
                    const unsigned char* __restrict__ wg, const unsigned char* __restrict__ wu, const unsigned char* __restrict__ wd,
                    const unsigned char* __restrict__ gatew, unsigned char* __restrict__ out, unsigned char* __restrict__ ws,
                    int pace_kv, int pace_w) {
    extern __shared__ __attribute__((aligned(16))) unsigned char smem[];
    LAS unsigned char* L = (LAS unsigned char*)smem;
    LAS unsigned* ctl = (LAS unsigned*)(L + CTL_OFF);
    const int wave = __builtin_amdgcn_readfirstlane((int)threadIdx.x >> 6);
    const int h = blockIdx.x & (NH - 1), b = blockIdx.x >> 5;

    if (wave == 0) {
        const int lane = fresh_lane(), c = lane & 15, g = lane >> 4;
        const unsigned long long t0 = rt();
        __builtin_amdgcn_s_setprio(3);
        if (lane < 64) ctl[lane] = 0u;
        LGKM0();
        const unsigned lds0 = (unsigned)(size_t)L;
        const unsigned char* kb = kc + (size_t)blockIdx.x * (SK * DH * 2);
        const unsigned char* vb = vc + (size_t)blockIdx.x * (SK * DH * 2);
        unsigned vk[4], vgu[1];
#pragma unroll
        for (int p = 0; p < 4; ++p) vk[p] = g * 256 + ((c ^ (4 * p + g)) << 4);
        vgu[0] = g * (FF * 2) + h * (FS * 2) + (c < 13 ? c : 13) * 16;
        const unsigned vlin = lane * 16;
        volatile LAS unsigned* vctl = (volatile LAS unsigned*)ctl;
        unsigned due = 0;
        int pub = 0;
        bool barrier_done = false;
        for (int f = 0; f < F_END; ++f) {
            const int slot = f & 7;
            if (f == 3) { __builtin_amdgcn_s_barrier(); barrier_done = true; }
            if (f >= NSLOT) {
                const unsigned need = (unsigned)(f >> 3) * 8u;
                if (rfl(vctl[C_FREE + slot]) < need) {
                    asm volatile("s_waitcnt vmcnt(0)" ::: "memory");
                    pub = f; if (lane == 0) vctl[C_LANDED] = (unsigned)pub;
                    for (unsigned s = 0; rfl(vctl[C_FREE + slot]) < need && s < SPIN_CAP; ++s) __builtin_amdgcn_s_sleep(1);
                }
            }
            {
                const unsigned long long tdue = t0 + (due >> 4);
                while (rt() < tdue) __builtin_amdgcn_s_sleep(1);
            }
            const unsigned dst = lds0 + slot * SLOT;
            bool heavy = true;
            if (f < F_V) {
                const int f3 = f / 3;
                if (f - 3 * f3 == 2) {
                    const unsigned char* src = ((f3 & 1) ? wu : wg) + (size_t)(f3 >> 1) * 64 * (FF * 2);
#pragma unroll
                    for (int p = 0; p < 16; ++p) glds<false>(src + (size_t)p * 4 * (FF * 2), vgu[0], dst + p * 1024);
                    heavy = false;
                } else {
                    const unsigned char* src = kb + (size_t)(f - f3) * SLOT;
#pragma unroll
                    for (int p = 0; p < 16; ++p) glds<true>(src + (p >> 2) * 4096 + (p & 3) * 1024, vk[p & 3] - (0), dst + p * 1024);
                }
            } else if (f < F_W) {
                const int fp = f - F_V, q5 = fp / 5, r5 = fp - 5 * q5;
                if (fp < 70 && r5 == 4) {
                    const unsigned char* src = wd + ((size_t)h * FS + 2 * q5) * (DM * 2);
#pragma unroll
                    for (int p = 0; p < 16; ++p) glds<false>(src + (size_t)(28 * (p >> 2) + ((p >> 1) & 1)) * (DM * 2) + (p & 1) * 1024, vlin, dst + p * 1024);
                    heavy = false;
                } else {
                    const int j = fp < 70 ? 4 * q5 + r5 : fp - 14;
                    const unsigned char* src = vb + (size_t)j * SLOT;
#pragma unroll
                    for (int p = 0; p < 16; ++p) glds<true>(src + p * 1024, vlin, dst + p * 1024);
                }
            } else {
                const int k = f - F_W;
                const unsigned char* src = wo + ((size_t)h * DH + 64 + 2 * k) * (DM * 2);
#pragma unroll
                for (int p = 0; p < 16; ++p) glds<false>(src + (size_t)(16 * (p >> 2) + ((p >> 1) & 1)) * (DM * 2) + (p & 1) * 1024, vlin, dst + p * 1024);
                heavy = false;
            }
            due += heavy ? pace_kv : pace_w;
            asm volatile("s_waitcnt vmcnt(%0)" ::"i"(16 * DEPTH) : "memory");
            if (f + 1 - DEPTH > pub && barrier_done) { pub = f + 1 - DEPTH; if (lane == 0) vctl[C_LANDED] = (unsigned)pub; }
        }
#pragma unroll
        for (int d = DEPTH - 1; d >= 0; --d) {
            if (d == 2) asm volatile("s_waitcnt vmcnt(32)" ::: "memory");
            else if (d == 1) asm volatile("s_waitcnt vmcnt(16)" ::: "memory");
            else if (d == 0) asm volatile("s_waitcnt vmcnt(0)" ::: "memory");
            if (F_END - d > pub) { pub = F_END - d; if (lane == 0) vctl[C_LANDED] = (unsigned)pub; }
        }
        return;
    }

    const int w = wave - 1;
    volatile LAS unsigned* vctl = (volatile LAS unsigned*)ctl;
    unsigned bphase = 0;
    gu64* gran = (gu64*)(ws + WS_GRAN);
    gu32* epoch = (gu32*)(ws + WS_EPOCH) + b * 64;
    LAS float* scr = (LAS float*)(L + SCR_OFF);

    unsigned tag;
    {
        const int lane = fresh_lane(), c = lane & 15, g = lane >> 4;
        v4u xv = {0u, 0u, 0u, 0u}, gv = {0u, 0u, 0u, 0u};
        if (lane < 16) {
            xv = *(const GAS v4u*)(xin + (size_t)b * (DM * 2) + w * 256 + lane * 16);
            gv = *(const GAS v4u*)(gatew + w * 256 + lane * 16);
        }
        tag = __hip_atomic_load(epoch, RLX_AGENT);
        v4u wr[32];
        const __amdgpu_buffer_rsrc_t rq = __builtin_amdgcn_make_buffer_rsrc((void*)wq, 0, DM * NH * DH * 2, 0x00020000);
        const int voq = 32 * g * (NH * DH * 2) + c * 16, soq = 128 * w * (NH * DH * 2) + h * 256;
#pragma unroll
        for (int t = 0; t < 32; ++t) wr[t] = __builtin_amdgcn_raw_buffer_load_b128(rq, voq, soq + t * (NH * DH * 2), W_AUX);
        __builtin_amdgcn_sched_barrier(0);
        __builtin_amdgcn_s_barrier();
        if (lane < 16) *(LAS v4u*)(L + X_OFF + w * 256 + lane * 16) = xv;
        {
            float s = 0.f;
#pragma unroll
            for (int k = 0; k < 4; ++k) s += rbf(bf_lo(xv[k]) * bf_lo(gv[k])) + rbf(bf_hi(xv[k]) * bf_hi(gv[k]));
            s = wave_sum(s);
            if (lane == 0) ((LAS float*)ctl)[C_GP + w] = s;
        }
        LGKM0();
        __builtin_amdgcn_sched_barrier(0);
        const Sel sel = make_sel(c);
        f32x4 accq = {0.f, 0.f, 0.f, 0.f}, a1 = {0.f, 0.f, 0.f, 0.f};
#pragma unroll
        for (int t4 = 0; t4 < 4; ++t4) {
            const v4u s = *(const LAS v4u*)(L + X_OFF + (128 * w + 32 * g + 8 * t4) * 2);
#pragma unroll
            for (int u = 0; u < 4; ++u) {
                accq = mfma(diagA(s[u], sel.s[0]), wr[8 * t4 + 2 * u], accq);
                a1 = mfma(diagA(s[u], sel.s[1]), wr[8 * t4 + 2 * u + 1], a1);
            }
        }
        accq += a1;
        if (g < 2) *(LAS f32x4*)(scr + w * 256 + 8 * c + 4 * g) = accq;
    }
    const int lane = fresh_lane(), c = lane & 15, g = lane >> 4;
    cons_barrier(ctl, bphase, lane);
    const Sel sel = make_sel(c);
    tag += 1u;
    v4u qf[4];
    {
        float s0 = 0.f, s1 = 0.f;
#pragma unroll
        for (int ww = 0; ww < NCW; ++ww) { const f32x2 v = *(const LAS f32x2*)(scr + ww * 256 + 2 * lane); s0 += v[0]; s1 += v[1]; }
        ((LAS unsigned*)(L + Q_OFF))[lane] = pack2(s0, s1);
        LGKM0();
#pragma unroll
        for (int s = 0; s < 4; ++s) qf[s] = *(const LAS v4u*)(L + Q_OFF + (32 * s + 8 * g) * 2);
    }
    float gate;
    {
        float z = 0.f;
#pragma unroll
        for (int ww = 0; ww < NCW; ++ww) z += ((LAS float*)ctl)[C_GP + ww];
        z = rbf(z);
        gate = rbf(1.0f / (1.0f + __expf(-z)));
    }
    if (g < 2) {
#pragma unroll
        for (int i = 0; i < 4; ++i)
            __hip_atomic_store(gran + (size_t)blockIdx.x * DM + 128 * w + 8 * c + 4 * g + i, (unsigned long long)(tag - 1u) << 32, RLX_AGENT);
    }
    cons_barrier(ctl, bphase, lane);

    f32x4 accg = {0.f, 0.f, 0.f, 0.f}, accu = {0.f, 0.f, 0.f, 0.f};
    float mx = -INFINITY;
    LAS float* sc = (LAS float*)(L + SC_OFF);
    const LAS unsigned char* myslot = L + w * SLOT;
    for (int f = w; f < F_V; f += NSLOT) {
        wait_ge(vctl + C_LANDED, (unsigned)f + 1u);
        const int f3 = f / 3;
        if (f - 3 * f3 == 2) {
            const LAS unsigned char* vec = L + X_OFF + (f3 >> 1) * 128;
            const f32x4 t = slot_rows64(myslot, vec, sel, c, g, f32x4{0.f, 0.f, 0.f, 0.f});
            release_slot(ctl, w, 8u, lane);
            if (f3 & 1) accu += t; else accg += t;
        } else {
            f32x4 s4[4];
#pragma unroll
            for (int kg = 0; kg < 4; ++kg) {
                f32x4 a = {0.f, 0.f, 0.f, 0.f};
#pragma unroll
                for (int s = 0; s < 4; ++s) {
                    const v4u kf = *(const LAS v4u*)(myslot + (16 * kg + c) * 256 + (((4 * s + g) ^ c) << 4));
                    a = mfma(kf, qf[s], a);
                }
                s4[kg] = a;
            }
            release_slot(ctl, w, 8u, lane);
            const int kg = c & 3;
            f32x4 v = kg == 0 ? s4[0] : kg == 1 ? s4[1] : kg == 2 ? s4[2] : s4[3];
#pragma unroll
            for (int i = 0; i < 4; ++i) { v[i] = rbf(rbf(v[i]) * 0.08838834764831845f); mx = fmaxf(mx, v[i]); }
            if (c < 4) *(LAS f32x4*)(sc + (f - f3) * 64 + 16 * c + 4 * g) = v;
        }
    }
    mx = wave_max(mx);
    if (lane == 0) ((LAS float*)ctl)[C_WMAX + w] = mx;
    if (g < 2) { *(LAS f32x4*)(scr + w * 256 + 8 * c + 4 * g) = accg; *(LAS f32x4*)(scr + w * 256 + 128 + 8 * c + 4 * g) = accu; }
    cons_barrier(ctl, bphase, lane);
    {
        float M = ((LAS float*)ctl)[C_WMAX];
#pragma unroll
        for (int ww = 1; ww < NCW; ++ww) M = fmaxf(M, ((LAS float*)ctl)[C_WMAX + ww]);
        const f32x4 sa = *(const LAS f32x4*)(sc + 512 * w + 8 * lane), sb = *(const LAS f32x4*)(sc + 512 * w + 8 * lane + 4);
        float e[8];
#pragma unroll
        for (int i = 0; i < 4; ++i) { e[i] = __expf(sa[i] - M); e[4 + i] = __expf(sb[i] - M); }
        float s = ((e[0] + e[1]) + (e[2] + e[3])) + ((e[4] + e[5]) + (e[6] + e[7]));
        s = wave_sum(s);
        if (lane == 0) ((LAS float*)ctl)[C_WSUM + w] = s;
        if (w == 0 && lane < FS / 2) {
            float g0 = 0.f, g1 = 0.f, u0 = 0.f, u1 = 0.f;
#pragma unroll
            for (int ww = 0; ww < NCW; ++ww) {
                const f32x2 a = *(const LAS f32x2*)(scr + ww * 256 + 2 * lane), bb = *(const LAS f32x2*)(scr + ww * 256 + 128 + 2 * lane);
                g0 += a[0]; g1 += a[1]; u0 += bb[0]; u1 += bb[1];
            }
            g0 = rbf(g0); g1 = rbf(g1); u0 = rbf(u0); u1 = rbf(u1);
            const float k0 = 0.7978845608028654f, k1 = 0.044715f;
            const float ge0 = rbf(0.5f * g0 * (1.0f + tanhf(k0 * (g0 + k1 * g0 * g0 * g0))));
            const float ge1 = rbf(0.5f * g1 * (1.0f + tanhf(k0 * (g1 + k1 * g1 * g1 * g1))));
            ((LAS unsigned*)(L + HH_OFF))[lane] = pack2(ge0 * u0, ge1 * u1);
        }
        cons_barrier(ctl, bphase, lane);
        float Ls = 0.f;
#pragma unroll
        for (int ww = 0; ww < NCW; ++ww) Ls += ((LAS float*)ctl)[C_WSUM + ww];
        const float inv = 1.0f / Ls;
        v4u pk;
#pragma unroll
        for (int i = 0; i < 4; ++i) pk[i] = pack2(e[2 * i] * inv, e[2 * i + 1] * inv);
        *(LAS v4u*)(L + SC_OFF + (512 * w + 8 * lane) * 2) = pk;
    }
    cons_barrier(ctl, bphase, lane);

    v4u wor[16];
    {
        const __amdgpu_buffer_rsrc_t ro = __builtin_amdgcn_make_buffer_rsrc((void*)wo, 0, DM * NH * DH * 2, 0x00020000);
        const int voo = 16 * g * (DM * 2) + c * 16, soo = h * DH * (DM * 2) + w * 256;
#pragma unroll
        for (int t = 0; t < 16; ++t) wor[t] = __builtin_amdgcn_raw_buffer_load_b128(ro, voo, soo + t * (DM * 2), W_AUX);
        __builtin_amdgcn_sched_barrier(0);
    }
    f32x4 acco = {0.f, 0.f, 0.f, 0.f}, accb = {0.f, 0.f, 0.f, 0.f};
    for (int f = F_V; f < F_W; ++f) {
        const int fp = f - F_V, q5 = fp / 5, r5 = fp - 5 * q5, slot = f & 7;
        if (fp < 70 && r5 == 4) {
            wait_ge(vctl + C_LANDED, (unsigned)f + 1u);
            const unsigned S = ((const LAS unsigned*)(L + HH_OFF))[14 * g + q5];
            const LAS unsigned char* base = L + slot * SLOT + g * 4096 + w * 256 + c * 16;
            const v4u b0 = *(const LAS v4u*)base, b1 = *(const LAS v4u*)(base + 2048);
            accb = mfma(diagA(S, sel.s[0]), b0, accb);
            accb = mfma(diagA(S, sel.s[1]), b1, accb);
            release_slot(ctl, slot, 1u, lane);
        } else if (slot == w) {
            wait_ge(vctl + C_LANDED, (unsigned)f + 1u);
            const int j = fp < 70 ? 4 * q5 + r5 : fp - 14;
            acco = slot_rows64(myslot, L + SC_OFF + j * 128, sel, c, g, acco);
            release_slot(ctl, w, 8u, lane);
        }
    }
    if (g < 2) *(LAS f32x4*)(scr + w * 256 + 8 * c + 4 * g) = acco;
    cons_barrier(ctl, bphase, lane);
    v4u of[4];
    {
        float s0 = 0.f, s1 = 0.f;
#pragma unroll
        for (int ww = 0; ww < NCW; ++ww) { const f32x2 v = *(const LAS f32x2*)(scr + ww * 256 + 2 * lane); s0 += v[0]; s1 += v[1]; }
        ((LAS unsigned*)(L + O_OFF))[lane] = pack2(s0, s1);
        LGKM0();
        of[0] = *(const LAS v4u*)(L + O_OFF + 32 * g); of[1] = *(const LAS v4u*)(L + O_OFF + 32 * g + 16);
        of[2] = *(const LAS v4u*)(L + O_OFF + 128 + 32 * g); of[3] = *(const LAS v4u*)(L + O_OFF + 128 + 32 * g + 16);
    }
    f32x4 acca = {0.f, 0.f, 0.f, 0.f}, acca1 = {0.f, 0.f, 0.f, 0.f};
#pragma unroll
    for (int t = 0; t < 16; t += 2) {
        const unsigned S = of[t >> 3][(t >> 1) & 3];
        acca = mfma(diagA(S, sel.s[0]), wor[t], acca);
        acca1 = mfma(diagA(S, sel.s[1]), wor[t + 1], acca1);
    }
#pragma unroll
    for (int k = 0; k < 8; ++k) {
        const int f = F_W + k, slot = f & 7;
        wait_ge(vctl + C_LANDED, (unsigned)f + 1u);
        const unsigned S = of[2 + (k >> 2)][k & 3];
        const LAS unsigned char* base = L + slot * SLOT + g * 4096 + w * 256 + c * 16;
        const v4u b0 = *(const LAS v4u*)base, b1 = *(const LAS v4u*)(base + 2048);
        acca = mfma(diagA(S, sel.s[0]), b0, acca);
        acca1 = mfma(diagA(S, sel.s[1]), b1, acca1);
    }
    acca += acca1;
    {
        const float omg = rbf(1.0f - gate);
        if (g < 2) {
#pragma unroll
            for (int i = 0; i < 4; ++i) {
                const float v = gate * acca[i] + omg * accb[i];
                __hip_atomic_store(gran + (size_t)blockIdx.x * DM + 128 * w + 8 * c + 4 * g + i,
                                   ((unsigned long long)tag << 32) | __builtin_bit_cast(unsigned, v), RLX_AGENT);
            }
        }
        const int hp = lane & 31, col = 32 * h + 4 * w + 2 * (lane >> 5);
        gu64* src = gran + ((size_t)b * NH + hp) * DM + col;
        float v0, v1;
        for (unsigned s = 0;; ++s) {
            const unsigned long long x0 = __hip_atomic_load(src, RLX_AGENT), x1 = __hip_atomic_load(src + 1, RLX_AGENT);
            v0 = __builtin_bit_cast(float, (unsigned)x0); v1 = __builtin_bit_cast(float, (unsigned)x1);
            const bool ok = (unsigned)(x0 >> 32) == tag && (unsigned)(x1 >> 32) == tag;
            if (__all(ok) || s > SPIN_CAP) break;
            __builtin_amdgcn_s_sleep(1);
        }
#pragma unroll
        for (int o = 1; o <= 16; o <<= 1) { v0 += __shfl_xor(v0, o); v1 += __shfl_xor(v1, o); }
        if (hp == 0) *(GAS unsigned*)(out + ((size_t)b * DM + col) * 2) = pack2(v0, v1);
        if (h == 0 && w == 0 && lane == 0) __hip_atomic_store(epoch, tag, RLX_AGENT);
    }
}

}

extern "C" void kernel_launch(void* const* d_in, const int* in_sizes, int n_in, void* d_out, int out_size, void* d_ws, size_t ws_size,
                              hipStream_t stream) {
    (void)in_sizes; (void)n_in; (void)out_size; (void)ws_size;
    (void)hipFuncSetAttribute((const void*)fused_decode_kernel, hipFuncAttributeMaxDynamicSharedMemorySize, LDS_BYTES);
    const int pace_kv = 16 * 57, pace_w = 16 * 8;
    fused_decode_kernel<<<dim3(NB * NH), dim3(NTHR), LDS_BYTES, stream>>>(
        (const unsigned char*)d_in[0], (const unsigned char*)d_in[1], (const unsigned char*)d_in[2], (const unsigned char*)d_in[3],
        (const unsigned char*)d_in[4], (const unsigned char*)d_in[5], (const unsigned char*)d_in[6], (const unsigned char*)d_in[7],
        (const unsigned char*)d_in[8], (const unsigned char*)d_in[9], (unsigned char*)d_out, (unsigned char*)d_ws, pace_kv, pace_w);
}
```

```cpp
#include <hip/hip_runtime.h>
#include <hip/hip_bf16.h>

#define GAS __attribute__((address_space(1)))
#define LAS __attribute__((address_space(3)))
typedef unsigned v4u __attribute__((ext_vector_type(4)));
typedef unsigned v2u __attribute__((ext_vector_type(2)));
typedef float f32x4 __attribute__((ext_vector_type(4)));
typedef float f32x2 __attribute__((ext_vector_type(2)));
typedef short bf16x8 __attribute__((ext_vector_type(8)));
typedef GAS unsigned gu32;
typedef GAS unsigned long long gu64;
#define RLX_AGENT __ATOMIC_RELAXED, __HIP_MEMORY_SCOPE_AGENT
#define RLX_WG __ATOMIC_RELAXED, __HIP_MEMORY_SCOPE_WORKGROUP

namespace {

constexpr int NB = 8, DM = 1024, NH = 32, SK = 4096, DH = 128, FF = 3584;
constexpr int FS = FF / NH;
constexpr int NW = 8, NTHR = 64 * NW;
constexpr int SLOT = 16384;
#define GUNT " nt"
#define ROTK 0
#define PRE 1
#define GU_T 2
#define WO_AUX 0
#define WD_AUX 0
#define POLL_SLEEP 1
#define QNT " nt"
#define PACE (16 * 61)
#define TSTART 600
#define DEPTH 4

constexpr int SC_OFF = NW * SLOT;
constexpr int GU_OFF = SC_OFF + NW * 2048;
constexpr int Q_OFF = GU_OFF + 14336;
constexpr int O_OFF = Q_OFF + 256;
constexpr int HH_OFF = O_OFF + 256;
constexpr int CTL_OFF = HH_OFF + 256;
constexpr int LDS_BYTES = CTL_OFF + 256;
constexpr int C_ISS = 0, C_LAND = 1, C_FFN = 2, C_O = 3, C_QP = 4, C_QR = 5, C_GU = 6, C_OUT = 7, C_TAG = 8, C_POLL = 10, C_WMAX = 16, C_WSUM = 24, C_GP = 32;
static_assert(LDS_BYTES <= 160 * 1024, "LDS");

constexpr size_t WS_EPOCH = 0, WS_GRAN = 4096, WS_QGRAN = WS_GRAN + (size_t)NB * NH * DM * 8, WS_GUGRAN = WS_QGRAN + (size_t)NH * NB * NB * DH * 8;

constexpr unsigned SPIN_CAP = 1u << 22;

__device__ __forceinline__ f32x4 mfma(v4u a, v4u b, f32x4 c) {
    return __builtin_amdgcn_mfma_f32_16x16x32_bf16(__builtin_bit_cast(bf16x8, a), __builtin_bit_cast(bf16x8, b), c, 0, 0, 0);
}
__device__ __forceinline__ float bf_lo(unsigned u) { return __builtin_bit_cast(float, u << 16); }
__device__ __forceinline__ float bf_hi(unsigned u) { return __builtin_bit_cast(float, u & 0xffff0000u); }
__device__ __forceinline__ float rbf(float f) { return (float)(__bf16)f; }
__device__ __forceinline__ unsigned bf_bits(float f) { return (unsigned)__builtin_bit_cast(unsigned short, (__bf16)f); }
__device__ __forceinline__ unsigned pack2(float lo, float hi) { return bf_bits(lo) | (bf_bits(hi) << 16); }
__device__ __forceinline__ unsigned gran(float f, unsigned tag_lo) { return ((__builtin_bit_cast(unsigned, f) + 0x80u) & 0xffffff00u) | tag_lo; }
__device__ __forceinline__ void store_dev(v4u d, __amdgpu_buffer_rsrc_t r, int vo, int so) {
    __builtin_amdgcn_raw_buffer_store_b128(d, r, vo, so, 16);
    asm volatile("s_nop 1" ::"v"(d) : "memory");
}
__device__ __forceinline__ unsigned long long rt() { return __builtin_amdgcn_s_memrealtime(); }
__device__ __forceinline__ unsigned rfl(unsigned v) { return (unsigned)__builtin_amdgcn_readfirstlane((int)v); }
#define LGKM0() asm volatile("s_waitcnt lgkmcnt(0)" ::: "memory")
#define VMCNT(n) asm volatile("s_waitcnt vmcnt(%0)" ::"i"(n) : "memory")
#define CBAR() asm volatile("" ::: "memory")
#define PIN2(a, b) do { asm volatile("" : "+v"(a), "+v"(b)); __builtin_amdgcn_sched_barrier(0); } while (0)

__device__ __forceinline__ void glds4(const void* sbase, unsigned v0, unsigned v1, unsigned v2, unsigned v3, unsigned lds) {
    unsigned keep;
    asm volatile("s_mov_b32 %0, m0\n\ts_mov_b32 m0, %6\n\ts_nop 0\n\t"
                 "global_load_lds_dwordx4 %1, %5 nt\n\t"
                 "global_load_lds_dwordx4 %2, %5 offset:1024 nt\n\t"
                 "global_load_lds_dwordx4 %3, %5 offset:2048 nt\n\t"
                 "global_load_lds_dwordx4 %4, %5 offset:3072 nt\n\t"
                 "s_mov_b32 m0, %0"
                 : "=&s"(keep) : "v"(v0), "v"(v1), "v"(v2), "v"(v3), "s"(sbase), "s"(lds) : "memory");
}
__device__ __forceinline__ void fill(const unsigned char* src, unsigned v0, unsigned v1, unsigned v2, unsigned v3, unsigned lds) {
    LGKM0();
#pragma unroll
    for (int q = 0; q < 4; ++q) glds4(src + q * 4096, v0, v1, v2, v3, lds + q * 4096);
}

__device__ __forceinline__ void wait_ge(LAS unsigned* w, unsigned need) {
    for (unsigned s = 0; rfl(*(volatile LAS unsigned*)w) < need && s < SPIN_CAP; ++s) __builtin_amdgcn_s_sleep(0);
    CBAR();
}
__device__ __forceinline__ void arrive(LAS unsigned* w, int lane) {
    LGKM0();
    if (lane == 0) __hip_atomic_fetch_add(w, 1u, RLX_WG);
    CBAR();
}

struct Sel { unsigned s[2][4]; };
__device__ __forceinline__ v4u diagA(unsigned S, const unsigned (&sel)[4]) {
    return v4u{__builtin_amdgcn_perm(0u, S, sel[0]), __builtin_amdgcn_perm(0u, S, sel[1]), __builtin_amdgcn_perm(0u, S, sel[2]), __builtin_amdgcn_perm(0u, S, sel[3])};
}
__device__ __forceinline__ int fresh_lane() {
    int l;
    asm volatile("v_mbcnt_lo_u32_b32 %0, -1, 0\n\tv_mbcnt_hi_u32_b32 %0, -1, %0" : "=v"(l));
    return l;
}
__device__ __forceinline__ Sel make_sel(int c) {
    Sel sel;
    const int dm = (c & 7) >> 1, hf = c & 1;
#pragma unroll
    for (int par = 0; par < 2; ++par)
#pragma unroll
        for (int d = 0; d < 4; ++d) {
            const unsigned lohi = (unsigned)(2 * par) | ((unsigned)(2 * par + 1) << 8);
            const unsigned v = hf ? (0x0c0cu | (lohi << 16)) : (0x0c0c0000u | lohi);
            sel.s[par][d] = (c < 8 && d == dm) ? v : 0x0c0c0c0cu;
        }
    return sel;
}

__device__ __forceinline__ void slot_rows64(const LAS unsigned char* slot, const LAS unsigned char* vec, const Sel& sel, int c, int g,
                                            f32x4& acc, f32x4& a1) {
    const LAS unsigned char* base = slot + g * 4096 + c * 16;
#pragma unroll
    for (int hf = 0; hf < 2; ++hf) {
        const v4u s = *(const LAS v4u*)(vec + g * 32 + hf * 16);
        v4u bb[8];
#pragma unroll
        for (int t = 0; t < 8; ++t) bb[t] = *(const LAS v4u*)(base + (8 * hf + t) * 256);
        __builtin_amdgcn_sched_barrier(0);
#pragma unroll
        for (int u = 0; u < 4; ++u) {
            acc = mfma(diagA(s[u], sel.s[0]), bb[2 * u], acc);
            a1 = mfma(diagA(s[u], sel.s[1]), bb[2 * u + 1], a1);
            PIN2(acc, a1);
        }
    }
}

__device__ __forceinline__ float gelu_tanh(float x) {
    const float y = 0.7978845608028654f * (x + 0.044715f * x * x * x);
    return rbf(x / (1.0f + __expf(-2.0f * y)));
}
__device__ __forceinline__ float wave_max(float v) {
#pragma unroll
    for (int o = 32; o >= 1; o >>= 1) v = fmaxf(v, __shfl_xor(v, o));
    return v;
}
__device__ __forceinline__ float wave_sum(float v) {
#pragma unroll
    for (int o = 32; o >= 1; o >>= 1) v += __shfl_xor(v, o);
    return v;
}

__global__ void __launch_bounds__(NTHR)
fused_decode_kernel(const unsigned char* __restrict__ xin, const unsigned char* __restrict__ kc, const unsigned char* __restrict__ vc,
                    const unsigned char* __restrict__ wq, const unsigned char* __restrict__ wo,
                    const unsigned char* __restrict__ wg, const unsigned char* __restrict__ wu, const unsigned char* __restrict__ wd,
                    const unsigned char* __restrict__ gatew, unsigned char* __restrict__ out, unsigned char* __restrict__ ws,
                    int pace, int tstart, int depth) {
    extern __shared__ __attribute__((aligned(16))) unsigned char smem[];
    LAS unsigned char* L = (LAS unsigned char*)smem;
    LAS unsigned* ctl = (LAS unsigned*)(L + CTL_OFF);
    const unsigned long long t0 = rt();
    const int w = __builtin_amdgcn_readfirstlane((int)threadIdx.x >> 6);
    const int lane = threadIdx.x & 63, c = lane & 15, g = lane >> 4;
    const int h = (blockIdx.x & 7) * 4 + ((blockIdx.x >> 3) & 3), b = blockIdx.x >> 5;
    const int fs = h, bh = b * NH + h;
    const unsigned myslot_a = (unsigned)(size_t)L + w * SLOT;
    const LAS unsigned char* myslot = L + w * SLOT;
    const unsigned char* kb = kc + (size_t)bh * (SK * DH * 2);
    const unsigned char* vb = vc + (size_t)bh * (SK * DH * 2);
    const unsigned rot = (unsigned)(ROTK) + (w ^ 4);
    auto toff = [&](int t) { return (size_t)(((8u * t + rot) & 63u) * SLOT); };
    const unsigned vk0 = g * 256 + ((c ^ g) << 4), vk1 = g * 256 + ((c ^ (4 + g)) << 4), vk2 = g * 256 + ((c ^ (8 + g)) << 4),
                   vk3 = g * 256 + ((c ^ (12 + g)) << 4);
    const int u = w ^ 4;
    auto take_turn = [&](int f) {
        const unsigned long long td = t0 + (unsigned)(tstart + (((f - 8) * pace) >> 4));
        while (rt() < td) __builtin_amdgcn_s_sleep(1);
        for (unsigned s = 0; s < SPIN_CAP; ++s) {
            const v2u st = *(volatile LAS v2u*)(ctl + C_ISS);
            if (rfl(st[0]) >= (unsigned)f && rfl(st[1]) + (unsigned)depth > (unsigned)f) break;
            __builtin_amdgcn_s_sleep(0);
        }
        CBAR();
    };
    auto issued = [&](int f) { if (lane == 0) *(volatile LAS unsigned*)(ctl + C_ISS) = (unsigned)f + 1u; CBAR(); };
    auto landed = [&]() { if (lane == 0) __hip_atomic_fetch_add(ctl + C_LAND, 1u, RLX_WG); CBAR(); };
    const Sel sel = make_sel(c & 7);
    LAS float* sc = (LAS float*)(L + SC_OFF + w * 2048);
    const __amdgpu_buffer_rsrc_t rgran = __builtin_amdgcn_make_buffer_rsrc((void*)(ws + WS_GRAN), 0, NB * NH * DM * 8, 0x00020000);
    const int gvo = (8 * c + 4 * (g & 1) + 2 * (g >> 1)) * 8, gso = bh * (DM * 8) + w * 1024;
    unsigned long long epoch;

    if (__builtin_expect(w < 4, 1)) {
        v4u x8[4], wr[8];
        v2u ep;
        const unsigned char* xs = xin + (128 * b + 32 * w) * 2;
        const unsigned vx = (c >> 3) * (DM * 2) + g * 16;
        const unsigned char* qs = wq + (size_t)(128 * b + 32 * w) * (NH * DH * 2) + h * 256;
        const unsigned vq = g * 8 * (NH * DH * 2) + c * 16;
#pragma unroll
        for (int t = 0; t < 8; ++t)
            asm volatile("global_load_dwordx4 %0, %1, %2" QNT : "=v"(wr[t]) : "v"(vq), "s"(qs + (size_t)t * (NH * DH * 2)) : "memory");
#pragma unroll
        for (int j = 0; j < 4; ++j)
            asm volatile("global_load_dwordx4 %0, %1, %2" : "=v"(x8[j]) : "v"(vx), "s"(xs + j * (2 * DM * 2)) : "memory");
        asm volatile("global_load_dwordx2 %0, %1, %2 sc1" : "=v"(ep) : "v"(0u), "s"(ws + WS_EPOCH) : "memory");
        const unsigned char* inbox = ws + WS_QGRAN + (size_t)(h * NB + b) * NB * (DH * 8);
        unsigned warm;
        asm volatile("global_load_dword %0, %1, %2 sc1" : "=v"(warm) : "v"(lane * 16), "s"(inbox) : "memory");
        __builtin_amdgcn_s_barrier();
        VMCNT(0);
        asm volatile("" : "+v"(x8[0]), "+v"(x8[1]), "+v"(x8[2]), "+v"(x8[3]), "+v"(wr[0]), "+v"(wr[1]), "+v"(wr[2]), "+v"(wr[3]),
                     "+v"(wr[4]), "+v"(wr[5]), "+v"(wr[6]), "+v"(wr[7]), "+v"(warm), "+v"(ep) :: "memory");
        {
            const unsigned e0 = ep[0], e1 = ep[1];
            epoch = (((unsigned long long)rfl(e1) << 32) | rfl(e0)) + 1ull;
        }
        const unsigned qtag_hi = (unsigned)(epoch >> 8), qtag_lo = (unsigned)epoch & 0xffu;
#pragma unroll
        for (int j = 0; j < 4; ++j) {
            f32x4 a0 = {0.f, 0.f, 0.f, 0.f}, a1 = {0.f, 0.f, 0.f, 0.f};
#pragma unroll
            for (int k = 0; k < 4; ++k) {
                a0 = mfma(diagA(x8[j][k], sel.s[0]), wr[2 * k], a0);
                a1 = mfma(diagA(x8[j][k], sel.s[1]), wr[2 * k + 1], a1);
            }
            a0 += a1;
            *(LAS f32x4*)(L + SC_OFF + ((w * 8 + 2 * j + (g >> 1)) * 128 + 8 * c + 4 * (g & 1)) * 4) = a0;
        }
        if (w == 0 && lane == 0) *(LAS v2u*)(ctl + C_TAG) = v2u{(unsigned)epoch, (unsigned)(epoch >> 32)};
        arrive(ctl + C_QP, lane);
        wait_ge(ctl + C_QP, 4);
        {
            const __amdgpu_buffer_rsrc_t rqg = __builtin_amdgcn_make_buffer_rsrc((void*)(ws + WS_QGRAN), 0, NH * NB * NB * DH * 8, 0x00020000);
#pragma unroll
            for (int r = 0; r < 2; ++r) {
                float s0 = 0.f, s1 = 0.f;
#pragma unroll
                for (int k = 0; k < 4; ++k) { const f32x2 v = *(const LAS f32x2*)(L + SC_OFF + ((k * 8 + 2 * w + r) * 128 + 2 * lane) * 4); s0 += v[0]; s1 += v[1]; }
                store_dev(v4u{gran(s0, qtag_lo), qtag_hi, gran(s1, qtag_lo), qtag_hi}, rqg, lane * 16, ((h * NB + 2 * w + r) * NB + b) * (DH * 8));
            }
        }
        arrive(ctl + C_QP, lane);
        if (w == 3) {
            VMCNT(0);
            const unsigned vo = lane * 16;
            v4u y[NB];
            for (unsigned s = 0;; ++s) {
#pragma unroll
                for (int p = 0; p < NB; ++p)
                    asm volatile("global_load_dwordx4 %0, %1, %2 offset:%3 sc1" : "=v"(y[p]) : "v"(vo), "s"(inbox + (p >> 2) * 4096), "i"((p & 3) * 1024) : "memory");
                if (s == 0) {
                    if (lane == 0) *(volatile LAS unsigned*)(ctl + C_POLL) = 1u;
                    fill(kb + toff(0), vk0, vk1, vk2, vk3, myslot_a);
                    VMCNT(16);
                } else {
                    VMCNT(0);
                }
                asm volatile("" : "+v"(y[0]), "+v"(y[1]), "+v"(y[2]), "+v"(y[3]), "+v"(y[4]), "+v"(y[5]), "+v"(y[6]), "+v"(y[7]) :: "memory");
                unsigned bad = 0u;
#pragma unroll
                for (int p = 0; p < NB; ++p) bad |= (y[p][1] ^ qtag_hi) | (y[p][3] ^ qtag_hi) | (((y[p][0] ^ qtag_lo) | (y[p][2] ^ qtag_lo)) & 0xffu);
                if (__all(bad == 0u) || s > SPIN_CAP) break;
                __builtin_amdgcn_s_sleep(POLL_SLEEP);
            }
            float s0 = 0.f, s1 = 0.f;
#pragma unroll
            for (int p = 0; p < NB; ++p) { s0 += __builtin_bit_cast(float, y[p][0] & 0xffffff00u); s1 += __builtin_bit_cast(float, y[p][2] & 0xffffff00u); }
            ((LAS unsigned*)(L + Q_OFF))[lane] = pack2(s0, s1);
            wait_ge(ctl + C_QP, 8);
            arrive(ctl + C_QR, lane);
        } else {
            if (w >= PRE) wait_ge(ctl + C_POLL, 1);
            fill(kb + toff(0), vk0, vk1, vk2, vk3, myslot_a);
        }
        VMCNT(0);
        landed();
    } else {
        if (w == 4 && lane < 16) ctl[lane] = lane == C_ISS ? 8u : 0u;
        LGKM0();
        __builtin_amdgcn_s_barrier();
        fill(kb + toff(0), vk0, vk1, vk2, vk3, myslot_a);
        VMCNT(0);
        landed();
        wait_ge(ctl + C_QP, 4);
        const v2u e = *(volatile LAS v2u*)(ctl + C_TAG);
        const unsigned e0 = e[0], e1 = e[1];
        epoch = ((unsigned long long)rfl(e1) << 32) | rfl(e0);
    }
    const unsigned tag_hi = (unsigned)(epoch >> 8), tag_lo = (unsigned)epoch & 0xffu;
    wait_ge(ctl + C_QR, 1);
    v4u qf[4];
#pragma unroll
    for (int s = 0; s < 4; ++s) qf[s] = *(const LAS v4u*)(L + Q_OFF + (32 * s + 8 * g) * 2);
    LGKM0();
    float mx = -INFINITY;
    v4u wor[32];
    const __amdgpu_buffer_rsrc_t ro = __builtin_amdgcn_make_buffer_rsrc((void*)wo, 0, DM * NH * DH * 2, 0x00020000);
    const int soo = h * DH * (DM * 2) + w * 256;
#pragma unroll
    for (int t = 0; t < 8; ++t) {
        if (t > 0) { take_turn(8 * t + u); fill(kb + toff(t), vk0, vk1, vk2, vk3, myslot_a); issued(8 * t + u); }
        v4u xg[4][2], gw[16];
        if (t > 0) {
            const int l2 = fresh_lane();
            const int voo = 32 * (l2 >> 4) * (DM * 2) + (l2 & 15) * 16;
            v4u* wp = wor + 4 * (t - 1);
#pragma unroll
            for (int r = 0; r < 4; ++r) wp[r] = __builtin_amdgcn_raw_buffer_load_b128(ro, voo, soo + (4 * (t - 1) + r) * (DM * 2), WO_AUX);
            if (t == 1) {
                const __amdgpu_buffer_rsrc_t rx = __builtin_amdgcn_make_buffer_rsrc((void*)xin, 0, NB * DM * 2, 0x00020000);
                const __amdgpu_buffer_rsrc_t rw = __builtin_amdgcn_make_buffer_rsrc((void*)gatew, 0, DM * 2, 0x00020000);
                v4u xv = __builtin_amdgcn_raw_buffer_load_b128(rx, (l2 & 15) * 16, b * (DM * 2) + w * 256, 0);
                v4u gv = __builtin_amdgcn_raw_buffer_load_b128(rw, (l2 & 15) * 16, w * 256, 0);
                asm volatile("s_waitcnt vmcnt(0)" : "+v"(wp[0]), "+v"(wp[1]), "+v"(wp[2]), "+v"(wp[3]), "+v"(xv), "+v"(gv) :: "memory");
                landed();
                store_dev(v4u{tag_lo ^ 1u, ~tag_hi, tag_lo ^ 1u, ~tag_hi}, rgran, gvo, gso);
                float s = 0.f;
#pragma unroll
                for (int k = 0; k < 4; ++k) s += rbf(bf_lo(xv[k]) * bf_lo(gv[k])) + rbf(bf_hi(xv[k]) * bf_hi(gv[k]));
                s = wave_sum(l2 < 16 ? s : 0.f);
                if (l2 == 0) ((LAS float*)ctl)[C_GP + w] = s;
            } else if (t == GU_T && w >= 4) {
                const int m = (w >> 1) & 1, hf = w & 1;
                const unsigned char* gs = (m ? wu : wg) + (size_t)(128 * b + 64 * hf) * (FF * 2) + fs * (FS * 2);
                const unsigned vog = 16 * (l2 >> 4) * (FF * 2) + ((l2 & 15) < 14 ? (l2 & 15) : 13) * 16;
#pragma unroll
                for (int k = 0; k < 16; ++k)
                    asm volatile("global_load_dwordx4 %0, %1, %2" GUNT : "=v"(gw[k]) : "v"(vog), "s"(gs + (size_t)k * (FF * 2)) : "memory");
                const unsigned char* xs = xin + (128 * b + 64 * hf) * 2;
                const unsigned vox = ((l2 & 15) >> 3) * (DM * 2) + (l2 >> 4) * 32;
#pragma unroll
                for (int j = 0; j < 4; ++j) {
                    asm volatile("global_load_dwordx4 %0, %1, %2" : "=v"(xg[j][0]) : "v"(vox), "s"(xs + j * (2 * DM * 2)) : "memory");
                    asm volatile("global_load_dwordx4 %0, %1, %2 offset:16" : "=v"(xg[j][1]) : "v"(vox), "s"(xs + j * (2 * DM * 2)) : "memory");
                }
                VMCNT(24);
            } else if (t == GU_T + 1 && w >= 4) {
                asm volatile("s_waitcnt vmcnt(0)" : "+v"(wp[0]), "+v"(wp[1]), "+v"(wp[2]), "+v"(wp[3]) :: "memory");
                landed();
                wait_ge(ctl + C_GU, 4);
                const __amdgpu_buffer_rsrc_t rgg = __builtin_amdgcn_make_buffer_rsrc((void*)(ws + WS_GUGRAN), 0, NH * NB * NB * 2048, 0x00020000);
#pragma unroll
                for (int r = 0; r < 2; ++r)
#pragma unroll
                    for (int q = 0; q < 2; ++q) {
                        const int p = q == 0 ? l2 : (l2 < 48 ? 64 + l2 : 111);
                        const int mm = p >= 56 ? 1 : 0, col = 2 * (p - 56 * mm), cons = 2 * (w - 4) + r;
                        const f32x2 v0 = *(const LAS f32x2*)(L + GU_OFF + (((2 * mm) * 8 + cons) * FS + col) * 4);
                        const f32x2 v1 = *(const LAS f32x2*)(L + GU_OFF + (((2 * mm + 1) * 8 + cons) * FS + col) * 4);
                        const float s0 = v0[0] + v1[0], s1 = v0[1] + v1[1];
                        if (q == 0 || l2 < 48)
                            store_dev(v4u{gran(s0, tag_lo), tag_hi, gran(s1, tag_lo), tag_hi}, rgg, p * 16, ((h * NB + cons) * NB + b) * 2048);
                    }
                arrive(ctl + C_GU, l2);
            } else if (t == 7) {
                const __amdgpu_buffer_rsrc_t rgg = __builtin_amdgcn_make_buffer_rsrc((void*)(ws + WS_GUGRAN), 0, NH * NB * NB * 2048, 0x00020000);
                const int sgg = ((h * NB + b) * NB + w) * 2048, v1o = 1024 + (l2 < 48 ? l2 : 47) * 16;
                v4u y0, y1;
                for (unsigned s = 0;; ++s) {
                    y0 = __builtin_amdgcn_raw_buffer_load_b128(rgg, l2 * 16, sgg, 16);
                    y1 = __builtin_amdgcn_raw_buffer_load_b128(rgg, v1o, sgg, 16);
                    asm volatile("s_waitcnt vmcnt(0)" : "+v"(wp[0]), "+v"(wp[1]), "+v"(wp[2]), "+v"(wp[3]), "+v"(y0), "+v"(y1) :: "memory");
                    const unsigned bad = (y0[1] ^ tag_hi) | (y0[3] ^ tag_hi) | (y1[1] ^ tag_hi) | (y1[3] ^ tag_hi) | (((y0[0] ^ tag_lo) | (y0[2] ^ tag_lo) | (y1[0] ^ tag_lo) | (y1[2] ^ tag_lo)) & 0xffu);
                    if (s == 0) landed();
                    if (__all(bad == 0u) || s > SPIN_CAP) break;
                    __builtin_amdgcn_s_sleep(1);
                }
                wait_ge(ctl + C_GU, 8);
                *(LAS v2u*)(L + GU_OFF + w * 896 + l2 * 8) = v2u{y0[0] & 0xffffff00u, y0[2] & 0xffffff00u};
                if (l2 < 48) *(LAS v2u*)(L + GU_OFF + w * 896 + 512 + l2 * 8) = v2u{y1[0] & 0xffffff00u, y1[2] & 0xffffff00u};
                arrive(ctl + C_FFN, l2);
            } else {
                asm volatile("s_waitcnt vmcnt(0)" : "+v"(wp[0]), "+v"(wp[1]), "+v"(wp[2]), "+v"(wp[3]) :: "memory");
            }
            if (t != 7 && t != 1 && !(t == GU_T + 1 && w >= 4)) landed();
        }
        f32x4 s4[4];
#pragma unroll
        for (int kg = 0; kg < 4; ++kg) {
            f32x4 a = {0.f, 0.f, 0.f, 0.f};
#pragma unroll
            for (int s = 0; s < 4; ++s) {
                const v4u kf = *(const LAS v4u*)(myslot + (16 * kg + c) * 256 + (((4 * s + g) ^ c) << 4));
                a = mfma(kf, qf[s], a);
            }
            s4[kg] = a;
        }
        f32x4 v;
#pragma unroll
        for (int i = 0; i < 4; ++i) {
            const float lo = (c & 1) ? s4[1][i] : s4[0][i], hi = (c & 1) ? s4[3][i] : s4[2][i];
            v[i] = rbf(rbf((c & 2) ? hi : lo) * 0.08838834764831845f);
            mx = fmaxf(mx, v[i]);
        }
        if (c < 4) *(LAS f32x4*)(sc + t * 64 + 16 * c + 4 * g) = v;
        __builtin_amdgcn_sched_barrier(0);
        if (t == GU_T && w >= 4) {
            v4u* wp = wor + 4 * (t - 1);
            asm volatile("s_waitcnt vmcnt(0)" : "+v"(gw[0]), "+v"(gw[1]), "+v"(gw[2]), "+v"(gw[3]), "+v"(gw[4]), "+v"(gw[5]), "+v"(gw[6]), "+v"(gw[7]),
                         "+v"(gw[8]), "+v"(gw[9]), "+v"(gw[10]), "+v"(gw[11]), "+v"(gw[12]), "+v"(gw[13]), "+v"(gw[14]), "+v"(gw[15]),
                         "+v"(xg[0][0]), "+v"(xg[0][1]), "+v"(xg[1][0]), "+v"(xg[1][1]), "+v"(xg[2][0]), "+v"(xg[2][1]), "+v"(xg[3][0]), "+v"(xg[3][1]) :: "memory");
            asm volatile("" : "+v"(wp[0]), "+v"(wp[1]), "+v"(wp[2]), "+v"(wp[3]));
            f32x4 a[4];
#pragma unroll
            for (int j = 0; j < 4; ++j) a[j] = f32x4{0.f, 0.f, 0.f, 0.f};
#pragma unroll
            for (int k = 0; k < 16; ++k) {
#pragma unroll
                for (int j = 0; j < 4; ++j) a[j] = mfma(diagA(xg[j][k >> 3][(k >> 1) & 3], sel.s[k & 1]), gw[k], a[j]);
                PIN2(a[0], a[1]); PIN2(a[2], a[3]);
            }
            if (c < 14) {
#pragma unroll
                for (int j = 0; j < 4; ++j)
                    *(LAS f32x4*)(L + GU_OFF + ((((w - 4) * 8 + 2 * j + (g >> 1)) * FS) + 8 * c + 4 * (g & 1)) * 4) = a[j];
            }
            arrive(ctl + C_GU, lane);
        }
    }
    float lsum;
    mx = wave_max(mx);
    {
        const f32x4 sa = *(const LAS f32x4*)(sc + 8 * lane), sb = *(const LAS f32x4*)(sc + 8 * lane + 4);
        float e[8];
#pragma unroll
        for (int i = 0; i < 4; ++i) { e[i] = __expf(sa[i] - mx); e[4 + i] = __expf(sb[i] - mx); }
        lsum = wave_sum(((e[0] + e[1]) + (e[2] + e[3])) + ((e[4] + e[5]) + (e[6] + e[7])));
        v4u pk;
#pragma unroll
        for (int i = 0; i < 4; ++i) pk[i] = pack2(e[2 * i], e[2 * i + 1]);
        LGKM0();
        *(LAS v4u*)((LAS unsigned char*)sc + 16 * lane) = pk;
    }
    f32x4 acco = {0.f, 0.f, 0.f, 0.f}, acco1 = {0.f, 0.f, 0.f, 0.f}, accb = {0.f, 0.f, 0.f, 0.f};
    {
        const unsigned vlin = lane * 16;
        {
            take_turn(64 + u);
            fill(vb + toff(0), vlin, vlin, vlin, vlin, myslot_a);
            issued(64 + u);
            const int voo = 32 * g * (DM * 2) + c * 16;
#pragma unroll
            for (int r = 0; r < 4; ++r) wor[28 + r] = __builtin_amdgcn_raw_buffer_load_b128(ro, voo, soo + (28 + r) * (DM * 2), WO_AUX);
            asm volatile("s_waitcnt vmcnt(0)" : "+v"(wor[28]), "+v"(wor[29]), "+v"(wor[30]), "+v"(wor[31]) :: "memory");
            landed();
            slot_rows64(myslot, (const LAS unsigned char*)sc, sel, c, g, acco, acco1);
        }
        const __amdgpu_buffer_rsrc_t rd = __builtin_amdgcn_make_buffer_rsrc((void*)wd, 0, FF * DM * 2, 0x00020000);
        const int vod = 28 * g * (DM * 2) + c * 16;
#pragma unroll 1
        for (int t = 1; t < 8; ++t) {
            take_turn(64 + 8 * t + u);
            fill(vb + toff(t), vlin, vlin, vlin, vlin, myslot_a);
            issued(64 + 8 * t + u);
            const int tt = t - 1;
            const int sod = (fs * FS + 4 * tt) * (DM * 2) + w * 256;
            v4u dw[4];
#pragma unroll
            for (int r = 0; r < 4; ++r) dw[r] = __builtin_amdgcn_raw_buffer_load_b128(rd, vod, sod + r * (DM * 2), WD_AUX);
            if (t == 1) {
                wait_ge(ctl + C_FFN, NW);
                const int l2 = fresh_lane();
                if (l2 < FS / 2) {
                    float g0 = 0.f, g1 = 0.f, u0 = 0.f, u1 = 0.f;
#pragma unroll
                    for (int ww = 0; ww < NW; ++ww) {
                        const f32x2 a = *(const LAS f32x2*)(L + GU_OFF + ww * 896 + 8 * l2), bb = *(const LAS f32x2*)(L + GU_OFF + ww * 896 + 448 + 8 * l2);
                        g0 += a[0]; g1 += a[1]; u0 += bb[0]; u1 += bb[1];
                    }
                    ((LAS unsigned*)(L + HH_OFF))[l2] = pack2(gelu_tanh(rbf(g0)) * rbf(u0), gelu_tanh(rbf(g1)) * rbf(u1));
                }
                LGKM0();
            }
            const v2u hs = *(const LAS v2u*)(L + HH_OFF + (28 * g + 4 * tt) * 2);
            asm volatile("s_waitcnt vmcnt(0)" : "+v"(dw[0]), "+v"(dw[1]), "+v"(dw[2]), "+v"(dw[3]) :: "memory");
            landed();
            slot_rows64(myslot, (const LAS unsigned char*)sc + t * 128, sel, c, g, acco, acco1);
#pragma unroll
            for (int r = 0; r < 4; ++r) accb = mfma(diagA(hs[r >> 1], sel.s[r & 1]), dw[r], accb);
        }
    }
    acco += acco1;
    if (g < 2) *(LAS f32x4*)((LAS unsigned char*)sc + 1024 + (8 * c + 4 * g) * 4) = acco;
    if (lane == 0) { ((LAS float*)ctl)[C_WMAX + w] = mx; ((LAS float*)ctl)[C_WSUM + w] = lsum; }
    arrive(ctl + C_O, lane);
    wait_ge(ctl + C_O, NW);
    v4u of[4];
    {
        float m[NW], M = -INFINITY;
#pragma unroll
        for (int ww = 0; ww < NW; ++ww) { m[ww] = ((LAS float*)ctl)[C_WMAX + ww]; M = fmaxf(M, m[ww]); }
        float Ls = 0.f, s0 = 0.f, s1 = 0.f;
#pragma unroll
        for (int ww = 0; ww < NW; ++ww) {
            const float f = __expf(m[ww] - M);
            Ls += f * ((LAS float*)ctl)[C_WSUM + ww];
            const f32x2 v = *(const LAS f32x2*)(L + SC_OFF + ww * 2048 + 1024 + 8 * lane);
            s0 += f * v[0]; s1 += f * v[1];
        }
        const float inv = 1.0f / Ls;
        ((LAS unsigned*)(L + O_OFF))[lane] = pack2(s0 * inv, s1 * inv);
        LGKM0();
#pragma unroll
        for (int k = 0; k < 4; ++k) of[k] = *(const LAS v4u*)(L + O_OFF + 64 * g + 16 * k);
    }
    f32x4 acca = {0.f, 0.f, 0.f, 0.f}, acca1 = {0.f, 0.f, 0.f, 0.f};
#pragma unroll
    for (int t = 0; t < 32; t += 2) {
        const unsigned S = of[t >> 3][(t >> 1) & 3];
        acca = mfma(diagA(S, sel.s[0]), wor[t], acca);
        acca1 = mfma(diagA(S, sel.s[1]), wor[t + 1], acca1);
    }
    acca += acca1;
    {
        float z = 0.f;
#pragma unroll
        for (int ww = 0; ww < NW; ++ww) z += ((LAS float*)ctl)[C_GP + ww];
        z = rbf(z);
        const float gate = rbf(1.0f / (1.0f + __expf(-z)));
        const float omg = rbf(1.0f - gate);
        {
            const float r0 = gate * (g < 2 ? acca[0] : acca[2]) + omg * (g < 2 ? accb[0] : accb[2]);
            const float r1 = gate * (g < 2 ? acca[1] : acca[3]) + omg * (g < 2 ? accb[1] : accb[3]);
            store_dev(v4u{gran(r0, tag_lo), tag_hi, gran(r1, tag_lo), tag_hi}, rgran, gvo, gso);
        }
        const int l2 = fresh_lane();
        const int vo = ((l2 >> 4) * DM + 2 * (l2 & 15)) * 8, so = ((b * NH + 4 * w) * DM + 32 * h) * 8;
        v4u y;
        for (unsigned s = 0;; ++s) {
            y = __builtin_amdgcn_raw_buffer_load_b128(rgran, vo, so, 16);
            const unsigned bad = (y[1] ^ tag_hi) | (y[3] ^ tag_hi) | (((y[0] ^ tag_lo) | (y[2] ^ tag_lo)) & 0xffu);
            if (__all(bad == 0u) || s > SPIN_CAP) break;
            __builtin_amdgcn_s_sleep(1);
        }
        float v0 = __builtin_bit_cast(float, y[0] & 0xffffff00u), v1 = __builtin_bit_cast(float, y[2] & 0xffffff00u);
        v0 += __shfl_xor(v0, 16); v1 += __shfl_xor(v1, 16);
        v0 += __shfl_xor(v0, 32); v1 += __shfl_xor(v1, 32);
        if (l2 < 16) *(LAS f32x2*)(L + GU_OFF + w * 128 + l2 * 8) = f32x2{v0, v1};
        LGKM0();
        unsigned old = 0u;
        if (l2 == 0) old = __hip_atomic_fetch_add(ctl + C_OUT, 1u, RLX_WG);
        if (rfl(old) == NW - 1) {
            CBAR();
            if (l2 < 16) {
                float s0 = 0.f, s1 = 0.f;
#pragma unroll
                for (int ww = 0; ww < NW; ++ww) { const f32x2 v = *(const LAS f32x2*)(L + GU_OFF + ww * 128 + l2 * 8); s0 += v[0]; s1 += v[1]; }
                *(GAS unsigned*)(out + ((size_t)b * DM + 32 * h + 2 * l2) * 2) = pack2(s0, s1);
            }
            if (bh == 0 && l2 == 0) __hip_atomic_store((gu64*)(ws + WS_EPOCH), epoch, RLX_AGENT);
        }
    }
}

}

extern "C" void kernel_launch(void* const* d_in, const int* in_sizes, int n_in, void* d_out, int out_size, void* d_ws, size_t ws_size,
                              hipStream_t stream) {
    (void)in_sizes; (void)n_in; (void)out_size; (void)ws_size;
    (void)hipFuncSetAttribute((const void*)fused_decode_kernel, hipFuncAttributeMaxDynamicSharedMemorySize, LDS_BYTES);
    int pace = PACE, tstart = TSTART, depth = DEPTH;
    fused_decode_kernel<<<dim3(NB * NH), dim3(NTHR), LDS_BYTES, stream>>>(
        (const unsigned char*)d_in[0], (const unsigned char*)d_in[1], (const unsigned char*)d_in[2], (const unsigned char*)d_in[3],
        (const unsigned char*)d_in[4], (const unsigned char*)d_in[6], (const unsigned char*)d_in[7],
        (const unsigned char*)d_in[8], (const unsigned char*)d_in[9], (unsigned char*)d_out, (unsigned char*)d_ws, pace, tstart, depth);
}
```

```cpp
#define MS_ID 42
#define MS_MUL 10u
#include <hip/hip_runtime.h>
#include <hip/hip_bf16.h>

#define GAS __attribute__((address_space(1)))
#define LAS __attribute__((address_space(3)))
typedef unsigned v4u __attribute__((ext_vector_type(4)));
typedef unsigned v2u __attribute__((ext_vector_type(2)));
typedef float f32x4 __attribute__((ext_vector_type(4)));
typedef float f32x2 __attribute__((ext_vector_type(2)));
typedef short bf16x8 __attribute__((ext_vector_type(8)));
typedef GAS unsigned gu32;
typedef GAS unsigned long long gu64;
#define RLX_AGENT __ATOMIC_RELAXED, __HIP_MEMORY_SCOPE_AGENT
#define RLX_WG __ATOMIC_RELAXED, __HIP_MEMORY_SCOPE_WORKGROUP

namespace {

constexpr int NB = 8, DM = 1024, NH = 32, SK = 4096, DH = 128, FF = 3584;
constexpr int FS = FF / NH;
constexpr int NW = 8, NTHR = 64 * NW;
constexpr int SLOT = 16384;
#define GUNT " nt"
#define ROTK 0
#define PRE 1
#define GU_T 2
#define WO_AUX 0
#define WD_AUX 0
#define POLL_SLEEP 1
#define QNT " nt"
#define PACE (16 * 61)
#define TSTART 600
#define DEPTH 4

constexpr int SC_OFF = NW * SLOT;
constexpr int GU_OFF = SC_OFF + NW * 2048;
constexpr int Q_OFF = GU_OFF + 14336;
constexpr int O_OFF = Q_OFF + 256;
constexpr int HH_OFF = O_OFF + 256;
constexpr int CTL_OFF = HH_OFF + 256;
constexpr int LDS_BYTES = CTL_OFF + 256;
constexpr int C_ISS = 0, C_LAND = 1, C_FFN = 2, C_O = 3, C_QP = 4, C_QR = 5, C_GU = 6, C_OUT = 7, C_TAG = 8, C_POLL = 10, C_WMAX = 16, C_WSUM = 24, C_GP = 32;
static_assert(LDS_BYTES <= 160 * 1024, "LDS");

constexpr size_t WS_EPOCH = 0, WS_GRAN = 4096, WS_QGRAN = WS_GRAN + (size_t)NB * NH * DM * 8, WS_GUGRAN = WS_QGRAN + (size_t)NH * NB * NB * DH * 8;

constexpr unsigned SPIN_CAP = 1u << 22;
#define STAMP(i) do { if ((i) == MS_ID) ms_ = (unsigned)rt(); } while (0)
#define SDUMP() do { const unsigned long long dl = t0 + 15000u + MS_MUL * (ms_ - (unsigned)t0); while (rt() < dl) __builtin_amdgcn_s_sleep(2); } while (0)

__device__ __forceinline__ f32x4 mfma(v4u a, v4u b, f32x4 c) {
    return __builtin_amdgcn_mfma_f32_16x16x32_bf16(__builtin_bit_cast(bf16x8, a), __builtin_bit_cast(bf16x8, b), c, 0, 0, 0);
}
__device__ __forceinline__ float bf_lo(unsigned u) { return __builtin_bit_cast(float, u << 16); }
__device__ __forceinline__ float bf_hi(unsigned u) { return __builtin_bit_cast(float, u & 0xffff0000u); }
__device__ __forceinline__ float rbf(float f) { return (float)(__bf16)f; }
__device__ __forceinline__ unsigned bf_bits(float f) { return (unsigned)__builtin_bit_cast(unsigned short, (__bf16)f); }
__device__ __forceinline__ unsigned pack2(float lo, float hi) { return bf_bits(lo) | (bf_bits(hi) << 16); }
__device__ __forceinline__ unsigned gran(float f, unsigned tag_lo) { return ((__builtin_bit_cast(unsigned, f) + 0x80u) & 0xffffff00u) | tag_lo; }
__device__ __forceinline__ void store_dev(v4u d, __amdgpu_buffer_rsrc_t r, int vo, int so) {
    __builtin_amdgcn_raw_buffer_store_b128(d, r, vo, so, 16);
    asm volatile("s_nop 1" ::"v"(d) : "memory");
}
__device__ __forceinline__ unsigned long long rt() { return __builtin_amdgcn_s_memrealtime(); }
__device__ __forceinline__ unsigned rfl(unsigned v) { return (unsigned)__builtin_amdgcn_readfirstlane((int)v); }
#define LGKM0() asm volatile("s_waitcnt lgkmcnt(0)" ::: "memory")
#define VMCNT(n) asm volatile("s_waitcnt vmcnt(%0)" ::"i"(n) : "memory")
#define CBAR() asm volatile("" ::: "memory")
#define PIN2(a, b) do { asm volatile("" : "+v"(a), "+v"(b)); __builtin_amdgcn_sched_barrier(0); } while (0)

__device__ __forceinline__ void glds4(const void* sbase, unsigned v0, unsigned v1, unsigned v2, unsigned v3, unsigned lds) {
    unsigned keep;
    asm volatile("s_mov_b32 %0, m0\n\ts_mov_b32 m0, %6\n\ts_nop 0\n\t"
                 "global_load_lds_dwordx4 %1, %5 nt\n\t"
                 "global_load_lds_dwordx4 %2, %5 offset:1024 nt\n\t"
                 "global_load_lds_dwordx4 %3, %5 offset:2048 nt\n\t"
                 "global_load_lds_dwordx4 %4, %5 offset:3072 nt\n\t"
                 "s_mov_b32 m0, %0"
                 : "=&s"(keep) : "v"(v0), "v"(v1), "v"(v2), "v"(v3), "s"(sbase), "s"(lds) : "memory");
}
__device__ __forceinline__ void fill(const unsigned char* src, unsigned v0, unsigned v1, unsigned v2, unsigned v3, unsigned lds) {
    LGKM0();
#pragma unroll
    for (int q = 0; q < 4; ++q) glds4(src + q * 4096, v0, v1, v2, v3, lds + q * 4096);
}

__device__ __forceinline__ void wait_ge(LAS unsigned* w, unsigned need) {
    for (unsigned s = 0; rfl(*(volatile LAS unsigned*)w) < need && s < SPIN_CAP; ++s) __builtin_amdgcn_s_sleep(0);
    CBAR();
}
__device__ __forceinline__ void arrive(LAS unsigned* w, int lane) {
    LGKM0();
    if (lane == 0) __hip_atomic_fetch_add(w, 1u, RLX_WG);
    CBAR();
}

struct Sel { unsigned s[2][4]; };
__device__ __forceinline__ v4u diagA(unsigned S, const unsigned (&sel)[4]) {
    return v4u{__builtin_amdgcn_perm(0u, S, sel[0]), __builtin_amdgcn_perm(0u, S, sel[1]), __builtin_amdgcn_perm(0u, S, sel[2]), __builtin_amdgcn_perm(0u, S, sel[3])};
}
__device__ __forceinline__ int fresh_lane() {
    int l;
    asm volatile("v_mbcnt_lo_u32_b32 %0, -1, 0\n\tv_mbcnt_hi_u32_b32 %0, -1, %0" : "=v"(l));
    return l;
}
__device__ __forceinline__ Sel make_sel(int c) {
    Sel sel;
    const int dm = (c & 7) >> 1, hf = c & 1;
#pragma unroll
    for (int par = 0; par < 2; ++par)
#pragma unroll
        for (int d = 0; d < 4; ++d) {
            const unsigned lohi = (unsigned)(2 * par) | ((unsigned)(2 * par + 1) << 8);
            const unsigned v = hf ? (0x0c0cu | (lohi << 16)) : (0x0c0c0000u | lohi);
            sel.s[par][d] = (c < 8 && d == dm) ? v : 0x0c0c0c0cu;
        }
    return sel;
}

__device__ __forceinline__ void slot_rows64(const LAS unsigned char* slot, const LAS unsigned char* vec, const Sel& sel, int c, int g,
                                            f32x4& acc, f32x4& a1) {
    const LAS unsigned char* base = slot + g * 4096 + c * 16;
#pragma unroll
    for (int hf = 0; hf < 2; ++hf) {
        const v4u s = *(const LAS v4u*)(vec + g * 32 + hf * 16);
        v4u bb[8];
#pragma unroll
        for (int t = 0; t < 8; ++t) bb[t] = *(const LAS v4u*)(base + (8 * hf + t) * 256);
        __builtin_amdgcn_sched_barrier(0);
#pragma unroll
        for (int u = 0; u < 4; ++u) {
            acc = mfma(diagA(s[u], sel.s[0]), bb[2 * u], acc);
            a1 = mfma(diagA(s[u], sel.s[1]), bb[2 * u + 1], a1);
            PIN2(acc, a1);
        }
    }
}

__device__ __forceinline__ float gelu_tanh(float x) {
    const float y = 0.7978845608028654f * (x + 0.044715f * x * x * x);
    return rbf(x / (1.0f + __expf(-2.0f * y)));
}
__device__ __forceinline__ float wave_max(float v) {
#pragma unroll
    for (int o = 32; o >= 1; o >>= 1) v = fmaxf(v, __shfl_xor(v, o));
    return v;
}
__device__ __forceinline__ float wave_sum(float v) {
#pragma unroll
    for (int o = 32; o >= 1; o >>= 1) v += __shfl_xor(v, o);
    return v;
}

__global__ void __launch_bounds__(NTHR)
fused_decode_kernel(const unsigned char* __restrict__ xin, const unsigned char* __restrict__ kc, const unsigned char* __restrict__ vc,
                    const unsigned char* __restrict__ wq, const unsigned char* __restrict__ wo,
                    const unsigned char* __restrict__ wg, const unsigned char* __restrict__ wu, const unsigned char* __restrict__ wd,
                    const unsigned char* __restrict__ gatew, unsigned char* __restrict__ out, unsigned char* __restrict__ ws,
                    int pace, int tstart, int depth) {
    extern __shared__ __attribute__((aligned(16))) unsigned char smem[];
    LAS unsigned char* L = (LAS unsigned char*)smem;
    LAS unsigned* ctl = (LAS unsigned*)(L + CTL_OFF);
    const unsigned long long t0 = rt();
    const int w = __builtin_amdgcn_readfirstlane((int)threadIdx.x >> 6);
    const int lane = threadIdx.x & 63, c = lane & 15, g = lane >> 4;
    const int h = (blockIdx.x & 7) * 4 + ((blockIdx.x >> 3) & 3), b = blockIdx.x >> 5;
    const int fs = h, bh = b * NH + h;
    unsigned ms_ = (unsigned)t0;
    const unsigned myslot_a = (unsigned)(size_t)L + w * SLOT;
    const LAS unsigned char* myslot = L + w * SLOT;
    const unsigned char* kb = kc + (size_t)bh * (SK * DH * 2);
    const unsigned char* vb = vc + (size_t)bh * (SK * DH * 2);
    const unsigned rot = (unsigned)(ROTK) + (w ^ 4);
    auto toff = [&](int t) { return (size_t)(((8u * t + rot) & 63u) * SLOT); };
    const unsigned vk0 = g * 256 + ((c ^ g) << 4), vk1 = g * 256 + ((c ^ (4 + g)) << 4), vk2 = g * 256 + ((c ^ (8 + g)) << 4),
                   vk3 = g * 256 + ((c ^ (12 + g)) << 4);
    const int u = w ^ 4;
    auto take_turn = [&](int f) {
        const unsigned long long td = t0 + (unsigned)(tstart + (((f - 8) * pace) >> 4));
        while (rt() < td) __builtin_amdgcn_s_sleep(1);
        for (unsigned s = 0; s < SPIN_CAP; ++s) {
            const v2u st = *(volatile LAS v2u*)(ctl + C_ISS);
            if (rfl(st[0]) >= (unsigned)f && rfl(st[1]) + (unsigned)depth > (unsigned)f) break;
            __builtin_amdgcn_s_sleep(0);
        }
        CBAR();
    };
    auto issued = [&](int f) { if (lane == 0) *(volatile LAS unsigned*)(ctl + C_ISS) = (unsigned)f + 1u; CBAR(); };
    auto landed = [&]() { if (lane == 0) __hip_atomic_fetch_add(ctl + C_LAND, 1u, RLX_WG); CBAR(); };
    const Sel sel = make_sel(c & 7);
    LAS float* sc = (LAS float*)(L + SC_OFF + w * 2048);
    const __amdgpu_buffer_rsrc_t rgran = __builtin_amdgcn_make_buffer_rsrc((void*)(ws + WS_GRAN), 0, NB * NH * DM * 8, 0x00020000);
    const int gvo = (8 * c + 4 * (g & 1) + 2 * (g >> 1)) * 8, gso = bh * (DM * 8) + w * 1024;
    unsigned long long epoch;

    if (__builtin_expect(w < 4, 1)) {
        v4u x8[4], wr[8];
        v2u ep;
        const unsigned char* xs = xin + (128 * b + 32 * w) * 2;
        const unsigned vx = (c >> 3) * (DM * 2) + g * 16;
        const unsigned char* qs = wq + (size_t)(128 * b + 32 * w) * (NH * DH * 2) + h * 256;
        const unsigned vq = g * 8 * (NH * DH * 2) + c * 16;
#pragma unroll
        for (int t = 0; t < 8; ++t)
            asm volatile("global_load_dwordx4 %0, %1, %2" QNT : "=v"(wr[t]) : "v"(vq), "s"(qs + (size_t)t * (NH * DH * 2)) : "memory");
#pragma unroll
        for (int j = 0; j < 4; ++j)
            asm volatile("global_load_dwordx4 %0, %1, %2" : "=v"(x8[j]) : "v"(vx), "s"(xs + j * (2 * DM * 2)) : "memory");
        asm volatile("global_load_dwordx2 %0, %1, %2 sc1" : "=v"(ep) : "v"(0u), "s"(ws + WS_EPOCH) : "memory");
        const unsigned char* inbox = ws + WS_QGRAN + (size_t)(h * NB + b) * NB * (DH * 8);
        unsigned warm;
        asm volatile("global_load_dword %0, %1, %2 sc1" : "=v"(warm) : "v"(lane * 16), "s"(inbox) : "memory");
        STAMP(42);
        __builtin_amdgcn_s_barrier();
        VMCNT(0);
        asm volatile("" : "+v"(x8[0]), "+v"(x8[1]), "+v"(x8[2]), "+v"(x8[3]), "+v"(wr[0]), "+v"(wr[1]), "+v"(wr[2]), "+v"(wr[3]),
                     "+v"(wr[4]), "+v"(wr[5]), "+v"(wr[6]), "+v"(wr[7]), "+v"(warm), "+v"(ep) :: "memory");
        STAMP(45);
        {
            const unsigned e0 = ep[0], e1 = ep[1];
            epoch = (((unsigned long long)rfl(e1) << 32) | rfl(e0)) + 1ull;
        }
        const unsigned qtag_hi = (unsigned)(epoch >> 8), qtag_lo = (unsigned)epoch & 0xffu;
#pragma unroll
        for (int j = 0; j < 4; ++j) {
            f32x4 a0 = {0.f, 0.f, 0.f, 0.f}, a1 = {0.f, 0.f, 0.f, 0.f};
#pragma unroll
            for (int k = 0; k < 4; ++k) {
                a0 = mfma(diagA(x8[j][k], sel.s[0]), wr[2 * k], a0);
                a1 = mfma(diagA(x8[j][k], sel.s[1]), wr[2 * k + 1], a1);
            }
            a0 += a1;
            *(LAS f32x4*)(L + SC_OFF + ((w * 8 + 2 * j + (g >> 1)) * 128 + 8 * c + 4 * (g & 1)) * 4) = a0;
        }
        if (w == 0 && lane == 0) *(LAS v2u*)(ctl + C_TAG) = v2u{(unsigned)epoch, (unsigned)(epoch >> 32)};
        arrive(ctl + C_QP, lane);
        wait_ge(ctl + C_QP, 4);
        STAMP(1);
        {
            const __amdgpu_buffer_rsrc_t rqg = __builtin_amdgcn_make_buffer_rsrc((void*)(ws + WS_QGRAN), 0, NH * NB * NB * DH * 8, 0x00020000);
#pragma unroll
            for (int r = 0; r < 2; ++r) {
                float s0 = 0.f, s1 = 0.f;
#pragma unroll
                for (int k = 0; k < 4; ++k) { const f32x2 v = *(const LAS f32x2*)(L + SC_OFF + ((k * 8 + 2 * w + r) * 128 + 2 * lane) * 4); s0 += v[0]; s1 += v[1]; }
                store_dev(v4u{gran(s0, qtag_lo), qtag_hi, gran(s1, qtag_lo), qtag_hi}, rqg, lane * 16, ((h * NB + 2 * w + r) * NB + b) * (DH * 8));
            }
        }
        arrive(ctl + C_QP, lane);
        STAMP(46);
        if (w == 3) {
            VMCNT(0);
            STAMP(55);
            const unsigned vo = lane * 16;
            v4u y[NB];
            for (unsigned s = 0;; ++s) {
#pragma unroll
                for (int p = 0; p < NB; ++p)
                    asm volatile("global_load_dwordx4 %0, %1, %2 offset:%3 sc1" : "=v"(y[p]) : "v"(vo), "s"(inbox + (p >> 2) * 4096), "i"((p & 3) * 1024) : "memory");
                if (s == 0) {
                    if (lane == 0) *(volatile LAS unsigned*)(ctl + C_POLL) = 1u;
                    fill(kb + toff(0), vk0, vk1, vk2, vk3, myslot_a);
                    VMCNT(16);
                } else {
                    VMCNT(0);
                }
                asm volatile("" : "+v"(y[0]), "+v"(y[1]), "+v"(y[2]), "+v"(y[3]), "+v"(y[4]), "+v"(y[5]), "+v"(y[6]), "+v"(y[7]) :: "memory");
                unsigned bad = 0u;
#pragma unroll
                for (int p = 0; p < NB; ++p) bad |= (y[p][1] ^ qtag_hi) | (y[p][3] ^ qtag_hi) | (((y[p][0] ^ qtag_lo) | (y[p][2] ^ qtag_lo)) & 0xffu);
                if (__all(bad == 0u) || s > SPIN_CAP) break;
                __builtin_amdgcn_s_sleep(POLL_SLEEP);
            }
            float s0 = 0.f, s1 = 0.f;
#pragma unroll
            for (int p = 0; p < NB; ++p) { s0 += __builtin_bit_cast(float, y[p][0] & 0xffffff00u); s1 += __builtin_bit_cast(float, y[p][2] & 0xffffff00u); }
            ((LAS unsigned*)(L + Q_OFF))[lane] = pack2(s0, s1);
            wait_ge(ctl + C_QP, 8);
            arrive(ctl + C_QR, lane);
        } else {
            if (w >= PRE) wait_ge(ctl + C_POLL, 1);
            fill(kb + toff(0), vk0, vk1, vk2, vk3, myslot_a);
        }
        STAMP(47);
        VMCNT(0);
        landed();
    } else {
        if (w == 4 && lane < 16) ctl[lane] = lane == C_ISS ? 8u : 0u;
        LGKM0();
        __builtin_amdgcn_s_barrier();
        fill(kb + toff(0), vk0, vk1, vk2, vk3, myslot_a);
        STAMP(43);
        VMCNT(0);
        landed();
        STAMP(2);
        wait_ge(ctl + C_QP, 4);
        const v2u e = *(volatile LAS v2u*)(ctl + C_TAG);
        const unsigned e0 = e[0], e1 = e[1];
        epoch = ((unsigned long long)rfl(e1) << 32) | rfl(e0);
    }
    const unsigned tag_hi = (unsigned)(epoch >> 8), tag_lo = (unsigned)epoch & 0xffu;
    wait_ge(ctl + C_QR, 1);
    v4u qf[4];
#pragma unroll
    for (int s = 0; s < 4; ++s) qf[s] = *(const LAS v4u*)(L + Q_OFF + (32 * s + 8 * g) * 2);
    LGKM0();
    STAMP(3);

    float mx = -INFINITY;
    v4u wor[32];
    const __amdgpu_buffer_rsrc_t ro = __builtin_amdgcn_make_buffer_rsrc((void*)wo, 0, DM * NH * DH * 2, 0x00020000);
    const int soo = h * DH * (DM * 2) + w * 256;
#pragma unroll
    for (int t = 0; t < 8; ++t) {
        if (t > 0) { take_turn(8 * t + u); fill(kb + toff(t), vk0, vk1, vk2, vk3, myslot_a); issued(8 * t + u); }
        STAMP(8 + t);
        v4u xg[4][2], gw[16];
        if (t > 0) {
            const int l2 = fresh_lane();
            const int voo = 32 * (l2 >> 4) * (DM * 2) + (l2 & 15) * 16;
            v4u* wp = wor + 4 * (t - 1);
#pragma unroll
            for (int r = 0; r < 4; ++r) wp[r] = __builtin_amdgcn_raw_buffer_load_b128(ro, voo, soo + (4 * (t - 1) + r) * (DM * 2), WO_AUX);
            if (t == 1) {
                const __amdgpu_buffer_rsrc_t rx = __builtin_amdgcn_make_buffer_rsrc((void*)xin, 0, NB * DM * 2, 0x00020000);
                const __amdgpu_buffer_rsrc_t rw = __builtin_amdgcn_make_buffer_rsrc((void*)gatew, 0, DM * 2, 0x00020000);
                v4u xv = __builtin_amdgcn_raw_buffer_load_b128(rx, (l2 & 15) * 16, b * (DM * 2) + w * 256, 0);
                v4u gv = __builtin_amdgcn_raw_buffer_load_b128(rw, (l2 & 15) * 16, w * 256, 0);
                asm volatile("s_waitcnt vmcnt(0)" : "+v"(wp[0]), "+v"(wp[1]), "+v"(wp[2]), "+v"(wp[3]), "+v"(xv), "+v"(gv) :: "memory");
                landed();
                store_dev(v4u{tag_lo ^ 1u, ~tag_hi, tag_lo ^ 1u, ~tag_hi}, rgran, gvo, gso);
                float s = 0.f;
#pragma unroll
                for (int k = 0; k < 4; ++k) s += rbf(bf_lo(xv[k]) * bf_lo(gv[k])) + rbf(bf_hi(xv[k]) * bf_hi(gv[k]));
                s = wave_sum(l2 < 16 ? s : 0.f);
                if (l2 == 0) ((LAS float*)ctl)[C_GP + w] = s;
            } else if (t == GU_T && w >= 4) {
                const int m = (w >> 1) & 1, hf = w & 1;
                const unsigned char* gs = (m ? wu : wg) + (size_t)(128 * b + 64 * hf) * (FF * 2) + fs * (FS * 2);
                const unsigned vog = 16 * (l2 >> 4) * (FF * 2) + ((l2 & 15) < 14 ? (l2 & 15) : 13) * 16;
#pragma unroll
                for (int k = 0; k < 16; ++k)
                    asm volatile("global_load_dwordx4 %0, %1, %2" GUNT : "=v"(gw[k]) : "v"(vog), "s"(gs + (size_t)k * (FF * 2)) : "memory");
                const unsigned char* xs = xin + (128 * b + 64 * hf) * 2;
                const unsigned vox = ((l2 & 15) >> 3) * (DM * 2) + (l2 >> 4) * 32;
#pragma unroll
                for (int j = 0; j < 4; ++j) {
                    asm volatile("global_load_dwordx4 %0, %1, %2" : "=v"(xg[j][0]) : "v"(vox), "s"(xs + j * (2 * DM * 2)) : "memory");
                    asm volatile("global_load_dwordx4 %0, %1, %2 offset:16" : "=v"(xg[j][1]) : "v"(vox), "s"(xs + j * (2 * DM * 2)) : "memory");
                }
                VMCNT(24);
            } else if (t == GU_T + 1 && w >= 4) {
                asm volatile("s_waitcnt vmcnt(0)" : "+v"(wp[0]), "+v"(wp[1]), "+v"(wp[2]), "+v"(wp[3]) :: "memory");
                landed();
                wait_ge(ctl + C_GU, 4);
                const __amdgpu_buffer_rsrc_t rgg = __builtin_amdgcn_make_buffer_rsrc((void*)(ws + WS_GUGRAN), 0, NH * NB * NB * 2048, 0x00020000);
#pragma unroll
                for (int r = 0; r < 2; ++r)
#pragma unroll
                    for (int q = 0; q < 2; ++q) {
                        const int p = q == 0 ? l2 : (l2 < 48 ? 64 + l2 : 111);
                        const int mm = p >= 56 ? 1 : 0, col = 2 * (p - 56 * mm), cons = 2 * (w - 4) + r;
                        const f32x2 v0 = *(const LAS f32x2*)(L + GU_OFF + (((2 * mm) * 8 + cons) * FS + col) * 4);
                        const f32x2 v1 = *(const LAS f32x2*)(L + GU_OFF + (((2 * mm + 1) * 8 + cons) * FS + col) * 4);
                        const float s0 = v0[0] + v1[0], s1 = v0[1] + v1[1];
                        if (q == 0 || l2 < 48)
                            store_dev(v4u{gran(s0, tag_lo), tag_hi, gran(s1, tag_lo), tag_hi}, rgg, p * 16, ((h * NB + cons) * NB + b) * 2048);
                    }
                arrive(ctl + C_GU, l2);
            } else if (t == 7) {
                const __amdgpu_buffer_rsrc_t rgg = __builtin_amdgcn_make_buffer_rsrc((void*)(ws + WS_GUGRAN), 0, NH * NB * NB * 2048, 0x00020000);
                const int sgg = ((h * NB + b) * NB + w) * 2048, v1o = 1024 + (l2 < 48 ? l2 : 47) * 16;
                v4u y0, y1;
                for (unsigned s = 0;; ++s) {
                    y0 = __builtin_amdgcn_raw_buffer_load_b128(rgg, l2 * 16, sgg, 16);
                    y1 = __builtin_amdgcn_raw_buffer_load_b128(rgg, v1o, sgg, 16);
                    asm volatile("s_waitcnt vmcnt(0)" : "+v"(wp[0]), "+v"(wp[1]), "+v"(wp[2]), "+v"(wp[3]), "+v"(y0), "+v"(y1) :: "memory");
                    const unsigned bad = (y0[1] ^ tag_hi) | (y0[3] ^ tag_hi) | (y1[1] ^ tag_hi) | (y1[3] ^ tag_hi) | (((y0[0] ^ tag_lo) | (y0[2] ^ tag_lo) | (y1[0] ^ tag_lo) | (y1[2] ^ tag_lo)) & 0xffu);
                    if (s == 0) landed();
                    if (__all(bad == 0u) || s > SPIN_CAP) break;
                    __builtin_amdgcn_s_sleep(1);
                }
                wait_ge(ctl + C_GU, 8);
                *(LAS v2u*)(L + GU_OFF + w * 896 + l2 * 8) = v2u{y0[0] & 0xffffff00u, y0[2] & 0xffffff00u};
                if (l2 < 48) *(LAS v2u*)(L + GU_OFF + w * 896 + 512 + l2 * 8) = v2u{y1[0] & 0xffffff00u, y1[2] & 0xffffff00u};
                arrive(ctl + C_FFN, l2);
            } else {
                asm volatile("s_waitcnt vmcnt(0)" : "+v"(wp[0]), "+v"(wp[1]), "+v"(wp[2]), "+v"(wp[3]) :: "memory");
            }
            if (t != 7 && t != 1 && !(t == GU_T + 1 && w >= 4)) landed();
        }
        STAMP(16 + t);
        f32x4 s4[4];
#pragma unroll
        for (int kg = 0; kg < 4; ++kg) {
            f32x4 a = {0.f, 0.f, 0.f, 0.f};
#pragma unroll
            for (int s = 0; s < 4; ++s) {
                const v4u kf = *(const LAS v4u*)(myslot + (16 * kg + c) * 256 + (((4 * s + g) ^ c) << 4));
                a = mfma(kf, qf[s], a);
            }
            s4[kg] = a;
        }
        f32x4 v;
#pragma unroll
        for (int i = 0; i < 4; ++i) {
            const float lo = (c & 1) ? s4[1][i] : s4[0][i], hi = (c & 1) ? s4[3][i] : s4[2][i];
            v[i] = rbf(rbf((c & 2) ? hi : lo) * 0.08838834764831845f);
            mx = fmaxf(mx, v[i]);
        }
        if (c < 4) *(LAS f32x4*)(sc + t * 64 + 16 * c + 4 * g) = v;
        __builtin_amdgcn_sched_barrier(0);
        if (t == GU_T && w >= 4) {
            v4u* wp = wor + 4 * (t - 1);
            asm volatile("s_waitcnt vmcnt(0)" : "+v"(gw[0]), "+v"(gw[1]), "+v"(gw[2]), "+v"(gw[3]), "+v"(gw[4]), "+v"(gw[5]), "+v"(gw[6]), "+v"(gw[7]),
                         "+v"(gw[8]), "+v"(gw[9]), "+v"(gw[10]), "+v"(gw[11]), "+v"(gw[12]), "+v"(gw[13]), "+v"(gw[14]), "+v"(gw[15]),
                         "+v"(xg[0][0]), "+v"(xg[0][1]), "+v"(xg[1][0]), "+v"(xg[1][1]), "+v"(xg[2][0]), "+v"(xg[2][1]), "+v"(xg[3][0]), "+v"(xg[3][1]) :: "memory");
            asm volatile("" : "+v"(wp[0]), "+v"(wp[1]), "+v"(wp[2]), "+v"(wp[3]));
            STAMP(44);
            f32x4 a[4];
#pragma unroll
            for (int j = 0; j < 4; ++j) a[j] = f32x4{0.f, 0.f, 0.f, 0.f};
#pragma unroll
            for (int k = 0; k < 16; ++k) {
#pragma unroll
                for (int j = 0; j < 4; ++j) a[j] = mfma(diagA(xg[j][k >> 3][(k >> 1) & 3], sel.s[k & 1]), gw[k], a[j]);
                PIN2(a[0], a[1]); PIN2(a[2], a[3]);
            }
            if (c < 14) {
#pragma unroll
                for (int j = 0; j < 4; ++j)
                    *(LAS f32x4*)(L + GU_OFF + ((((w - 4) * 8 + 2 * j + (g >> 1)) * FS) + 8 * c + 4 * (g & 1)) * 4) = a[j];
            }
            arrive(ctl + C_GU, lane);
            STAMP(56);
        }
    }
    STAMP(4);
    float lsum;
    mx = wave_max(mx);
    {
        const f32x4 sa = *(const LAS f32x4*)(sc + 8 * lane), sb = *(const LAS f32x4*)(sc + 8 * lane + 4);
        float e[8];
#pragma unroll
        for (int i = 0; i < 4; ++i) { e[i] = __expf(sa[i] - mx); e[4 + i] = __expf(sb[i] - mx); }
        lsum = wave_sum(((e[0] + e[1]) + (e[2] + e[3])) + ((e[4] + e[5]) + (e[6] + e[7])));
        v4u pk;
#pragma unroll
        for (int i = 0; i < 4; ++i) pk[i] = pack2(e[2 * i], e[2 * i + 1]);
        LGKM0();
        *(LAS v4u*)((LAS unsigned char*)sc + 16 * lane) = pk;
    }
    STAMP(5);

    f32x4 acco = {0.f, 0.f, 0.f, 0.f}, acco1 = {0.f, 0.f, 0.f, 0.f}, accb = {0.f, 0.f, 0.f, 0.f};
    {
        const unsigned vlin = lane * 16;
        {
            take_turn(64 + u);
            fill(vb + toff(0), vlin, vlin, vlin, vlin, myslot_a);
            issued(64 + u);
            STAMP(24);
            const int voo = 32 * g * (DM * 2) + c * 16;
#pragma unroll
            for (int r = 0; r < 4; ++r) wor[28 + r] = __builtin_amdgcn_raw_buffer_load_b128(ro, voo, soo + (28 + r) * (DM * 2), WO_AUX);
            asm volatile("s_waitcnt vmcnt(0)" : "+v"(wor[28]), "+v"(wor[29]), "+v"(wor[30]), "+v"(wor[31]) :: "memory");
            landed();
            STAMP(32);
            slot_rows64(myslot, (const LAS unsigned char*)sc, sel, c, g, acco, acco1);
        }
        const __amdgpu_buffer_rsrc_t rd = __builtin_amdgcn_make_buffer_rsrc((void*)wd, 0, FF * DM * 2, 0x00020000);
        const int vod = 28 * g * (DM * 2) + c * 16;
#pragma unroll 1
        for (int t = 1; t < 8; ++t) {
            take_turn(64 + 8 * t + u);
            fill(vb + toff(t), vlin, vlin, vlin, vlin, myslot_a);
            issued(64 + 8 * t + u);
            STAMP(24 + t);
            const int tt = t - 1;
            const int sod = (fs * FS + 4 * tt) * (DM * 2) + w * 256;
            v4u dw[4];
#pragma unroll
            for (int r = 0; r < 4; ++r) dw[r] = __builtin_amdgcn_raw_buffer_load_b128(rd, vod, sod + r * (DM * 2), WD_AUX);
            if (t == 1) {
                wait_ge(ctl + C_FFN, NW);
                const int l2 = fresh_lane();
                if (l2 < FS / 2) {
                    float g0 = 0.f, g1 = 0.f, u0 = 0.f, u1 = 0.f;
#pragma unroll
                    for (int ww = 0; ww < NW; ++ww) {
                        const f32x2 a = *(const LAS f32x2*)(L + GU_OFF + ww * 896 + 8 * l2), bb = *(const LAS f32x2*)(L + GU_OFF + ww * 896 + 448 + 8 * l2);
                        g0 += a[0]; g1 += a[1]; u0 += bb[0]; u1 += bb[1];
                    }
                    ((LAS unsigned*)(L + HH_OFF))[l2] = pack2(gelu_tanh(rbf(g0)) * rbf(u0), gelu_tanh(rbf(g1)) * rbf(u1));
                }
                LGKM0();
            }
            const v2u hs = *(const LAS v2u*)(L + HH_OFF + (28 * g + 4 * tt) * 2);
            asm volatile("s_waitcnt vmcnt(0)" : "+v"(dw[0]), "+v"(dw[1]), "+v"(dw[2]), "+v"(dw[3]) :: "memory");
            landed();
            STAMP(32 + t);
            slot_rows64(myslot, (const LAS unsigned char*)sc + t * 128, sel, c, g, acco, acco1);
#pragma unroll
            for (int r = 0; r < 4; ++r) accb = mfma(diagA(hs[r >> 1], sel.s[r & 1]), dw[r], accb);
        }
    }
    STAMP(6);
    acco += acco1;
    if (g < 2) *(LAS f32x4*)((LAS unsigned char*)sc + 1024 + (8 * c + 4 * g) * 4) = acco;
    if (lane == 0) { ((LAS float*)ctl)[C_WMAX + w] = mx; ((LAS float*)ctl)[C_WSUM + w] = lsum; }
    arrive(ctl + C_O, lane);
    wait_ge(ctl + C_O, NW);
    v4u of[4];
    {
        float m[NW], M = -INFINITY;
#pragma unroll
        for (int ww = 0; ww < NW; ++ww) { m[ww] = ((LAS float*)ctl)[C_WMAX + ww]; M = fmaxf(M, m[ww]); }
        float Ls = 0.f, s0 = 0.f, s1 = 0.f;
#pragma unroll
        for (int ww = 0; ww < NW; ++ww) {
            const float f = __expf(m[ww] - M);
            Ls += f * ((LAS float*)ctl)[C_WSUM + ww];
            const f32x2 v = *(const LAS f32x2*)(L + SC_OFF + ww * 2048 + 1024 + 8 * lane);
            s0 += f * v[0]; s1 += f * v[1];
        }
        const float inv = 1.0f / Ls;
        ((LAS unsigned*)(L + O_OFF))[lane] = pack2(s0 * inv, s1 * inv);
        LGKM0();
#pragma unroll
        for (int k = 0; k < 4; ++k) of[k] = *(const LAS v4u*)(L + O_OFF + 64 * g + 16 * k);
    }
    STAMP(7);
    f32x4 acca = {0.f, 0.f, 0.f, 0.f}, acca1 = {0.f, 0.f, 0.f, 0.f};
#pragma unroll
    for (int t = 0; t < 32; t += 2) {
        const unsigned S = of[t >> 3][(t >> 1) & 3];
        acca = mfma(diagA(S, sel.s[0]), wor[t], acca);
        acca1 = mfma(diagA(S, sel.s[1]), wor[t + 1], acca1);
    }
    acca += acca1;
    STAMP(40);
    {
        float z = 0.f;
#pragma unroll
        for (int ww = 0; ww < NW; ++ww) z += ((LAS float*)ctl)[C_GP + ww];
        z = rbf(z);
        const float gate = rbf(1.0f / (1.0f + __expf(-z)));
        const float omg = rbf(1.0f - gate);
        {
            const float r0 = gate * (g < 2 ? acca[0] : acca[2]) + omg * (g < 2 ? accb[0] : accb[2]);
            const float r1 = gate * (g < 2 ? acca[1] : acca[3]) + omg * (g < 2 ? accb[1] : accb[3]);
            store_dev(v4u{gran(r0, tag_lo), tag_hi, gran(r1, tag_lo), tag_hi}, rgran, gvo, gso);
        }
        const int l2 = fresh_lane();
        const int vo = ((l2 >> 4) * DM + 2 * (l2 & 15)) * 8, so = ((b * NH + 4 * w) * DM + 32 * h) * 8;
        v4u y;
        for (unsigned s = 0;; ++s) {
            y = __builtin_amdgcn_raw_buffer_load_b128(rgran, vo, so, 16);
            const unsigned bad = (y[1] ^ tag_hi) | (y[3] ^ tag_hi) | (((y[0] ^ tag_lo) | (y[2] ^ tag_lo)) & 0xffu);
            if (__all(bad == 0u) || s > SPIN_CAP) break;
            __builtin_amdgcn_s_sleep(1);
        }
        STAMP(41);
        float v0 = __builtin_bit_cast(float, y[0] & 0xffffff00u), v1 = __builtin_bit_cast(float, y[2] & 0xffffff00u);
        v0 += __shfl_xor(v0, 16); v1 += __shfl_xor(v1, 16);
        v0 += __shfl_xor(v0, 32); v1 += __shfl_xor(v1, 32);
        if (l2 < 16) *(LAS f32x2*)(L + GU_OFF + w * 128 + l2 * 8) = f32x2{v0, v1};
        LGKM0();
        unsigned old = 0u;
        if (l2 == 0) old = __hip_atomic_fetch_add(ctl + C_OUT, 1u, RLX_WG);
        if (rfl(old) == NW - 1) {
            CBAR();
            if (l2 < 16) {
                float s0 = 0.f, s1 = 0.f;
#pragma unroll
                for (int ww = 0; ww < NW; ++ww) { const f32x2 v = *(const LAS f32x2*)(L + GU_OFF + ww * 128 + l2 * 8); s0 += v[0]; s1 += v[1]; }
                __hip_atomic_store((gu32*)(out + ((size_t)b * DM + 32 * h + 2 * l2) * 2), pack2(s0, s1), __ATOMIC_RELAXED, __HIP_MEMORY_SCOPE_SYSTEM);
            }
            if (bh == 0 && l2 == 0) __hip_atomic_store((gu64*)(ws + WS_EPOCH), epoch, RLX_AGENT);
        }
        STAMP(57);
    }
    SDUMP();
}

}

extern "C" void kernel_launch(void* const* d_in, const int* in_sizes, int n_in, void* d_out, int out_size, void* d_ws, size_t ws_size,
                              hipStream_t stream) {
    (void)in_sizes; (void)n_in; (void)out_size; (void)ws_size;
    (void)hipFuncSetAttribute((const void*)fused_decode_kernel, hipFuncAttributeMaxDynamicSharedMemorySize, LDS_BYTES);
    int pace = PACE, tstart = TSTART, depth = DEPTH;
    fused_decode_kernel<<<dim3(NB * NH), dim3(NTHR), LDS_BYTES, stream>>>(
        (const unsigned char*)d_in[0], (const unsigned char*)d_in[1], (const unsigned char*)d_in[2], (const unsigned char*)d_in[3],
        (const unsigned char*)d_in[4], (const unsigned char*)d_in[6], (const unsigned char*)d_in[7],
        (const unsigned char*)d_in[8], (const unsigned char*)d_in[9], (unsigned char*)d_out, (unsigned char*)d_ws, pace, tstart, depth);
}
```

```cpp
#include <hip/hip_runtime.h>
#include <hip/hip_bf16.h>

#define GAS __attribute__((address_space(1)))
#define LAS __attribute__((address_space(3)))
typedef unsigned v4u __attribute__((ext_vector_type(4)));
typedef unsigned v2u __attribute__((ext_vector_type(2)));
typedef float f32x4 __attribute__((ext_vector_type(4)));
typedef float f32x2 __attribute__((ext_vector_type(2)));
typedef short bf16x8 __attribute__((ext_vector_type(8)));
typedef GAS unsigned gu32;
typedef GAS unsigned long long gu64;
#define RLX_AGENT __ATOMIC_RELAXED, __HIP_MEMORY_SCOPE_AGENT
#define RLX_WG __ATOMIC_RELAXED, __HIP_MEMORY_SCOPE_WORKGROUP

namespace {

constexpr int NB = 8, DM = 1024, NH = 32, SK = 4096, DH = 128, FF = 3584;
constexpr int FS = FF / NH;
constexpr int NW = 8, NTHR = 64 * NW;
constexpr int SLOT = 16384;
#define GUNT " nt"
#define ROTK 0
#define PRE 1
#define GU_T 2
#define WO_AUX 0
#define WD_AUX 0
#define POLL_SLEEP 1
#define QNT " nt"
#define CODE_PF 1
#define CODE_PF2 5
#define INVAL 0
#define PACE (16 * 61)
#define TSTART 600
#define DEPTH 4

constexpr int SC_OFF = NW * SLOT;
constexpr int GU_OFF = SC_OFF + NW * 2048;
constexpr int Q_OFF = GU_OFF + 14336;
constexpr int O_OFF = Q_OFF + 256;
constexpr int HH_OFF = O_OFF + 256;
constexpr int CTL_OFF = HH_OFF + 256;
constexpr int LDS_BYTES = CTL_OFF + 256;
constexpr int C_ISS = 0, C_LAND = 1, C_FFN = 2, C_O = 3, C_QP = 4, C_QR = 5, C_GU = 6, C_OUT = 7, C_TAG = 8, C_POLL = 10, C_WMAX = 16, C_WSUM = 24, C_GP = 32;
static_assert(LDS_BYTES <= 160 * 1024, "LDS");

constexpr size_t WS_EPOCH = 0, WS_GRAN = 4096, WS_QGRAN = WS_GRAN + (size_t)NB * NH * DM * 8, WS_GUGRAN = WS_QGRAN + (size_t)NH * NB * NB * DH * 8;

constexpr unsigned SPIN_CAP = 1u << 22;
#define STAMP(i) do {} while (0)
#define SDUMP() do {} while (0)

__device__ __forceinline__ f32x4 mfma(v4u a, v4u b, f32x4 c) {
    return __builtin_amdgcn_mfma_f32_16x16x32_bf16(__builtin_bit_cast(bf16x8, a), __builtin_bit_cast(bf16x8, b), c, 0, 0, 0);
}
__device__ __forceinline__ float bf_lo(unsigned u) { return __builtin_bit_cast(float, u << 16); }
__device__ __forceinline__ float bf_hi(unsigned u) { return __builtin_bit_cast(float, u & 0xffff0000u); }
__device__ __forceinline__ float rbf(float f) { return (float)(__bf16)f; }
__device__ __forceinline__ unsigned bf_bits(float f) { return (unsigned)__builtin_bit_cast(unsigned short, (__bf16)f); }
__device__ __forceinline__ unsigned pack2(float lo, float hi) { return bf_bits(lo) | (bf_bits(hi) << 16); }
__device__ __forceinline__ unsigned gran(float f, unsigned tag_lo) { return ((__builtin_bit_cast(unsigned, f) + 0x80u) & 0xffffff00u) | tag_lo; }
__device__ __forceinline__ void store_dev(v4u d, __amdgpu_buffer_rsrc_t r, int vo, int so) {
    __builtin_amdgcn_raw_buffer_store_b128(d, r, vo, so, 16);
    asm volatile("s_nop 1" ::"v"(d) : "memory");
}
__device__ __forceinline__ unsigned long long rt() { return __builtin_amdgcn_s_memrealtime(); }
__device__ __forceinline__ unsigned rfl(unsigned v) { return (unsigned)__builtin_amdgcn_readfirstlane((int)v); }
#define LGKM0() asm volatile("s_waitcnt lgkmcnt(0)" ::: "memory")
#define VMCNT(n) asm volatile("s_waitcnt vmcnt(%0)" ::"i"(n) : "memory")
#define CBAR() asm volatile("" ::: "memory")
#define PIN2(a, b) do { asm volatile("" : "+v"(a), "+v"(b)); __builtin_amdgcn_sched_barrier(0); } while (0)

__device__ __forceinline__ void glds4(const void* sbase, unsigned v0, unsigned v1, unsigned v2, unsigned v3, unsigned lds) {
    unsigned keep;
    asm volatile("s_mov_b32 %0, m0\n\ts_mov_b32 m0, %6\n\ts_nop 0\n\t"
                 "global_load_lds_dwordx4 %1, %5 nt\n\t"
                 "global_load_lds_dwordx4 %2, %5 offset:1024 nt\n\t"
                 "global_load_lds_dwordx4 %3, %5 offset:2048 nt\n\t"
                 "global_load_lds_dwordx4 %4, %5 offset:3072 nt\n\t"
                 "s_mov_b32 m0, %0"
                 : "=&s"(keep) : "v"(v0), "v"(v1), "v"(v2), "v"(v3), "s"(sbase), "s"(lds) : "memory");
}
__device__ __forceinline__ void fill(const unsigned char* src, unsigned v0, unsigned v1, unsigned v2, unsigned v3, unsigned lds) {
    LGKM0();
#pragma unroll
    for (int q = 0; q < 4; ++q) glds4(src + q * 4096, v0, v1, v2, v3, lds + q * 4096);
}

__device__ __forceinline__ void wait_ge(LAS unsigned* w, unsigned need) {
    for (unsigned s = 0; rfl(*(volatile LAS unsigned*)w) < need && s < SPIN_CAP; ++s) __builtin_amdgcn_s_sleep(0);
    CBAR();
}
__device__ __forceinline__ void arrive(LAS unsigned* w, int lane) {
    LGKM0();
    if (lane == 0) __hip_atomic_fetch_add(w, 1u, RLX_WG);
    CBAR();
}

struct Sel { unsigned s[2][4]; };
__device__ __forceinline__ v4u diagA(unsigned S, const unsigned (&sel)[4]) {
    return v4u{__builtin_amdgcn_perm(0u, S, sel[0]), __builtin_amdgcn_perm(0u, S, sel[1]), __builtin_amdgcn_perm(0u, S, sel[2]), __builtin_amdgcn_perm(0u, S, sel[3])};
}
__device__ __forceinline__ int fresh_lane() {
    int l;
    asm volatile("v_mbcnt_lo_u32_b32 %0, -1, 0\n\tv_mbcnt_hi_u32_b32 %0, -1, %0" : "=v"(l));
    __builtin_assume((unsigned)l < 64u);
    return l;
}
__device__ __forceinline__ Sel make_sel(int c) {
    Sel sel;
    const int dm = (c & 7) >> 1, hf = c & 1;
#pragma unroll
    for (int par = 0; par < 2; ++par)
#pragma unroll
        for (int d = 0; d < 4; ++d) {
            const unsigned lohi = (unsigned)(2 * par) | ((unsigned)(2 * par + 1) << 8);
            const unsigned v = hf ? (0x0c0cu | (lohi << 16)) : (0x0c0c0000u | lohi);
            sel.s[par][d] = (c < 8 && d == dm) ? v : 0x0c0c0c0cu;
        }
    return sel;
}

__device__ __forceinline__ void slot_rows64(const LAS unsigned char* slot, const LAS unsigned char* vec, const Sel& sel, int c, int g,
                                            f32x4& acc, f32x4& a1) {
    const LAS unsigned char* base = slot + g * 4096 + c * 16;
#pragma unroll
    for (int hf = 0; hf < 2; ++hf) {
        const v4u s = *(const LAS v4u*)(vec + g * 32 + hf * 16);
        v4u bb[8];
#pragma unroll
        for (int t = 0; t < 8; ++t) bb[t] = *(const LAS v4u*)(base + (8 * hf + t) * 256);
        __builtin_amdgcn_sched_barrier(0);
#pragma unroll
        for (int u = 0; u < 4; ++u) {
            acc = mfma(diagA(s[u], sel.s[0]), bb[2 * u], acc);
            a1 = mfma(diagA(s[u], sel.s[1]), bb[2 * u + 1], a1);
            PIN2(acc, a1);
        }
    }
}

__device__ __forceinline__ float gelu_tanh(float x) {
    const float y = 0.7978845608028654f * (x + 0.044715f * x * x * x);
    return rbf(x / (1.0f + __expf(-2.0f * y)));
}
__device__ __forceinline__ float wave_max(float v) {
#pragma unroll
    for (int o = 32; o >= 1; o >>= 1) v = fmaxf(v, __shfl_xor(v, o));
    return v;
}
__device__ __forceinline__ float wave_sum(float v) {
#pragma unroll
    for (int o = 32; o >= 1; o >>= 1) v += __shfl_xor(v, o);
    return v;
}

__global__ void __launch_bounds__(NTHR)
fused_decode_kernel(const unsigned char* __restrict__ xin, const unsigned char* __restrict__ kc, const unsigned char* __restrict__ vc,
                    const unsigned char* __restrict__ wq, const unsigned char* __restrict__ wo,
                    const unsigned char* __restrict__ wg, const unsigned char* __restrict__ wu, const unsigned char* __restrict__ wd,
                    const unsigned char* __restrict__ gatew, unsigned char* __restrict__ out, unsigned char* __restrict__ ws,
                    int pace, int tstart, int depth) {
    extern __shared__ __attribute__((aligned(16))) unsigned char smem[];
    LAS unsigned char* L = (LAS unsigned char*)smem;
    LAS unsigned* ctl = (LAS unsigned*)(L + CTL_OFF);
    asm volatile(".Lfdk_begin:");
    const unsigned long long t0 = rt();
    asm volatile("" ::"s"(xin), "s"(kc), "s"(vc), "s"(wq), "s"(wo), "s"(wg), "s"(wu), "s"(wd), "s"(gatew), "s"(out), "s"(ws), "s"(pace), "s"(tstart), "s"(depth));
    const int w = __builtin_amdgcn_readfirstlane((int)threadIdx.x >> 6);
    const int h = (blockIdx.x & 7) * 4 + ((blockIdx.x >> 3) & 3), b = blockIdx.x >> 5;
    const int fs = h, bh = b * NH + h;
    STAMP(0);
    const unsigned myslot_a = (unsigned)(size_t)L + w * SLOT;
    const LAS unsigned char* myslot = L + w * SLOT;
    const unsigned rot = (unsigned)(ROTK) + (w ^ 4);
    auto toff = [&](int t) { return (size_t)(((8u * t + rot) & 63u) * SLOT); };
    int lane, c, g;
    unsigned vk0, vk1, vk2, vk3;
    Sel sel;
    const unsigned char* kb;
    auto setup = [&]() {
        lane = fresh_lane(); c = lane & 15; g = lane >> 4;
        vk0 = g * 256 + ((c ^ g) << 4); vk1 = g * 256 + ((c ^ (4 + g)) << 4); vk2 = g * 256 + ((c ^ (8 + g)) << 4); vk3 = g * 256 + ((c ^ (12 + g)) << 4);
        sel = make_sel(c & 7);
        int bh2 = bh;
        asm volatile("" : "+s"(bh2));
        kb = kc + (size_t)bh2 * (SK * DH * 2);
    };
    auto code_prefetch = [&](int l) {
        unsigned long long pc;
        unsigned off, len, v = 0u;
        asm volatile("s_getpc_b64 %0\n.Lfdk_pc%=:\n\ts_mov_b32 %1, .Lfdk_pc%=-.Lfdk_begin\n\ts_mov_b32 %2, .Lfdk_end-.Lfdk_begin" : "=s"(pc), "=s"(off), "=s"(len));
        const unsigned o = (((blockIdx.x >> 3) & 31u) + 32u * (unsigned)l) * 128u;
        if (o < len) asm volatile("global_load_dword %0, %1, %2" : "+v"(v) : "v"(o), "s"(pc - off) : "memory");
        return v;
    };
    const int u = w ^ 4;
    auto take_turn = [&](int f) {
        const unsigned long long td = t0 + (unsigned)(tstart + (((f - 8) * pace) >> 4));
        while (rt() < td) __builtin_amdgcn_s_sleep(1);
        for (unsigned s = 0; s < SPIN_CAP; ++s) {
            const v2u st = *(volatile LAS v2u*)(ctl + C_ISS);
            if (rfl(st[0]) >= (unsigned)f && rfl(st[1]) + (unsigned)depth > (unsigned)f) break;
            __builtin_amdgcn_s_sleep(0);
        }
        CBAR();
    };
    auto issued = [&](int f) { if (lane == 0) *(volatile LAS unsigned*)(ctl + C_ISS) = (unsigned)f + 1u; CBAR(); };
    auto landed = [&]() { if (lane == 0) __hip_atomic_fetch_add(ctl + C_LAND, 1u, RLX_WG); CBAR(); };
    LAS float* sc = (LAS float*)(L + SC_OFF + w * 2048);
    unsigned long long epoch;

    if (__builtin_expect(w < 4, 1)) {
        v4u x8[4], wr[8];
        v2u ep;
        const unsigned l0 = threadIdx.x & 63;
        const unsigned char* qs = wq + (size_t)(128 * b + 32 * w) * (NH * DH * 2) + h * 256;
        const unsigned vq = (l0 >> 4) * 8 * (NH * DH * 2) + (l0 & 15) * 16;
#pragma unroll
        for (int t = 0; t < 8; ++t)
            asm volatile("global_load_dwordx4 %0, %1, %2" QNT : "=v"(wr[t]) : "v"(vq), "s"(qs + (size_t)t * (NH * DH * 2)) : "memory");
        __builtin_amdgcn_sched_barrier(0);
        const unsigned char* xs = xin + (128 * b + 32 * w) * 2;
        const unsigned vx = ((l0 >> 3) & 1) * (DM * 2) + (l0 >> 4) * 16;
#pragma unroll
        for (int j = 0; j < 4; ++j)
            asm volatile("global_load_dwordx4 %0, %1, %2" : "=v"(x8[j]) : "v"(vx), "s"(xs + j * (2 * DM * 2)) : "memory");
        asm volatile("global_load_dwordx2 %0, %1, %2 sc1" : "=v"(ep) : "v"(0u), "s"(ws + WS_EPOCH) : "memory");
        const unsigned char* inbox = ws + WS_QGRAN + (size_t)(h * NB + b) * NB * (DH * 8);
        unsigned warm;
        asm volatile("global_load_dword %0, %1, %2 sc1" : "=v"(warm) : "v"(l0 * 16), "s"(inbox) : "memory");
        STAMP(42);
        __builtin_amdgcn_s_barrier();
        setup();
        VMCNT(0);
        asm volatile("" : "+v"(x8[0]), "+v"(x8[1]), "+v"(x8[2]), "+v"(x8[3]), "+v"(wr[0]), "+v"(wr[1]), "+v"(wr[2]), "+v"(wr[3]),
                     "+v"(wr[4]), "+v"(wr[5]), "+v"(wr[6]), "+v"(wr[7]), "+v"(warm), "+v"(ep) :: "memory");
        STAMP(45);
        {
            const unsigned e0 = ep[0], e1 = ep[1];
            epoch = (((unsigned long long)rfl(e1) << 32) | rfl(e0)) + 1ull;
        }
        const unsigned qtag_hi = (unsigned)(epoch >> 8), qtag_lo = (unsigned)epoch & 0xffu;
#pragma unroll
        for (int j = 0; j < 4; ++j) {
            f32x4 a0 = {0.f, 0.f, 0.f, 0.f}, a1 = {0.f, 0.f, 0.f, 0.f};
#pragma unroll
            for (int k = 0; k < 4; ++k) {
                a0 = mfma(diagA(x8[j][k], sel.s[0]), wr[2 * k], a0);
                a1 = mfma(diagA(x8[j][k], sel.s[1]), wr[2 * k + 1], a1);
            }
            a0 += a1;
            *(LAS f32x4*)(L + SC_OFF + ((w * 8 + 2 * j + (g >> 1)) * 128 + 8 * c + 4 * (g & 1)) * 4) = a0;
        }
        if (w == 0 && lane == 0) *(LAS v2u*)(ctl + C_TAG) = v2u{(unsigned)epoch, (unsigned)(epoch >> 32)};
        arrive(ctl + C_QP, lane);
        wait_ge(ctl + C_QP, 4);
        STAMP(1);
        {
            const __amdgpu_buffer_rsrc_t rqg = __builtin_amdgcn_make_buffer_rsrc((void*)(ws + WS_QGRAN), 0, NH * NB * NB * DH * 8, 0x00020000);
#pragma unroll
            for (int r = 0; r < 2; ++r) {
                float s0 = 0.f, s1 = 0.f;
#pragma unroll
                for (int k = 0; k < 4; ++k) { const f32x2 v = *(const LAS f32x2*)(L + SC_OFF + ((k * 8 + 2 * w + r) * 128 + 2 * lane) * 4); s0 += v[0]; s1 += v[1]; }
                store_dev(v4u{gran(s0, qtag_lo), qtag_hi, gran(s1, qtag_lo), qtag_hi}, rqg, lane * 16, ((h * NB + 2 * w + r) * NB + b) * (DH * 8));
            }
        }
        arrive(ctl + C_QP, lane);
        STAMP(46);
        if (w == 3) {
            VMCNT(0);
            STAMP(55);
            const unsigned vo = lane * 16;
            v4u y[NB];
            for (unsigned s = 0;; ++s) {
#pragma unroll
                for (int p = 0; p < NB; ++p)
                    asm volatile("global_load_dwordx4 %0, %1, %2 offset:%3 sc1" : "=v"(y[p]) : "v"(vo), "s"(inbox + (p >> 2) * 4096), "i"((p & 3) * 1024) : "memory");
                if (s == 0) {
                    if (lane == 0) *(volatile LAS unsigned*)(ctl + C_POLL) = 1u;
                    fill(kb + toff(0), vk0, vk1, vk2, vk3, myslot_a);
                    VMCNT(16);
                } else {
                    VMCNT(0);
                }
                asm volatile("" : "+v"(y[0]), "+v"(y[1]), "+v"(y[2]), "+v"(y[3]), "+v"(y[4]), "+v"(y[5]), "+v"(y[6]), "+v"(y[7]) :: "memory");
                unsigned bad = 0u;
#pragma unroll
                for (int p = 0; p < NB; ++p) bad |= (y[p][1] ^ qtag_hi) | (y[p][3] ^ qtag_hi) | (((y[p][0] ^ qtag_lo) | (y[p][2] ^ qtag_lo)) & 0xffu);
                if (__all(bad == 0u) || s > SPIN_CAP) break;
                __builtin_amdgcn_s_sleep(POLL_SLEEP);
            }
            float s0 = 0.f, s1 = 0.f;
#pragma unroll
            for (int p = 0; p < NB; ++p) { s0 += __builtin_bit_cast(float, y[p][0] & 0xffffff00u); s1 += __builtin_bit_cast(float, y[p][2] & 0xffffff00u); }
            ((LAS unsigned*)(L + Q_OFF))[lane] = pack2(s0, s1);
            wait_ge(ctl + C_QP, 8);
            arrive(ctl + C_QR, lane);
        } else {
            if (w >= PRE) wait_ge(ctl + C_POLL, 1);
            fill(kb + toff(0), vk0, vk1, vk2, vk3, myslot_a);
        }
        STAMP(47);
        VMCNT(0);
        landed();
    } else {
        setup();
        if (w == 4 && lane < 16) ctl[lane] = lane == C_ISS ? 8u : 0u;
        unsigned pf = 0u;
        if (CODE_PF && w == 5 && lane < 16) pf = code_prefetch(lane);
        LGKM0();
        __builtin_amdgcn_s_barrier();
        fill(kb + toff(0), vk0, vk1, vk2, vk3, myslot_a);
        STAMP(43);
        asm volatile("s_waitcnt vmcnt(0)" : "+v"(pf)::"memory");
        landed();
        STAMP(2);
        wait_ge(ctl + C_QP, 4);
        const v2u e = *(volatile LAS v2u*)(ctl + C_TAG);
        const unsigned e0 = e[0], e1 = e[1];
        epoch = ((unsigned long long)rfl(e1) << 32) | rfl(e0);
    }
    const unsigned tag_hi = (unsigned)(epoch >> 8), tag_lo = (unsigned)epoch & 0xffu;
    const unsigned char* vb = vc + (size_t)bh * (SK * DH * 2);
    const __amdgpu_buffer_rsrc_t rgran = __builtin_amdgcn_make_buffer_rsrc((void*)(ws + WS_GRAN), 0, NB * NH * DM * 8, 0x00020000);
    const int gvo = (8 * c + 4 * (g & 1) + 2 * (g >> 1)) * 8, gso = bh * (DM * 8) + w * 1024;
    wait_ge(ctl + C_QR, 1);
    v4u qf[4];
#pragma unroll
    for (int s = 0; s < 4; ++s) qf[s] = *(const LAS v4u*)(L + Q_OFF + (32 * s + 8 * g) * 2);
    LGKM0();
    STAMP(3);

    float mx = -INFINITY;
    v4u wor[32];
    const __amdgpu_buffer_rsrc_t ro = __builtin_amdgcn_make_buffer_rsrc((void*)wo, 0, DM * NH * DH * 2, 0x00020000);
    const int soo = h * DH * (DM * 2) + w * 256;
#pragma unroll
    for (int t = 0; t < 8; ++t) {
        if (t > 0) { take_turn(8 * t + u); fill(kb + toff(t), vk0, vk1, vk2, vk3, myslot_a); issued(8 * t + u); }
        STAMP(8 + t);
        v4u xg[4][2], gw[16];
        if (t > 0) {
            const int l2 = fresh_lane();
            const int voo = 32 * (l2 >> 4) * (DM * 2) + (l2 & 15) * 16;
            v4u* wp = wor + 4 * (t - 1);
#pragma unroll
            for (int r = 0; r < 4; ++r) wp[r] = __builtin_amdgcn_raw_buffer_load_b128(ro, voo, soo + (4 * (t - 1) + r) * (DM * 2), WO_AUX);
            if (t == 1) {
                const __amdgpu_buffer_rsrc_t rx = __builtin_amdgcn_make_buffer_rsrc((void*)xin, 0, NB * DM * 2, 0x00020000);
                const __amdgpu_buffer_rsrc_t rw = __builtin_amdgcn_make_buffer_rsrc((void*)gatew, 0, DM * 2, 0x00020000);
                v4u xv = __builtin_amdgcn_raw_buffer_load_b128(rx, (l2 & 15) * 16, b * (DM * 2) + w * 256, 0);
                v4u gv = __builtin_amdgcn_raw_buffer_load_b128(rw, (l2 & 15) * 16, w * 256, 0);
                asm volatile("s_waitcnt vmcnt(0)" : "+v"(wp[0]), "+v"(wp[1]), "+v"(wp[2]), "+v"(wp[3]), "+v"(xv), "+v"(gv) :: "memory");
                landed();
                if (INVAL) store_dev(v4u{tag_lo ^ 1u, ~tag_hi, tag_lo ^ 1u, ~tag_hi}, rgran, gvo, gso);
                float s = 0.f;
#pragma unroll
                for (int k = 0; k < 4; ++k) s += rbf(bf_lo(xv[k]) * bf_lo(gv[k])) + rbf(bf_hi(xv[k]) * bf_hi(gv[k]));
                s = wave_sum(l2 < 16 ? s : 0.f);
                if (l2 == 0) ((LAS float*)ctl)[C_GP + w] = s;
            } else if (t == GU_T && w >= 4) {
                const int m = (w >> 1) & 1, hf = w & 1;
                const unsigned char* gs = (m ? wu : wg) + (size_t)(128 * b + 64 * hf) * (FF * 2) + fs * (FS * 2);
                const unsigned vog = 16 * (l2 >> 4) * (FF * 2) + ((l2 & 15) < 14 ? (l2 & 15) : 13) * 16;
#pragma unroll
                for (int k = 0; k < 16; ++k)
                    asm volatile("global_load_dwordx4 %0, %1, %2" GUNT : "=v"(gw[k]) : "v"(vog), "s"(gs + (size_t)k * (FF * 2)) : "memory");
                const unsigned char* xs = xin + (128 * b + 64 * hf) * 2;
                const unsigned vox = ((l2 & 15) >> 3) * (DM * 2) + (l2 >> 4) * 32;
#pragma unroll
                for (int j = 0; j < 4; ++j) {
                    asm volatile("global_load_dwordx4 %0, %1, %2" : "=v"(xg[j][0]) : "v"(vox), "s"(xs + j * (2 * DM * 2)) : "memory");
                    asm volatile("global_load_dwordx4 %0, %1, %2 offset:16" : "=v"(xg[j][1]) : "v"(vox), "s"(xs + j * (2 * DM * 2)) : "memory");
                }
                VMCNT(24);
            } else if (t == GU_T + 1 && w >= 4) {
                asm volatile("s_waitcnt vmcnt(0)" : "+v"(wp[0]), "+v"(wp[1]), "+v"(wp[2]), "+v"(wp[3]) :: "memory");
                landed();
                wait_ge(ctl + C_GU, 4);
                const __amdgpu_buffer_rsrc_t rgg = __builtin_amdgcn_make_buffer_rsrc((void*)(ws + WS_GUGRAN), 0, NH * NB * NB * 2048, 0x00020000);
#pragma unroll
                for (int r = 0; r < 2; ++r)
#pragma unroll
                    for (int q = 0; q < 2; ++q) {
                        const int p = q == 0 ? l2 : (l2 < 48 ? 64 + l2 : 111);
                        const int mm = p >= 56 ? 1 : 0, col = 2 * (p - 56 * mm), cons = 2 * (w - 4) + r;
                        const f32x2 v0 = *(const LAS f32x2*)(L + GU_OFF + (((2 * mm) * 8 + cons) * FS + col) * 4);
                        const f32x2 v1 = *(const LAS f32x2*)(L + GU_OFF + (((2 * mm + 1) * 8 + cons) * FS + col) * 4);
                        const float s0 = v0[0] + v1[0], s1 = v0[1] + v1[1];
                        if (q == 0 || l2 < 48)
                            store_dev(v4u{gran(s0, tag_lo), tag_hi, gran(s1, tag_lo), tag_hi}, rgg, p * 16, ((h * NB + cons) * NB + b) * 2048);
                    }
                arrive(ctl + C_GU, l2);
            } else if (t == 7) {
                const __amdgpu_buffer_rsrc_t rgg = __builtin_amdgcn_make_buffer_rsrc((void*)(ws + WS_GUGRAN), 0, NH * NB * NB * 2048, 0x00020000);
                const int sgg = ((h * NB + b) * NB + w) * 2048, v1o = 1024 + (l2 < 48 ? l2 : 47) * 16;
                v4u y0, y1;
                for (unsigned s = 0;; ++s) {
                    y0 = __builtin_amdgcn_raw_buffer_load_b128(rgg, l2 * 16, sgg, 16);
                    y1 = __builtin_amdgcn_raw_buffer_load_b128(rgg, v1o, sgg, 16);
                    asm volatile("s_waitcnt vmcnt(0)" : "+v"(wp[0]), "+v"(wp[1]), "+v"(wp[2]), "+v"(wp[3]), "+v"(y0), "+v"(y1) :: "memory");
                    const unsigned bad = (y0[1] ^ tag_hi) | (y0[3] ^ tag_hi) | (y1[1] ^ tag_hi) | (y1[3] ^ tag_hi) | (((y0[0] ^ tag_lo) | (y0[2] ^ tag_lo) | (y1[0] ^ tag_lo) | (y1[2] ^ tag_lo)) & 0xffu);
                    if (s == 0) landed();
                    if (__all(bad == 0u) || s > SPIN_CAP) break;
                    __builtin_amdgcn_s_sleep(1);
                }
                wait_ge(ctl + C_GU, 8);
                *(LAS v2u*)(L + GU_OFF + w * 896 + l2 * 8) = v2u{y0[0] & 0xffffff00u, y0[2] & 0xffffff00u};
                if (l2 < 48) *(LAS v2u*)(L + GU_OFF + w * 896 + 512 + l2 * 8) = v2u{y1[0] & 0xffffff00u, y1[2] & 0xffffff00u};
                arrive(ctl + C_FFN, l2);
            } else {
                asm volatile("s_waitcnt vmcnt(0)" : "+v"(wp[0]), "+v"(wp[1]), "+v"(wp[2]), "+v"(wp[3]) :: "memory");
            }
            if (t != 7 && t != 1 && !(t == GU_T + 1 && w >= 4)) landed();
        }
        STAMP(16 + t);
        f32x4 s4[4];
#pragma unroll
        for (int kg = 0; kg < 4; ++kg) {
            f32x4 a = {0.f, 0.f, 0.f, 0.f};
#pragma unroll
            for (int s = 0; s < 4; ++s) {
                const v4u kf = *(const LAS v4u*)(myslot + (16 * kg + c) * 256 + (((4 * s + g) ^ c) << 4));
                a = mfma(kf, qf[s], a);
            }
            s4[kg] = a;
        }
        f32x4 v;
#pragma unroll
        for (int i = 0; i < 4; ++i) {
            const float lo = (c & 1) ? s4[1][i] : s4[0][i], hi = (c & 1) ? s4[3][i] : s4[2][i];
            v[i] = rbf(rbf((c & 2) ? hi : lo) * 0.08838834764831845f);
            mx = fmaxf(mx, v[i]);
        }
        if (c < 4) *(LAS f32x4*)(sc + t * 64 + 16 * c + 4 * g) = v;
        __builtin_amdgcn_sched_barrier(0);
        if (t == GU_T && w >= 4) {
            v4u* wp = wor + 4 * (t - 1);
            asm volatile("s_waitcnt vmcnt(0)" : "+v"(gw[0]), "+v"(gw[1]), "+v"(gw[2]), "+v"(gw[3]), "+v"(gw[4]), "+v"(gw[5]), "+v"(gw[6]), "+v"(gw[7]),
                         "+v"(gw[8]), "+v"(gw[9]), "+v"(gw[10]), "+v"(gw[11]), "+v"(gw[12]), "+v"(gw[13]), "+v"(gw[14]), "+v"(gw[15]),
                         "+v"(xg[0][0]), "+v"(xg[0][1]), "+v"(xg[1][0]), "+v"(xg[1][1]), "+v"(xg[2][0]), "+v"(xg[2][1]), "+v"(xg[3][0]), "+v"(xg[3][1]) :: "memory");
            asm volatile("" : "+v"(wp[0]), "+v"(wp[1]), "+v"(wp[2]), "+v"(wp[3]));
            STAMP(44);
            f32x4 a[4];
#pragma unroll
            for (int j = 0; j < 4; ++j) a[j] = f32x4{0.f, 0.f, 0.f, 0.f};
#pragma unroll
            for (int k = 0; k < 16; ++k) {
#pragma unroll
                for (int j = 0; j < 4; ++j) a[j] = mfma(diagA(xg[j][k >> 3][(k >> 1) & 3], sel.s[k & 1]), gw[k], a[j]);
                PIN2(a[0], a[1]); PIN2(a[2], a[3]);
            }
            if (c < 14) {
#pragma unroll
                for (int j = 0; j < 4; ++j)
                    *(LAS f32x4*)(L + GU_OFF + ((((w - 4) * 8 + 2 * j + (g >> 1)) * FS) + 8 * c + 4 * (g & 1)) * 4) = a[j];
            }
            arrive(ctl + C_GU, lane);
            STAMP(56);
        }
    }
    STAMP(4);
    float lsum;
    mx = wave_max(mx);
    {
        const f32x4 sa = *(const LAS f32x4*)(sc + 8 * lane), sb = *(const LAS f32x4*)(sc + 8 * lane + 4);
        float e[8];
#pragma unroll
        for (int i = 0; i < 4; ++i) { e[i] = __expf(sa[i] - mx); e[4 + i] = __expf(sb[i] - mx); }
        lsum = wave_sum(((e[0] + e[1]) + (e[2] + e[3])) + ((e[4] + e[5]) + (e[6] + e[7])));
        v4u pk;
#pragma unroll
        for (int i = 0; i < 4; ++i) pk[i] = pack2(e[2 * i], e[2 * i + 1]);
        LGKM0();
        *(LAS v4u*)((LAS unsigned char*)sc + 16 * lane) = pk;
    }
    STAMP(5);

    f32x4 acco = {0.f, 0.f, 0.f, 0.f}, acco1 = {0.f, 0.f, 0.f, 0.f}, accb = {0.f, 0.f, 0.f, 0.f};
    {
        const unsigned vlin = lane * 16;
        {
            take_turn(64 + u);
            fill(vb + toff(0), vlin, vlin, vlin, vlin, myslot_a);
            issued(64 + u);
            STAMP(24);
            const int voo = 32 * g * (DM * 2) + c * 16;
#pragma unroll
            for (int r = 0; r < 4; ++r) wor[28 + r] = __builtin_amdgcn_raw_buffer_load_b128(ro, voo, soo + (28 + r) * (DM * 2), WO_AUX);
            asm volatile("s_waitcnt vmcnt(0)" : "+v"(wor[28]), "+v"(wor[29]), "+v"(wor[30]), "+v"(wor[31]) :: "memory");
            landed();
            STAMP(32);
            slot_rows64(myslot, (const LAS unsigned char*)sc, sel, c, g, acco, acco1);
        }
        const __amdgpu_buffer_rsrc_t rd = __builtin_amdgcn_make_buffer_rsrc((void*)wd, 0, FF * DM * 2, 0x00020000);
        const int vod = 28 * g * (DM * 2) + c * 16;
#pragma unroll 1
        for (int t = 1; t < 8; ++t) {
            take_turn(64 + 8 * t + u);
            fill(vb + toff(t), vlin, vlin, vlin, vlin, myslot_a);
            issued(64 + 8 * t + u);
            STAMP(24 + t);
            const int tt = t - 1;
            const int sod = (fs * FS + 4 * tt) * (DM * 2) + w * 256;
            v4u dw[4];
#pragma unroll
            for (int r = 0; r < 4; ++r) dw[r] = __builtin_amdgcn_raw_buffer_load_b128(rd, vod, sod + r * (DM * 2), WD_AUX);
            if (t == 1) {
                wait_ge(ctl + C_FFN, NW);
                const int l2 = fresh_lane();
                if (l2 < FS / 2) {
                    float g0 = 0.f, g1 = 0.f, u0 = 0.f, u1 = 0.f;
#pragma unroll
                    for (int ww = 0; ww < NW; ++ww) {
                        const f32x2 a = *(const LAS f32x2*)(L + GU_OFF + ww * 896 + 8 * l2), bb = *(const LAS f32x2*)(L + GU_OFF + ww * 896 + 448 + 8 * l2);
                        g0 += a[0]; g1 += a[1]; u0 += bb[0]; u1 += bb[1];
                    }
                    ((LAS unsigned*)(L + HH_OFF))[l2] = pack2(gelu_tanh(rbf(g0)) * rbf(u0), gelu_tanh(rbf(g1)) * rbf(u1));
                }
                LGKM0();
            }
            unsigned pf = 0u;
            if (CODE_PF2 && t == CODE_PF2 && w == 0 && lane < 16) pf = code_prefetch(lane);
            const v2u hs = *(const LAS v2u*)(L + HH_OFF + (28 * g + 4 * tt) * 2);
            asm volatile("s_waitcnt vmcnt(0)" : "+v"(dw[0]), "+v"(dw[1]), "+v"(dw[2]), "+v"(dw[3]), "+v"(pf) :: "memory");
            landed();
            STAMP(32 + t);
            slot_rows64(myslot, (const LAS unsigned char*)sc + t * 128, sel, c, g, acco, acco1);
#pragma unroll
            for (int r = 0; r < 4; ++r) accb = mfma(diagA(hs[r >> 1], sel.s[r & 1]), dw[r], accb);
        }
    }
    STAMP(6);
    acco += acco1;
    if (g < 2) *(LAS f32x4*)((LAS unsigned char*)sc + 1024 + (8 * c + 4 * g) * 4) = acco;
    if (lane == 0) { ((LAS float*)ctl)[C_WMAX + w] = mx; ((LAS float*)ctl)[C_WSUM + w] = lsum; }
    arrive(ctl + C_O, lane);
    wait_ge(ctl + C_O, NW);
    v4u of[4];
    {
        float m[NW], M = -INFINITY;
#pragma unroll
        for (int ww = 0; ww < NW; ++ww) { m[ww] = ((LAS float*)ctl)[C_WMAX + ww]; M = fmaxf(M, m[ww]); }
        float Ls = 0.f, s0 = 0.f, s1 = 0.f;
#pragma unroll
        for (int ww = 0; ww < NW; ++ww) {
            const float f = __expf(m[ww] - M);
            Ls += f * ((LAS float*)ctl)[C_WSUM + ww];
            const f32x2 v = *(const LAS f32x2*)(L + SC_OFF + ww * 2048 + 1024 + 8 * lane);
            s0 += f * v[0]; s1 += f * v[1];
        }
        const float inv = 1.0f / Ls;
        ((LAS unsigned*)(L + O_OFF))[lane] = pack2(s0 * inv, s1 * inv);
        LGKM0();
#pragma unroll
        for (int k = 0; k < 4; ++k) of[k] = *(const LAS v4u*)(L + O_OFF + 64 * g + 16 * k);
    }
    STAMP(7);
    f32x4 acca = {0.f, 0.f, 0.f, 0.f}, acca1 = {0.f, 0.f, 0.f, 0.f};
#pragma unroll
    for (int t = 0; t < 32; t += 2) {
        const unsigned S = of[t >> 3][(t >> 1) & 3];
        acca = mfma(diagA(S, sel.s[0]), wor[t], acca);
        acca1 = mfma(diagA(S, sel.s[1]), wor[t + 1], acca1);
    }
    acca += acca1;
    STAMP(40);
    {
        float z = 0.f;
#pragma unroll
        for (int ww = 0; ww < NW; ++ww) z += ((LAS float*)ctl)[C_GP + ww];
        z = rbf(z);
        const float gate = rbf(1.0f / (1.0f + __expf(-z)));
        const float omg = rbf(1.0f - gate);
        {
            const float r0 = gate * (g < 2 ? acca[0] : acca[2]) + omg * (g < 2 ? accb[0] : accb[2]);
            const float r1 = gate * (g < 2 ? acca[1] : acca[3]) + omg * (g < 2 ? accb[1] : accb[3]);
            store_dev(v4u{gran(r0, tag_lo), tag_hi, gran(r1, tag_lo), tag_hi}, rgran, gvo, gso);
        }
        const int l2 = fresh_lane();
        const int vo = ((l2 >> 4) * DM + 2 * (l2 & 15)) * 8, so = ((b * NH + 4 * w) * DM + 32 * h) * 8;
        v4u y;
        for (unsigned s = 0;; ++s) {
            y = __builtin_amdgcn_raw_buffer_load_b128(rgran, vo, so, 16);
            const unsigned bad = (y[1] ^ tag_hi) | (y[3] ^ tag_hi) | (((y[0] ^ tag_lo) | (y[2] ^ tag_lo)) & 0xffu);
            if (__all(bad == 0u) || s > SPIN_CAP) break;
            __builtin_amdgcn_s_sleep(1);
        }
        STAMP(41);
        float v0 = __builtin_bit_cast(float, y[0] & 0xffffff00u), v1 = __builtin_bit_cast(float, y[2] & 0xffffff00u);
        v0 += __shfl_xor(v0, 16); v1 += __shfl_xor(v1, 16);
        v0 += __shfl_xor(v0, 32); v1 += __shfl_xor(v1, 32);
        if (l2 < 16) *(LAS f32x2*)(L + GU_OFF + w * 128 + l2 * 8) = f32x2{v0, v1};
        LGKM0();
        unsigned old = 0u;
        if (l2 == 0) old = __hip_atomic_fetch_add(ctl + C_OUT, 1u, RLX_WG);
        if (rfl(old) == NW - 1) {
            CBAR();
            if (l2 < 16) {
                float s0 = 0.f, s1 = 0.f;
#pragma unroll
                for (int ww = 0; ww < NW; ++ww) { const f32x2 v = *(const LAS f32x2*)(L + GU_OFF + ww * 128 + l2 * 8); s0 += v[0]; s1 += v[1]; }
                __hip_atomic_store((gu32*)(out + ((size_t)b * DM + 32 * h + 2 * l2) * 2), pack2(s0, s1), __ATOMIC_RELAXED, __HIP_MEMORY_SCOPE_SYSTEM);
            }
            if (bh == 0 && l2 == 0) __hip_atomic_store((gu64*)(ws + WS_EPOCH), epoch, RLX_AGENT);
        }
        STAMP(57);
    }
    SDUMP();
    asm volatile(".Lfdk_end:");
}

}

extern "C" void kernel_launch(void* const* d_in, const int* in_sizes, int n_in, void* d_out, int out_size, void* d_ws, size_t ws_size,
                              hipStream_t stream) {
    (void)in_sizes; (void)n_in; (void)out_size; (void)ws_size;
    (void)hipFuncSetAttribute((const void*)fused_decode_kernel, hipFuncAttributeMaxDynamicSharedMemorySize, LDS_BYTES);
    int pace = PACE, tstart = TSTART, depth = DEPTH;
    fused_decode_kernel<<<dim3(NB * NH), dim3(NTHR), LDS_BYTES, stream>>>(
        (const unsigned char*)d_in[0], (const unsigned char*)d_in[1], (const unsigned char*)d_in[2], (const unsigned char*)d_in[3],
        (const unsigned char*)d_in[4], (const unsigned char*)d_in[6], (const unsigned char*)d_in[7],
        (const unsigned char*)d_in[8], (const unsigned char*)d_in[9], (unsigned char*)d_out, (unsigned char*)d_ws, pace, tstart, depth);
}
```

```cpp
#define CODE_PF3 4
#include <hip/hip_runtime.h>
#include <hip/hip_bf16.h>

#define GAS __attribute__((address_space(1)))
#define LAS __attribute__((address_space(3)))
typedef unsigned v4u __attribute__((ext_vector_type(4)));
typedef unsigned v2u __attribute__((ext_vector_type(2)));
typedef unsigned v16u __attribute__((ext_vector_type(16)));
typedef float f32x4 __attribute__((ext_vector_type(4)));
typedef float f32x2 __attribute__((ext_vector_type(2)));
typedef short bf16x8 __attribute__((ext_vector_type(8)));
typedef GAS unsigned gu32;
typedef GAS unsigned long long gu64;
#define RLX_AGENT __ATOMIC_RELAXED, __HIP_MEMORY_SCOPE_AGENT
#define RLX_WG __ATOMIC_RELAXED, __HIP_MEMORY_SCOPE_WORKGROUP

namespace {

constexpr int NB = 8, DM = 1024, NH = 32, SK = 4096, DH = 128, FF = 3584;
constexpr int FS = FF / NH;
constexpr int NW = 8, NTHR = 64 * NW;
constexpr int SLOT = 16384;
#define GUNT " nt"
#define ROTK 0
#define PRE 1
#define GU_T 2
#define WO_AUX 0
#define WD_AUX 0
#define POLL_SLEEP 1
#define QNT " nt"
#define CODE_PF 1
#define CODE_PF2 5
#define INVAL 0
#define PACE (16 * 61)
#define TSTART 600
#define DEPTH 4

constexpr int SC_OFF = NW * SLOT;
constexpr int GU_OFF = SC_OFF + NW * 2048;
constexpr int Q_OFF = GU_OFF + 14336;
constexpr int O_OFF = Q_OFF + 256;
constexpr int HH_OFF = O_OFF + 256;
constexpr int CTL_OFF = HH_OFF + 256;
constexpr int LDS_BYTES = CTL_OFF + 256;
constexpr int C_ISS = 0, C_LAND = 1, C_FFN = 2, C_O = 3, C_QP = 4, C_QR = 5, C_GU = 6, C_OUT = 7, C_TAG = 8, C_POLL = 10, C_WMAX = 16, C_WSUM = 24, C_GP = 32;
static_assert(LDS_BYTES <= 160 * 1024, "LDS");

constexpr size_t WS_EPOCH = 0, WS_GRAN = 4096, WS_QGRAN = WS_GRAN + (size_t)NB * NH * DM * 8, WS_GUGRAN = WS_QGRAN + (size_t)NH * NB * NB * DH * 8;

constexpr unsigned SPIN_CAP = 1u << 22;
#define STAMP(i) do {} while (0)
#define SDUMP() do {} while (0)

__device__ __forceinline__ f32x4 mfma(v4u a, v4u b, f32x4 c) {
    return __builtin_amdgcn_mfma_f32_16x16x32_bf16(__builtin_bit_cast(bf16x8, a), __builtin_bit_cast(bf16x8, b), c, 0, 0, 0);
}
__device__ __forceinline__ float bf_lo(unsigned u) { return __builtin_bit_cast(float, u << 16); }
__device__ __forceinline__ float bf_hi(unsigned u) { return __builtin_bit_cast(float, u & 0xffff0000u); }
__device__ __forceinline__ float rbf(float f) { return (float)(__bf16)f; }
__device__ __forceinline__ unsigned bf_bits(float f) { return (unsigned)__builtin_bit_cast(unsigned short, (__bf16)f); }
__device__ __forceinline__ unsigned pack2(float lo, float hi) { return bf_bits(lo) | (bf_bits(hi) << 16); }
__device__ __forceinline__ unsigned gran(float f, unsigned tag_lo) { return ((__builtin_bit_cast(unsigned, f) + 0x80u) & 0xffffff00u) | tag_lo; }
__device__ __forceinline__ void store_dev(v4u d, __amdgpu_buffer_rsrc_t r, int vo, int so) {
    __builtin_amdgcn_raw_buffer_store_b128(d, r, vo, so, 16);
    asm volatile("s_nop 1" ::"v"(d) : "memory");
}
__device__ __forceinline__ unsigned long long rt() { return __builtin_amdgcn_s_memrealtime(); }
__device__ __forceinline__ unsigned rfl(unsigned v) { return (unsigned)__builtin_amdgcn_readfirstlane((int)v); }
#define LGKM0() asm volatile("s_waitcnt lgkmcnt(0)" ::: "memory")
#define VMCNT(n) asm volatile("s_waitcnt vmcnt(%0)" ::"i"(n) : "memory")
#define CBAR() asm volatile("" ::: "memory")
#define PIN2(a, b) do { asm volatile("" : "+v"(a), "+v"(b)); __builtin_amdgcn_sched_barrier(0); } while (0)

__device__ __forceinline__ void glds4(const void* sbase, unsigned v0, unsigned v1, unsigned v2, unsigned v3, unsigned lds) {
    unsigned keep;
    asm volatile("s_mov_b32 %0, m0\n\ts_mov_b32 m0, %6\n\ts_nop 0\n\t"
                 "global_load_lds_dwordx4 %1, %5 nt\n\t"
                 "global_load_lds_dwordx4 %2, %5 offset:1024 nt\n\t"
                 "global_load_lds_dwordx4 %3, %5 offset:2048 nt\n\t"
                 "global_load_lds_dwordx4 %4, %5 offset:3072 nt\n\t"
                 "s_mov_b32 m0, %0"
                 : "=&s"(keep) : "v"(v0), "v"(v1), "v"(v2), "v"(v3), "s"(sbase), "s"(lds) : "memory");
}
__device__ __forceinline__ void fill(const unsigned char* src, unsigned v0, unsigned v1, unsigned v2, unsigned v3, unsigned lds) {
    LGKM0();
#pragma unroll
    for (int q = 0; q < 4; ++q) glds4(src + q * 4096, v0, v1, v2, v3, lds + q * 4096);
}

__device__ __forceinline__ void wait_ge(LAS unsigned* w, unsigned need) {
    for (unsigned s = 0; rfl(*(volatile LAS unsigned*)w) < need && s < SPIN_CAP; ++s) __builtin_amdgcn_s_sleep(0);
    CBAR();
}
__device__ __forceinline__ void arrive(LAS unsigned* w, int lane) {
    LGKM0();
    if (lane == 0) __hip_atomic_fetch_add(w, 1u, RLX_WG);
    CBAR();
}

struct Sel { unsigned s[2][4]; };
__device__ __forceinline__ v4u diagA(unsigned S, const unsigned (&sel)[4]) {
    return v4u{__builtin_amdgcn_perm(0u, S, sel[0]), __builtin_amdgcn_perm(0u, S, sel[1]), __builtin_amdgcn_perm(0u, S, sel[2]), __builtin_amdgcn_perm(0u, S, sel[3])};
}
__device__ __forceinline__ int fresh_lane() {
    int l;
    asm volatile("v_mbcnt_lo_u32_b32 %0, -1, 0\n\tv_mbcnt_hi_u32_b32 %0, -1, %0" : "=v"(l));
    __builtin_assume((unsigned)l < 64u);
    return l;
}
__device__ __forceinline__ Sel make_sel(int c) {
    Sel sel;
    const int dm = (c & 7) >> 1, hf = c & 1;
#pragma unroll
    for (int par = 0; par < 2; ++par)
#pragma unroll
        for (int d = 0; d < 4; ++d) {
            const unsigned lohi = (unsigned)(2 * par) | ((unsigned)(2 * par + 1) << 8);
            const unsigned v = hf ? (0x0c0cu | (lohi << 16)) : (0x0c0c0000u | lohi);
            sel.s[par][d] = (c < 8 && d == dm) ? v : 0x0c0c0c0cu;
        }
    return sel;
}

__device__ __forceinline__ void slot_rows64(const LAS unsigned char* slot, const LAS unsigned char* vec, const Sel& sel, int c, int g,
                                            f32x4& acc, f32x4& a1) {
    const LAS unsigned char* base = slot + g * 4096 + c * 16;
#pragma unroll
    for (int hf = 0; hf < 2; ++hf) {
        const v4u s = *(const LAS v4u*)(vec + g * 32 + hf * 16);
        v4u bb[8];
#pragma unroll
        for (int t = 0; t < 8; ++t) bb[t] = *(const LAS v4u*)(base + (8 * hf + t) * 256);
        __builtin_amdgcn_sched_barrier(0);
#pragma unroll
        for (int u = 0; u < 4; ++u) {
            acc = mfma(diagA(s[u], sel.s[0]), bb[2 * u], acc);
            a1 = mfma(diagA(s[u], sel.s[1]), bb[2 * u + 1], a1);
            PIN2(acc, a1);
        }
    }
}

__device__ __forceinline__ float gelu_tanh(float x) {
    const float y = 0.7978845608028654f * (x + 0.044715f * x * x * x);
    return rbf(x / (1.0f + __expf(-2.0f * y)));
}
__device__ __forceinline__ float wave_max(float v) {
#pragma unroll
    for (int o = 32; o >= 1; o >>= 1) v = fmaxf(v, __shfl_xor(v, o));
    return v;
}
__device__ __forceinline__ float wave_sum(float v) {
#pragma unroll
    for (int o = 32; o >= 1; o >>= 1) v += __shfl_xor(v, o);
    return v;
}

__global__ void __launch_bounds__(NTHR)
fused_decode_kernel(const unsigned char* a_x, const unsigned char* a_k, const unsigned char* a_v, const unsigned char* a_wq,
                    const unsigned char* a_wo, const unsigned char* a_wg, const unsigned char* a_wu, const unsigned char* a_wd,
                    const unsigned char* a_gw, unsigned char* a_out, unsigned char* a_ws
                    ) {
    constexpr int pace = PACE, tstart = TSTART, depth = DEPTH;
    extern __shared__ __attribute__((aligned(16))) unsigned char smem[];
    LAS unsigned char* L = (LAS unsigned char*)smem;
    LAS unsigned* ctl = (LAS unsigned*)(L + CTL_OFF);
    asm volatile(".Lfdk_begin:");
    v16u ka0;
    v4u ka1;
    v2u ka2;
    {
        const auto kp = __builtin_amdgcn_kernarg_segment_ptr();
        asm volatile("s_load_dwordx16 %0, %3, 0x0\n\ts_load_dwordx4 %1, %3, 0x40\n\ts_load_dwordx2 %2, %3, 0x50"
                     : "=&s"(ka0), "=&s"(ka1), "=&s"(ka2) : "s"(kp));
    }
    const unsigned long long t0 = rt();
    const unsigned char *xin, *kc, *vc, *wq, *wo, *wg, *wu, *wd, *gatew;
    unsigned char *out, *ws;
    auto args_ready = [&]() {
        asm volatile("s_waitcnt lgkmcnt(0)" : "+s"(ka0), "+s"(ka1), "+s"(ka2));
        auto ptr = [](unsigned lo, unsigned hi) { return (unsigned char*)(((unsigned long long)hi << 32) | lo); };
        xin = ptr(ka0[0], ka0[1]); kc = ptr(ka0[2], ka0[3]); vc = ptr(ka0[4], ka0[5]); wq = ptr(ka0[6], ka0[7]);
        wo = ptr(ka0[8], ka0[9]); wg = ptr(ka0[10], ka0[11]); wu = ptr(ka0[12], ka0[13]); wd = ptr(ka0[14], ka0[15]);
        gatew = ptr(ka1[0], ka1[1]); out = ptr(ka1[2], ka1[3]); ws = ptr(ka2[0], ka2[1]);
    };
    const int w = __builtin_amdgcn_readfirstlane((int)threadIdx.x >> 6);
    const int h = (blockIdx.x & 7) * 4 + ((blockIdx.x >> 3) & 3), b = blockIdx.x >> 5;
    const int fs = h, bh = b * NH + h;
    STAMP(0);
    const unsigned myslot_a = (unsigned)(size_t)L + w * SLOT;
    const LAS unsigned char* myslot = L + w * SLOT;
    const unsigned rot = (unsigned)(ROTK) + (w ^ 4);
    auto toff = [&](int t) { return (size_t)(((8u * t + rot) & 63u) * SLOT); };
    int lane, c, g;
    unsigned vk0, vk1, vk2, vk3;
    Sel sel;
    const unsigned char* kb;
    auto setup = [&]() {
        lane = fresh_lane(); c = lane & 15; g = lane >> 4;
        vk0 = g * 256 + ((c ^ g) << 4); vk1 = g * 256 + ((c ^ (4 + g)) << 4); vk2 = g * 256 + ((c ^ (8 + g)) << 4); vk3 = g * 256 + ((c ^ (12 + g)) << 4);
        sel = make_sel(c & 7);
    };
    auto code_prefetch = [&](int l) {
        unsigned long long pc;
        unsigned off, len, v = 0u;
        asm volatile("s_getpc_b64 %0\n.Lfdk_pc%=:\n\ts_mov_b32 %1, .Lfdk_pc%=-.Lfdk_begin\n\ts_mov_b32 %2, .Lfdk_end-.Lfdk_begin" : "=s"(pc), "=s"(off), "=s"(len));
        const unsigned o = (((blockIdx.x >> 3) & 31u) + 32u * (unsigned)l) * 128u;
        if (o < len) asm volatile("global_load_dword %0, %1, %2" : "+v"(v) : "v"(o), "s"(pc - off) : "memory");
        return v;
    };
    unsigned pf0 = 0u;
    if (CODE_PF && w == 5 && (threadIdx.x & 63) < 16) pf0 = code_prefetch(threadIdx.x & 63);
    const int u = w ^ 4;
    auto take_turn = [&](int f) {
        const unsigned long long td = t0 + (unsigned)(tstart + (((f - 8) * pace) >> 4));
        while (rt() < td) __builtin_amdgcn_s_sleep(1);
        for (unsigned s = 0; s < SPIN_CAP; ++s) {
            const v2u st = *(volatile LAS v2u*)(ctl + C_ISS);
            if (rfl(st[0]) >= (unsigned)f && rfl(st[1]) + (unsigned)depth > (unsigned)f) break;
            __builtin_amdgcn_s_sleep(0);
        }
        CBAR();
    };
    auto issued = [&](int f) { if (lane == 0) *(volatile LAS unsigned*)(ctl + C_ISS) = (unsigned)f + 1u; CBAR(); };
    auto landed = [&]() { if (lane == 0) __hip_atomic_fetch_add(ctl + C_LAND, 1u, RLX_WG); CBAR(); };
    LAS float* sc = (LAS float*)(L + SC_OFF + w * 2048);
    unsigned long long epoch;

    setup();
    unsigned vq[8], vx[4];
#pragma unroll
    for (int t = 0; t < 8; ++t) vq[t] = (g * 8 + t) * (NH * DH * 2) + c * 16;
#pragma unroll
    for (int j = 0; j < 4; ++j) vx[j] = (2 * j + (c >> 3)) * (DM * 2) + g * 16;
    unsigned qoff = (unsigned)(128 * b + 32 * w) * (NH * DH * 2) + h * 256, xoff = (128 * b + 32 * w) * 2;
    unsigned ioff = WS_QGRAN + (h * NB + b) * NB * (DH * 8), vi = lane * 16, vz = 0u;
    unsigned koff = (unsigned)bh * (SK * DH * 2) + (unsigned)toff(0);
    if (w == 4 && lane < 16) ctl[lane] = lane == C_ISS ? 8u : 0u;
    asm volatile("" : "+v"(vq[0]), "+v"(vq[1]), "+v"(vq[2]), "+v"(vq[3]), "+v"(vq[4]), "+v"(vq[5]), "+v"(vq[6]), "+v"(vq[7]),
                 "+v"(vx[0]), "+v"(vx[1]), "+v"(vx[2]), "+v"(vx[3]), "+v"(vi), "+v"(vz), "+v"(vk0), "+v"(vk1), "+v"(vk2), "+v"(vk3));
    asm volatile("" : "+s"(qoff), "+s"(xoff), "+s"(ioff), "+s"(koff));
    asm volatile("" : "+v"(sel.s[0][0]), "+v"(sel.s[0][1]), "+v"(sel.s[0][2]), "+v"(sel.s[0][3]), "+v"(sel.s[1][0]), "+v"(sel.s[1][1]), "+v"(sel.s[1][2]), "+v"(sel.s[1][3]));
    __builtin_amdgcn_sched_barrier(0);
    args_ready();
    __builtin_amdgcn_sched_barrier(0);
    if (__builtin_expect(w < 4, 1)) {
        v4u x8[4], wr[8];
        v2u ep;
        const unsigned char* qs = wq + qoff;
#pragma unroll
        for (int t = 0; t < 8; ++t)
            asm volatile("global_load_dwordx4 %0, %1, %2" QNT : "=v"(wr[t]) : "v"(vq[t]), "s"(qs) : "memory");
        const unsigned char* xs = xin + xoff;
#pragma unroll
        for (int j = 0; j < 4; ++j)
            asm volatile("global_load_dwordx4 %0, %1, %2" : "=v"(x8[j]) : "v"(vx[j]), "s"(xs) : "memory");
        asm volatile("global_load_dwordx2 %0, %1, %2 sc1" : "=v"(ep) : "v"(vz), "s"(ws + WS_EPOCH) : "memory");
        const unsigned char* inbox = ws + ioff;
        unsigned warm;
        asm volatile("global_load_dword %0, %1, %2 sc1" : "=v"(warm) : "v"(vi), "s"(inbox) : "memory");
        STAMP(42);
        __builtin_amdgcn_s_barrier();
        kb = kc + (size_t)bh * (SK * DH * 2);
        VMCNT(0);
        asm volatile("" : "+v"(x8[0]), "+v"(x8[1]), "+v"(x8[2]), "+v"(x8[3]), "+v"(wr[0]), "+v"(wr[1]), "+v"(wr[2]), "+v"(wr[3]),
                     "+v"(wr[4]), "+v"(wr[5]), "+v"(wr[6]), "+v"(wr[7]), "+v"(warm), "+v"(ep) :: "memory");
        STAMP(45);
        {
            const unsigned e0 = ep[0], e1 = ep[1];
            epoch = (((unsigned long long)rfl(e1) << 32) | rfl(e0)) + 1ull;
        }
        const unsigned qtag_hi = (unsigned)(epoch >> 8), qtag_lo = (unsigned)epoch & 0xffu;
#pragma unroll
        for (int j = 0; j < 4; ++j) {
            f32x4 a0 = {0.f, 0.f, 0.f, 0.f}, a1 = {0.f, 0.f, 0.f, 0.f};
#pragma unroll
            for (int k = 0; k < 4; ++k) {
                a0 = mfma(diagA(x8[j][k], sel.s[0]), wr[2 * k], a0);
                a1 = mfma(diagA(x8[j][k], sel.s[1]), wr[2 * k + 1], a1);
            }
            a0 += a1;
            *(LAS f32x4*)(L + SC_OFF + ((w * 8 + 2 * j + (g >> 1)) * 128 + 8 * c + 4 * (g & 1)) * 4) = a0;
        }
        if (w == 0 && lane == 0) *(LAS v2u*)(ctl + C_TAG) = v2u{(unsigned)epoch, (unsigned)(epoch >> 32)};
        arrive(ctl + C_QP, lane);
        wait_ge(ctl + C_QP, 4);
        STAMP(1);
        {
            const __amdgpu_buffer_rsrc_t rqg = __builtin_amdgcn_make_buffer_rsrc((void*)(ws + WS_QGRAN), 0, NH * NB * NB * DH * 8, 0x00020000);
#pragma unroll
            for (int r = 0; r < 2; ++r) {
                float s0 = 0.f, s1 = 0.f;
#pragma unroll
                for (int k = 0; k < 4; ++k) { const f32x2 v = *(const LAS f32x2*)(L + SC_OFF + ((k * 8 + 2 * w + r) * 128 + 2 * lane) * 4); s0 += v[0]; s1 += v[1]; }
                store_dev(v4u{gran(s0, qtag_lo), qtag_hi, gran(s1, qtag_lo), qtag_hi}, rqg, lane * 16, ((h * NB + 2 * w + r) * NB + b) * (DH * 8));
            }
        }
        arrive(ctl + C_QP, lane);
        STAMP(46);
        if (w == 3) {
            VMCNT(0);
            STAMP(55);
            const unsigned vo = lane * 16;
            v4u y[NB];
            for (unsigned s = 0;; ++s) {
#pragma unroll
                for (int p = 0; p < NB; ++p)
                    asm volatile("global_load_dwordx4 %0, %1, %2 offset:%3 sc1" : "=v"(y[p]) : "v"(vo), "s"(inbox + (p >> 2) * 4096), "i"((p & 3) * 1024) : "memory");
                if (s == 0) {
                    if (lane == 0) *(volatile LAS unsigned*)(ctl + C_POLL) = 1u;
                    fill(kb + toff(0), vk0, vk1, vk2, vk3, myslot_a);
                    VMCNT(16);
                } else {
                    VMCNT(0);
                }
                asm volatile("" : "+v"(y[0]), "+v"(y[1]), "+v"(y[2]), "+v"(y[3]), "+v"(y[4]), "+v"(y[5]), "+v"(y[6]), "+v"(y[7]) :: "memory");
                unsigned bad = 0u;
#pragma unroll
                for (int p = 0; p < NB; ++p) bad |= (y[p][1] ^ qtag_hi) | (y[p][3] ^ qtag_hi) | (((y[p][0] ^ qtag_lo) | (y[p][2] ^ qtag_lo)) & 0xffu);
                if (__all(bad == 0u) || s > SPIN_CAP) break;
                __builtin_amdgcn_s_sleep(POLL_SLEEP);
            }
            float s0 = 0.f, s1 = 0.f;
#pragma unroll
            for (int p = 0; p < NB; ++p) { s0 += __builtin_bit_cast(float, y[p][0] & 0xffffff00u); s1 += __builtin_bit_cast(float, y[p][2] & 0xffffff00u); }
            ((LAS unsigned*)(L + Q_OFF))[lane] = pack2(s0, s1);
            wait_ge(ctl + C_QP, 8);
            arrive(ctl + C_QR, lane);
        } else {
            if (w >= PRE) wait_ge(ctl + C_POLL, 1);
            fill(kb + toff(0), vk0, vk1, vk2, vk3, myslot_a);
        }
        STAMP(47);
        VMCNT(0);
        landed();
    } else {
        kb = kc + (size_t)bh * (SK * DH * 2);
        const unsigned char* k0 = kc + koff;
        __builtin_amdgcn_s_barrier();
        fill(k0, vk0, vk1, vk2, vk3, myslot_a);
        STAMP(43);
        asm volatile("s_waitcnt vmcnt(0)" : "+v"(pf0)::"memory");
        landed();
        STAMP(2);
        wait_ge(ctl + C_QP, 4);
        const v2u e = *(volatile LAS v2u*)(ctl + C_TAG);
        const unsigned e0 = e[0], e1 = e[1];
        epoch = ((unsigned long long)rfl(e1) << 32) | rfl(e0);
    }
    const unsigned tag_hi = (unsigned)(epoch >> 8), tag_lo = (unsigned)epoch & 0xffu;
    const unsigned char* vb = vc + (size_t)bh * (SK * DH * 2);
    const __amdgpu_buffer_rsrc_t rgran = __builtin_amdgcn_make_buffer_rsrc((void*)(ws + WS_GRAN), 0, NB * NH * DM * 8, 0x00020000);
    const int gvo = (8 * c + 4 * (g & 1) + 2 * (g >> 1)) * 8, gso = bh * (DM * 8) + w * 1024;
    wait_ge(ctl + C_QR, 1);
    v4u qf[4];
#pragma unroll
    for (int s = 0; s < 4; ++s) qf[s] = *(const LAS v4u*)(L + Q_OFF + (32 * s + 8 * g) * 2);
    LGKM0();
    STAMP(3);

    float mx = -INFINITY;
    v4u wor[32];
    const __amdgpu_buffer_rsrc_t ro = __builtin_amdgcn_make_buffer_rsrc((void*)wo, 0, DM * NH * DH * 2, 0x00020000);
    const int soo = h * DH * (DM * 2) + w * 256;
#pragma unroll
    for (int t = 0; t < 8; ++t) {
        if (t > 0) { take_turn(8 * t + u); fill(kb + toff(t), vk0, vk1, vk2, vk3, myslot_a); issued(8 * t + u); }
        STAMP(8 + t);
        v4u xg[4][2], gw[16];
        if (t > 0) {
            const int l2 = fresh_lane();
            const int voo = 32 * (l2 >> 4) * (DM * 2) + (l2 & 15) * 16;
            v4u* wp = wor + 4 * (t - 1);
            if (CODE_PF3 && t == CODE_PF3 && w == 1 && l2 < 16) { unsigned pf = code_prefetch(l2); asm volatile("s_waitcnt vmcnt(0)" : "+v"(pf)::"memory"); }
#pragma unroll
            for (int r = 0; r < 4; ++r) wp[r] = __builtin_amdgcn_raw_buffer_load_b128(ro, voo, soo + (4 * (t - 1) + r) * (DM * 2), WO_AUX);
            if (t == 1) {
                const __amdgpu_buffer_rsrc_t rx = __builtin_amdgcn_make_buffer_rsrc((void*)xin, 0, NB * DM * 2, 0x00020000);
                const __amdgpu_buffer_rsrc_t rw = __builtin_amdgcn_make_buffer_rsrc((void*)gatew, 0, DM * 2, 0x00020000);
                v4u xv = __builtin_amdgcn_raw_buffer_load_b128(rx, (l2 & 15) * 16, b * (DM * 2) + w * 256, 0);
                v4u gv = __builtin_amdgcn_raw_buffer_load_b128(rw, (l2 & 15) * 16, w * 256, 0);
                asm volatile("s_waitcnt vmcnt(0)" : "+v"(wp[0]), "+v"(wp[1]), "+v"(wp[2]), "+v"(wp[3]), "+v"(xv), "+v"(gv) :: "memory");
                landed();
                if (INVAL) store_dev(v4u{tag_lo ^ 1u, ~tag_hi, tag_lo ^ 1u, ~tag_hi}, rgran, gvo, gso);
                float s = 0.f;
#pragma unroll
                for (int k = 0; k < 4; ++k) s += rbf(bf_lo(xv[k]) * bf_lo(gv[k])) + rbf(bf_hi(xv[k]) * bf_hi(gv[k]));
                s = wave_sum(l2 < 16 ? s : 0.f);
                if (l2 == 0) ((LAS float*)ctl)[C_GP + w] = s;
            } else if (t == GU_T && w >= 4) {
                const int m = (w >> 1) & 1, hf = w & 1;
                const unsigned char* gs = (m ? wu : wg) + (size_t)(128 * b + 64 * hf) * (FF * 2) + fs * (FS * 2);
                const unsigned vog = 16 * (l2 >> 4) * (FF * 2) + ((l2 & 15) < 14 ? (l2 & 15) : 13) * 16;
#pragma unroll
                for (int k = 0; k < 16; ++k)
                    asm volatile("global_load_dwordx4 %0, %1, %2" GUNT : "=v"(gw[k]) : "v"(vog), "s"(gs + (size_t)k * (FF * 2)) : "memory");
                const unsigned char* xs = xin + (128 * b + 64 * hf) * 2;
                const unsigned vox = ((l2 & 15) >> 3) * (DM * 2) + (l2 >> 4) * 32;
#pragma unroll
                for (int j = 0; j < 4; ++j) {
                    asm volatile("global_load_dwordx4 %0, %1, %2" : "=v"(xg[j][0]) : "v"(vox), "s"(xs + j * (2 * DM * 2)) : "memory");
                    asm volatile("global_load_dwordx4 %0, %1, %2 offset:16" : "=v"(xg[j][1]) : "v"(vox), "s"(xs + j * (2 * DM * 2)) : "memory");
                }
                VMCNT(24);
            } else if (t == GU_T + 1 && w >= 4) {
                asm volatile("s_waitcnt vmcnt(0)" : "+v"(wp[0]), "+v"(wp[1]), "+v"(wp[2]), "+v"(wp[3]) :: "memory");
                landed();
                wait_ge(ctl + C_GU, 4);
                const __amdgpu_buffer_rsrc_t rgg = __builtin_amdgcn_make_buffer_rsrc((void*)(ws + WS_GUGRAN), 0, NH * NB * NB * 2048, 0x00020000);
#pragma unroll
                for (int r = 0; r < 2; ++r)
#pragma unroll
                    for (int q = 0; q < 2; ++q) {
                        const int p = q == 0 ? l2 : (l2 < 48 ? 64 + l2 : 111);
                        const int mm = p >= 56 ? 1 : 0, col = 2 * (p - 56 * mm), cons = 2 * (w - 4) + r;
                        const f32x2 v0 = *(const LAS f32x2*)(L + GU_OFF + (((2 * mm) * 8 + cons) * FS + col) * 4);
                        const f32x2 v1 = *(const LAS f32x2*)(L + GU_OFF + (((2 * mm + 1) * 8 + cons) * FS + col) * 4);
                        const float s0 = v0[0] + v1[0], s1 = v0[1] + v1[1];
                        if (q == 0 || l2 < 48)
                            store_dev(v4u{gran(s0, tag_lo), tag_hi, gran(s1, tag_lo), tag_hi}, rgg, p * 16, ((h * NB + cons) * NB + b) * 2048);
                    }
                arrive(ctl + C_GU, l2);
            } else if (t == 7) {
                const __amdgpu_buffer_rsrc_t rgg = __builtin_amdgcn_make_buffer_rsrc((void*)(ws + WS_GUGRAN), 0, NH * NB * NB * 2048, 0x00020000);
                const int sgg = ((h * NB + b) * NB + w) * 2048, v1o = 1024 + (l2 < 48 ? l2 : 47) * 16;
                v4u y0, y1;
                for (unsigned s = 0;; ++s) {
                    y0 = __builtin_amdgcn_raw_buffer_load_b128(rgg, l2 * 16, sgg, 16);
                    y1 = __builtin_amdgcn_raw_buffer_load_b128(rgg, v1o, sgg, 16);
                    asm volatile("s_waitcnt vmcnt(0)" : "+v"(wp[0]), "+v"(wp[1]), "+v"(wp[2]), "+v"(wp[3]), "+v"(y0), "+v"(y1) :: "memory");
                    const unsigned bad = (y0[1] ^ tag_hi) | (y0[3] ^ tag_hi) | (y1[1] ^ tag_hi) | (y1[3] ^ tag_hi) | (((y0[0] ^ tag_lo) | (y0[2] ^ tag_lo) | (y1[0] ^ tag_lo) | (y1[2] ^ tag_lo)) & 0xffu);
                    if (s == 0) landed();
                    if (__all(bad == 0u) || s > SPIN_CAP) break;
                    __builtin_amdgcn_s_sleep(1);
                }
                wait_ge(ctl + C_GU, 8);
                *(LAS v2u*)(L + GU_OFF + w * 896 + l2 * 8) = v2u{y0[0] & 0xffffff00u, y0[2] & 0xffffff00u};
                if (l2 < 48) *(LAS v2u*)(L + GU_OFF + w * 896 + 512 + l2 * 8) = v2u{y1[0] & 0xffffff00u, y1[2] & 0xffffff00u};
                arrive(ctl + C_FFN, l2);
            } else {
                asm volatile("s_waitcnt vmcnt(0)" : "+v"(wp[0]), "+v"(wp[1]), "+v"(wp[2]), "+v"(wp[3]) :: "memory");
            }
            if (t != 7 && t != 1 && !(t == GU_T + 1 && w >= 4)) landed();
        }
        STAMP(16 + t);
        f32x4 s4[4];
#pragma unroll
        for (int kg = 0; kg < 4; ++kg) {
            f32x4 a = {0.f, 0.f, 0.f, 0.f};
#pragma unroll
            for (int s = 0; s < 4; ++s) {
                const v4u kf = *(const LAS v4u*)(myslot + (16 * kg + c) * 256 + (((4 * s + g) ^ c) << 4));
                a = mfma(kf, qf[s], a);
            }
            s4[kg] = a;
        }
        f32x4 v;
#pragma unroll
        for (int i = 0; i < 4; ++i) {
            const float lo = (c & 1) ? s4[1][i] : s4[0][i], hi = (c & 1) ? s4[3][i] : s4[2][i];
            v[i] = rbf(rbf((c & 2) ? hi : lo) * 0.08838834764831845f);
            mx = fmaxf(mx, v[i]);
        }
        if (c < 4) *(LAS f32x4*)(sc + t * 64 + 16 * c + 4 * g) = v;
        __builtin_amdgcn_sched_barrier(0);
        if (t == GU_T && w >= 4) {
            v4u* wp = wor + 4 * (t - 1);
            asm volatile("s_waitcnt vmcnt(0)" : "+v"(gw[0]), "+v"(gw[1]), "+v"(gw[2]), "+v"(gw[3]), "+v"(gw[4]), "+v"(gw[5]), "+v"(gw[6]), "+v"(gw[7]),
                         "+v"(gw[8]), "+v"(gw[9]), "+v"(gw[10]), "+v"(gw[11]), "+v"(gw[12]), "+v"(gw[13]), "+v"(gw[14]), "+v"(gw[15]),
                         "+v"(xg[0][0]), "+v"(xg[0][1]), "+v"(xg[1][0]), "+v"(xg[1][1]), "+v"(xg[2][0]), "+v"(xg[2][1]), "+v"(xg[3][0]), "+v"(xg[3][1]) :: "memory");
            asm volatile("" : "+v"(wp[0]), "+v"(wp[1]), "+v"(wp[2]), "+v"(wp[3]));
            STAMP(44);
            f32x4 a[4];
#pragma unroll
            for (int j = 0; j < 4; ++j) a[j] = f32x4{0.f, 0.f, 0.f, 0.f};
#pragma unroll
            for (int k = 0; k < 16; ++k) {
#pragma unroll
                for (int j = 0; j < 4; ++j) a[j] = mfma(diagA(xg[j][k >> 3][(k >> 1) & 3], sel.s[k & 1]), gw[k], a[j]);
                PIN2(a[0], a[1]); PIN2(a[2], a[3]);
            }
            if (c < 14) {
#pragma unroll
                for (int j = 0; j < 4; ++j)
                    *(LAS f32x4*)(L + GU_OFF + ((((w - 4) * 8 + 2 * j + (g >> 1)) * FS) + 8 * c + 4 * (g & 1)) * 4) = a[j];
            }
            arrive(ctl + C_GU, lane);
            STAMP(56);
        }
    }
    STAMP(4);
    float lsum;
    mx = wave_max(mx);
    {
        const f32x4 sa = *(const LAS f32x4*)(sc + 8 * lane), sb = *(const LAS f32x4*)(sc + 8 * lane + 4);
        float e[8];
#pragma unroll
        for (int i = 0; i < 4; ++i) { e[i] = __expf(sa[i] - mx); e[4 + i] = __expf(sb[i] - mx); }
        lsum = wave_sum(((e[0] + e[1]) + (e[2] + e[3])) + ((e[4] + e[5]) + (e[6] + e[7])));
        v4u pk;
#pragma unroll
        for (int i = 0; i < 4; ++i) pk[i] = pack2(e[2 * i], e[2 * i + 1]);
        LGKM0();
        *(LAS v4u*)((LAS unsigned char*)sc + 16 * lane) = pk;
    }
    STAMP(5);

    f32x4 acco = {0.f, 0.f, 0.f, 0.f}, acco1 = {0.f, 0.f, 0.f, 0.f}, accb = {0.f, 0.f, 0.f, 0.f};
    {
        const unsigned vlin = lane * 16;
        {
            take_turn(64 + u);
            fill(vb + toff(0), vlin, vlin, vlin, vlin, myslot_a);
            issued(64 + u);
            STAMP(24);
            const int voo = 32 * g * (DM * 2) + c * 16;
#pragma unroll
            for (int r = 0; r < 4; ++r) wor[28 + r] = __builtin_amdgcn_raw_buffer_load_b128(ro, voo, soo + (28 + r) * (DM * 2), WO_AUX);
            asm volatile("s_waitcnt vmcnt(0)" : "+v"(wor[28]), "+v"(wor[29]), "+v"(wor[30]), "+v"(wor[31]) :: "memory");
            landed();
            STAMP(32);
            slot_rows64(myslot, (const LAS unsigned char*)sc, sel, c, g, acco, acco1);
        }
        const __amdgpu_buffer_rsrc_t rd = __builtin_amdgcn_make_buffer_rsrc((void*)wd, 0, FF * DM * 2, 0x00020000);
        const int vod = 28 * g * (DM * 2) + c * 16;
#pragma unroll 1
        for (int t = 1; t < 8; ++t) {
            take_turn(64 + 8 * t + u);
            fill(vb + toff(t), vlin, vlin, vlin, vlin, myslot_a);
            issued(64 + 8 * t + u);
            STAMP(24 + t);
            const int tt = t - 1;
            const int sod = (fs * FS + 4 * tt) * (DM * 2) + w * 256;
            v4u dw[4];
#pragma unroll
            for (int r = 0; r < 4; ++r) dw[r] = __builtin_amdgcn_raw_buffer_load_b128(rd, vod, sod + r * (DM * 2), WD_AUX);
            if (t == 1) {
                wait_ge(ctl + C_FFN, NW);
                const int l2 = fresh_lane();
                if (l2 < FS / 2) {
                    float g0 = 0.f, g1 = 0.f, u0 = 0.f, u1 = 0.f;
#pragma unroll
                    for (int ww = 0; ww < NW; ++ww) {
                        const f32x2 a = *(const LAS f32x2*)(L + GU_OFF + ww * 896 + 8 * l2), bb = *(const LAS f32x2*)(L + GU_OFF + ww * 896 + 448 + 8 * l2);
                        g0 += a[0]; g1 += a[1]; u0 += bb[0]; u1 += bb[1];
                    }
                    ((LAS unsigned*)(L + HH_OFF))[l2] = pack2(gelu_tanh(rbf(g0)) * rbf(u0), gelu_tanh(rbf(g1)) * rbf(u1));
                }
                LGKM0();
            }
            unsigned pf = 0u;
            if (CODE_PF2 && t == CODE_PF2 && w == 0 && lane < 16) pf = code_prefetch(lane);
            const v2u hs = *(const LAS v2u*)(L + HH_OFF + (28 * g + 4 * tt) * 2);
            asm volatile("s_waitcnt vmcnt(0)" : "+v"(dw[0]), "+v"(dw[1]), "+v"(dw[2]), "+v"(dw[3]), "+v"(pf) :: "memory");
            landed();
            STAMP(32 + t);
            slot_rows64(myslot, (const LAS unsigned char*)sc + t * 128, sel, c, g, acco, acco1);
#pragma unroll
            for (int r = 0; r < 4; ++r) accb = mfma(diagA(hs[r >> 1], sel.s[r & 1]), dw[r], accb);
        }
    }
    STAMP(6);
    acco += acco1;
    if (g < 2) *(LAS f32x4*)((LAS unsigned char*)sc + 1024 + (8 * c + 4 * g) * 4) = acco;
    if (lane == 0) { ((LAS float*)ctl)[C_WMAX + w] = mx; ((LAS float*)ctl)[C_WSUM + w] = lsum; }
    arrive(ctl + C_O, lane);
    wait_ge(ctl + C_O, NW);
    v4u of[4];
    {
        float m[NW], M = -INFINITY;
#pragma unroll
        for (int ww = 0; ww < NW; ++ww) { m[ww] = ((LAS float*)ctl)[C_WMAX + ww]; M = fmaxf(M, m[ww]); }
        float Ls = 0.f, s0 = 0.f, s1 = 0.f;
#pragma unroll
        for (int ww = 0; ww < NW; ++ww) {
            const float f = __expf(m[ww] - M);
            Ls += f * ((LAS float*)ctl)[C_WSUM + ww];
            const f32x2 v = *(const LAS f32x2*)(L + SC_OFF + ww * 2048 + 1024 + 8 * lane);
            s0 += f * v[0]; s1 += f * v[1];
        }
        const float inv = 1.0f / Ls;
        ((LAS unsigned*)(L + O_OFF))[lane] = pack2(s0 * inv, s1 * inv);
        LGKM0();
#pragma unroll
        for (int k = 0; k < 4; ++k) of[k] = *(const LAS v4u*)(L + O_OFF + 64 * g + 16 * k);
    }
    STAMP(7);
    f32x4 acca = {0.f, 0.f, 0.f, 0.f}, acca1 = {0.f, 0.f, 0.f, 0.f};
#pragma unroll
    for (int t = 0; t < 32; t += 2) {
        const unsigned S = of[t >> 3][(t >> 1) & 3];
        acca = mfma(diagA(S, sel.s[0]), wor[t], acca);
        acca1 = mfma(diagA(S, sel.s[1]), wor[t + 1], acca1);
    }
    acca += acca1;
    STAMP(40);
    {
        float z = 0.f;
#pragma unroll
        for (int ww = 0; ww < NW; ++ww) z += ((LAS float*)ctl)[C_GP + ww];
        z = rbf(z);
        const float gate = rbf(1.0f / (1.0f + __expf(-z)));
        const float omg = rbf(1.0f - gate);
        {
            const float r0 = gate * (g < 2 ? acca[0] : acca[2]) + omg * (g < 2 ? accb[0] : accb[2]);
            const float r1 = gate * (g < 2 ? acca[1] : acca[3]) + omg * (g < 2 ? accb[1] : accb[3]);
            store_dev(v4u{gran(r0, tag_lo), tag_hi, gran(r1, tag_lo), tag_hi}, rgran, gvo, gso);
        }
        const int l2 = fresh_lane();
        const int vo = ((l2 >> 4) * DM + 2 * (l2 & 15)) * 8, so = ((b * NH + 4 * w) * DM + 32 * h) * 8;
        v4u y;
        for (unsigned s = 0;; ++s) {
            y = __builtin_amdgcn_raw_buffer_load_b128(rgran, vo, so, 16);
            const unsigned bad = (y[1] ^ tag_hi) | (y[3] ^ tag_hi) | (((y[0] ^ tag_lo) | (y[2] ^ tag_lo)) & 0xffu);
            if (__all(bad == 0u) || s > SPIN_CAP) break;
            __builtin_amdgcn_s_sleep(1);
        }
        STAMP(41);
        float v0 = __builtin_bit_cast(float, y[0] & 0xffffff00u), v1 = __builtin_bit_cast(float, y[2] & 0xffffff00u);
        v0 += __shfl_xor(v0, 16); v1 += __shfl_xor(v1, 16);
        v0 += __shfl_xor(v0, 32); v1 += __shfl_xor(v1, 32);
        if (l2 < 16) *(LAS f32x2*)(L + GU_OFF + w * 128 + l2 * 8) = f32x2{v0, v1};
        LGKM0();
        unsigned old = 0u;
        if (l2 == 0) old = __hip_atomic_fetch_add(ctl + C_OUT, 1u, RLX_WG);
        if (rfl(old) == NW - 1) {
            CBAR();
            if (l2 < 16) {
                float s0 = 0.f, s1 = 0.f;
#pragma unroll
                for (int ww = 0; ww < NW; ++ww) { const f32x2 v = *(const LAS f32x2*)(L + GU_OFF + ww * 128 + l2 * 8); s0 += v[0]; s1 += v[1]; }
                __hip_atomic_store((gu32*)(out + ((size_t)b * DM + 32 * h + 2 * l2) * 2), pack2(s0, s1), __ATOMIC_RELAXED, __HIP_MEMORY_SCOPE_SYSTEM);
            }
            if (bh == 0 && l2 == 0) __hip_atomic_store((gu64*)(ws + WS_EPOCH), epoch, RLX_AGENT);
        }
        STAMP(57);
    }
    SDUMP();
    asm volatile(".Lfdk_end:");
}

}

extern "C" void kernel_launch(void* const* d_in, const int* in_sizes, int n_in, void* d_out, int out_size, void* d_ws, size_t ws_size,
                              hipStream_t stream) {
    (void)in_sizes; (void)n_in; (void)out_size; (void)ws_size;
    (void)hipFuncSetAttribute((const void*)fused_decode_kernel, hipFuncAttributeMaxDynamicSharedMemorySize, LDS_BYTES);
    fused_decode_kernel<<<dim3(NB * NH), dim3(NTHR), LDS_BYTES, stream>>>(
        (const unsigned char*)d_in[0], (const unsigned char*)d_in[1], (const unsigned char*)d_in[2], (const unsigned char*)d_in[3],
        (const unsigned char*)d_in[4], (const unsigned char*)d_in[6], (const unsigned char*)d_in[7],
        (const unsigned char*)d_in[8], (const unsigned char*)d_in[9], (unsigned char*)d_out, (unsigned char*)d_ws
    );
}
```

```cpp
#include <hip/hip_runtime.h>
#include <hip/hip_bf16.h>

#define GAS __attribute__((address_space(1)))
#define LAS __attribute__((address_space(3)))
typedef unsigned v4u __attribute__((ext_vector_type(4)));
typedef unsigned v2u __attribute__((ext_vector_type(2)));
typedef float f32x4 __attribute__((ext_vector_type(4)));
typedef float f32x2 __attribute__((ext_vector_type(2)));
typedef short bf16x8 __attribute__((ext_vector_type(8)));
typedef GAS unsigned gu32;
typedef GAS unsigned long long gu64;
#define RLX_AGENT __ATOMIC_RELAXED, __HIP_MEMORY_SCOPE_AGENT
#define RLX_WG __ATOMIC_RELAXED, __HIP_MEMORY_SCOPE_WORKGROUP

namespace {

constexpr int NB = 8, DM = 1024, NH = 32, SK = 4096, DH = 128, FF = 3584;
constexpr int FS = FF / NH;
constexpr int NW = 8, NTHR = 64 * NW;
constexpr int SLOT = 16384;
#define GUNT " nt"
#define ROTK 0
#define PRE 1
#define GU_T 2
#define WO_AUX 0
#define WD_AUX 0
#define POLL_SLEEP 1
#define QNT " nt"
#define CODE_PF 1
#define CODE_PF2 5
#define TLB_WARM 1
#define TLB_W 7
#define INVAL 0
#define PACE (16 * 61)
#define TSTART 600
#define DEPTH 4

constexpr int SC_OFF = NW * SLOT;
constexpr int GU_OFF = SC_OFF + NW * 2048;
constexpr int Q_OFF = GU_OFF + 14336;
constexpr int O_OFF = Q_OFF + 256;
constexpr int HH_OFF = O_OFF + 256;
constexpr int CTL_OFF = HH_OFF + 256;
constexpr int LDS_BYTES = CTL_OFF + 256;
constexpr int C_ISS = 0, C_LAND = 1, C_FFN = 2, C_O = 3, C_QP = 4, C_QR = 5, C_GU = 6, C_OUT = 7, C_TAG = 8, C_POLL = 10, C_WMAX = 16, C_WSUM = 24, C_GP = 32;
static_assert(LDS_BYTES <= 160 * 1024, "LDS");

constexpr size_t WS_EPOCH = 0, WS_GRAN = 4096, WS_QGRAN = WS_GRAN + (size_t)NB * NH * DM * 8, WS_GUGRAN = WS_QGRAN + (size_t)NH * NB * NB * DH * 8;

constexpr unsigned SPIN_CAP = 1u << 22;
#define STAMP(i) do {} while (0)
#define SDUMP() do {} while (0)

__device__ __forceinline__ f32x4 mfma(v4u a, v4u b, f32x4 c) {
    return __builtin_amdgcn_mfma_f32_16x16x32_bf16(__builtin_bit_cast(bf16x8, a), __builtin_bit_cast(bf16x8, b), c, 0, 0, 0);
}
__device__ __forceinline__ float bf_lo(unsigned u) { return __builtin_bit_cast(float, u << 16); }
__device__ __forceinline__ float bf_hi(unsigned u) { return __builtin_bit_cast(float, u & 0xffff0000u); }
__device__ __forceinline__ float rbf(float f) { return (float)(__bf16)f; }
__device__ __forceinline__ unsigned bf_bits(float f) { return (unsigned)__builtin_bit_cast(unsigned short, (__bf16)f); }
__device__ __forceinline__ unsigned pack2(float lo, float hi) { return bf_bits(lo) | (bf_bits(hi) << 16); }
__device__ __forceinline__ unsigned gran(float f, unsigned tag_lo) { return ((__builtin_bit_cast(unsigned, f) + 0x80u) & 0xffffff00u) | tag_lo; }
__device__ __forceinline__ void store_dev(v4u d, __amdgpu_buffer_rsrc_t r, int vo, int so) {
    __builtin_amdgcn_raw_buffer_store_b128(d, r, vo, so, 16);
    asm volatile("s_nop 1" ::"v"(d) : "memory");
}
__device__ __forceinline__ unsigned long long rt() { return __builtin_amdgcn_s_memrealtime(); }
__device__ __forceinline__ unsigned rfl(unsigned v) { return (unsigned)__builtin_amdgcn_readfirstlane((int)v); }
#define LGKM0() asm volatile("s_waitcnt lgkmcnt(0)" ::: "memory")
#define VMCNT(n) asm volatile("s_waitcnt vmcnt(%0)" ::"i"(n) : "memory")
#define CBAR() asm volatile("" ::: "memory")
#define PIN2(a, b) do { asm volatile("" : "+v"(a), "+v"(b)); __builtin_amdgcn_sched_barrier(0); } while (0)

__device__ __forceinline__ void glds4(const void* sbase, unsigned v0, unsigned v1, unsigned v2, unsigned v3, unsigned lds) {
    unsigned keep;
    asm volatile("s_mov_b32 %0, m0\n\ts_mov_b32 m0, %6\n\ts_nop 0\n\t"
                 "global_load_lds_dwordx4 %1, %5 nt\n\t"
                 "global_load_lds_dwordx4 %2, %5 offset:1024 nt\n\t"
                 "global_load_lds_dwordx4 %3, %5 offset:2048 nt\n\t"
                 "global_load_lds_dwordx4 %4, %5 offset:3072 nt\n\t"
                 "s_mov_b32 m0, %0"
                 : "=&s"(keep) : "v"(v0), "v"(v1), "v"(v2), "v"(v3), "s"(sbase), "s"(lds) : "memory");
}
__device__ __forceinline__ void fill(const unsigned char* src, unsigned v0, unsigned v1, unsigned v2, unsigned v3, unsigned lds) {
    LGKM0();
#pragma unroll
    for (int q = 0; q < 4; ++q) glds4(src + q * 4096, v0, v1, v2, v3, lds + q * 4096);
}

__device__ __forceinline__ void wait_ge(LAS unsigned* w, unsigned need) {
    for (unsigned s = 0; rfl(*(volatile LAS unsigned*)w) < need && s < SPIN_CAP; ++s) __builtin_amdgcn_s_sleep(0);
    CBAR();
}
__device__ __forceinline__ void arrive(LAS unsigned* w, int lane) {
    LGKM0();
    if (lane == 0) __hip_atomic_fetch_add(w, 1u, RLX_WG);
    CBAR();
}

struct Sel { unsigned s[2][4]; };
__device__ __forceinline__ v4u diagA(unsigned S, const unsigned (&sel)[4]) {
    return v4u{__builtin_amdgcn_perm(0u, S, sel[0]), __builtin_amdgcn_perm(0u, S, sel[1]), __builtin_amdgcn_perm(0u, S, sel[2]), __builtin_amdgcn_perm(0u, S, sel[3])};
}
__device__ __forceinline__ int fresh_lane() {
    int l;
    asm volatile("v_mbcnt_lo_u32_b32 %0, -1, 0\n\tv_mbcnt_hi_u32_b32 %0, -1, %0" : "=v"(l));
    __builtin_assume((unsigned)l < 64u);
    return l;
}
__device__ __forceinline__ Sel make_sel(int c) {
    Sel sel;
    const int dm = (c & 7) >> 1, hf = c & 1;
#pragma unroll
    for (int par = 0; par < 2; ++par)
#pragma unroll
        for (int d = 0; d < 4; ++d) {
            const unsigned lohi = (unsigned)(2 * par) | ((unsigned)(2 * par + 1) << 8);
            const unsigned v = hf ? (0x0c0cu | (lohi << 16)) : (0x0c0c0000u | lohi);
            sel.s[par][d] = (c < 8 && d == dm) ? v : 0x0c0c0c0cu;
        }
    return sel;
}

__device__ __forceinline__ void slot_rows64(const LAS unsigned char* slot, const LAS unsigned char* vec, const Sel& sel, int c, int g,
                                            f32x4& acc, f32x4& a1) {
    const LAS unsigned char* base = slot + g * 4096 + c * 16;
#pragma unroll
    for (int hf = 0; hf < 2; ++hf) {
        const v4u s = *(const LAS v4u*)(vec + g * 32 + hf * 16);
        v4u bb[8];
#pragma unroll
        for (int t = 0; t < 8; ++t) bb[t] = *(const LAS v4u*)(base + (8 * hf + t) * 256);
        __builtin_amdgcn_sched_barrier(0);
#pragma unroll
        for (int u = 0; u < 4; ++u) {
            acc = mfma(diagA(s[u], sel.s[0]), bb[2 * u], acc);
            a1 = mfma(diagA(s[u], sel.s[1]), bb[2 * u + 1], a1);
            PIN2(acc, a1);
        }
    }
}

__device__ __forceinline__ float gelu_tanh(float x) {
    const float y = 0.7978845608028654f * (x + 0.044715f * x * x * x);
    return rbf(x / (1.0f + __expf(-2.0f * y)));
}
__device__ __forceinline__ float wave_max(float v) {
#pragma unroll
    for (int o = 32; o >= 1; o >>= 1) v = fmaxf(v, __shfl_xor(v, o));
    return v;
}
__device__ __forceinline__ float wave_sum(float v) {
#pragma unroll
    for (int o = 32; o >= 1; o >>= 1) v += __shfl_xor(v, o);
    return v;
}

__global__ void __launch_bounds__(NTHR)
fused_decode_kernel(const unsigned char* __restrict__ xin, const unsigned char* __restrict__ kc, const unsigned char* __restrict__ vc,
                    const unsigned char* __restrict__ wq, const unsigned char* __restrict__ wo,
                    const unsigned char* __restrict__ wg, const unsigned char* __restrict__ wu, const unsigned char* __restrict__ wd,
                    const unsigned char* __restrict__ gatew, unsigned char* __restrict__ out, unsigned char* __restrict__ ws,
                    int pace, int tstart, int depth) {
    extern __shared__ __attribute__((aligned(16))) unsigned char smem[];
    LAS unsigned char* L = (LAS unsigned char*)smem;
    LAS unsigned* ctl = (LAS unsigned*)(L + CTL_OFF);
    asm volatile(".Lfdk_begin:");
    const unsigned long long t0 = rt();
    asm volatile("" ::"s"(xin), "s"(kc), "s"(vc), "s"(wq), "s"(wo), "s"(wg), "s"(wu), "s"(wd), "s"(gatew), "s"(out), "s"(ws), "s"(pace), "s"(tstart), "s"(depth));
    const int w = __builtin_amdgcn_readfirstlane((int)threadIdx.x >> 6);
    const int h = (blockIdx.x & 7) * 4 + ((blockIdx.x >> 3) & 3), b = blockIdx.x >> 5;
    const int fs = h, bh = b * NH + h;
    STAMP(0);
    const unsigned myslot_a = (unsigned)(size_t)L + w * SLOT;
    const LAS unsigned char* myslot = L + w * SLOT;
    const unsigned rot = (unsigned)(ROTK) + (w ^ 4);
    auto toff = [&](int t) { return (size_t)(((8u * t + rot) & 63u) * SLOT); };
    int lane, c, g;
    unsigned vk0, vk1, vk2, vk3;
    Sel sel;
    const unsigned char* kb;
    auto setup = [&]() {
        lane = fresh_lane(); c = lane & 15; g = lane >> 4;
        vk0 = g * 256 + ((c ^ g) << 4); vk1 = g * 256 + ((c ^ (4 + g)) << 4); vk2 = g * 256 + ((c ^ (8 + g)) << 4); vk3 = g * 256 + ((c ^ (12 + g)) << 4);
        sel = make_sel(c & 7);
        int bh2 = bh;
        asm volatile("" : "+s"(bh2));
        kb = kc + (size_t)bh2 * (SK * DH * 2);
    };
    auto code_prefetch = [&](int l) {
        unsigned long long pc;
        unsigned off, len, v = 0u;
        asm volatile("s_getpc_b64 %0\n.Lfdk_pc%=:\n\ts_mov_b32 %1, .Lfdk_pc%=-.Lfdk_begin\n\ts_mov_b32 %2, .Lfdk_end-.Lfdk_begin" : "=s"(pc), "=s"(off), "=s"(len));
        const unsigned o = (((blockIdx.x >> 3) & 31u) + 32u * (unsigned)l) * 128u;
        if (o < len) asm volatile("global_load_dword %0, %1, %2" : "+v"(v) : "v"(o), "s"(pc - off) : "memory");
        return v;
    };
    const int u = w ^ 4;
    auto take_turn = [&](int f) {
        const unsigned long long td = t0 + (unsigned)(tstart + (((f - 8) * pace) >> 4));
        while (rt() < td) __builtin_amdgcn_s_sleep(1);
        for (unsigned s = 0; s < SPIN_CAP; ++s) {
            const v2u st = *(volatile LAS v2u*)(ctl + C_ISS);
            if (rfl(st[0]) >= (unsigned)f && rfl(st[1]) + (unsigned)depth > (unsigned)f) break;
            __builtin_amdgcn_s_sleep(0);
        }
        CBAR();
    };
    auto issued = [&](int f) { if (lane == 0) *(volatile LAS unsigned*)(ctl + C_ISS) = (unsigned)f + 1u; CBAR(); };
    auto landed = [&]() { if (lane == 0) __hip_atomic_fetch_add(ctl + C_LAND, 1u, RLX_WG); CBAR(); };
    LAS float* sc = (LAS float*)(L + SC_OFF + w * 2048);
    unsigned long long epoch;

    if (__builtin_expect(w < 4, 1)) {
        v4u x8[4], wr[8];
        v2u ep;
        const unsigned l0 = threadIdx.x & 63;
        const unsigned char* qs = wq + (size_t)(128 * b + 32 * w) * (NH * DH * 2) + h * 256;
        const unsigned vq = (l0 >> 4) * 8 * (NH * DH * 2) + (l0 & 15) * 16;
#pragma unroll
        for (int t = 0; t < 8; ++t)
            asm volatile("global_load_dwordx4 %0, %1, %2" QNT : "=v"(wr[t]) : "v"(vq), "s"(qs + (size_t)t * (NH * DH * 2)) : "memory");
        __builtin_amdgcn_sched_barrier(0);
        const unsigned char* xs = xin + (128 * b + 32 * w) * 2;
        const unsigned vx = ((l0 >> 3) & 1) * (DM * 2) + (l0 >> 4) * 16;
#pragma unroll
        for (int j = 0; j < 4; ++j)
            asm volatile("global_load_dwordx4 %0, %1, %2" : "=v"(x8[j]) : "v"(vx), "s"(xs + j * (2 * DM * 2)) : "memory");
        asm volatile("global_load_dwordx2 %0, %1, %2 sc1" : "=v"(ep) : "v"(0u), "s"(ws + WS_EPOCH) : "memory");
        const unsigned char* inbox = ws + WS_QGRAN + (size_t)(h * NB + b) * NB * (DH * 8);
        unsigned warm;
        asm volatile("global_load_dword %0, %1, %2 sc1" : "=v"(warm) : "v"(l0 * 16), "s"(inbox) : "memory");
        STAMP(42);
        __builtin_amdgcn_s_barrier();
        setup();
        VMCNT(0);
        asm volatile("" : "+v"(x8[0]), "+v"(x8[1]), "+v"(x8[2]), "+v"(x8[3]), "+v"(wr[0]), "+v"(wr[1]), "+v"(wr[2]), "+v"(wr[3]),
                     "+v"(wr[4]), "+v"(wr[5]), "+v"(wr[6]), "+v"(wr[7]), "+v"(warm), "+v"(ep) :: "memory");
        STAMP(45);
        {
            const unsigned e0 = ep[0], e1 = ep[1];
            epoch = (((unsigned long long)rfl(e1) << 32) | rfl(e0)) + 1ull;
        }
        const unsigned qtag_hi = (unsigned)(epoch >> 8), qtag_lo = (unsigned)epoch & 0xffu;
#pragma unroll
        for (int j = 0; j < 4; ++j) {
            f32x4 a0 = {0.f, 0.f, 0.f, 0.f}, a1 = {0.f, 0.f, 0.f, 0.f};
#pragma unroll
            for (int k = 0; k < 4; ++k) {
                a0 = mfma(diagA(x8[j][k], sel.s[0]), wr[2 * k], a0);
                a1 = mfma(diagA(x8[j][k], sel.s[1]), wr[2 * k + 1], a1);
            }
            a0 += a1;
            *(LAS f32x4*)(L + SC_OFF + ((w * 8 + 2 * j + (g >> 1)) * 128 + 8 * c + 4 * (g & 1)) * 4) = a0;
        }
        if (w == 0 && lane == 0) *(LAS v2u*)(ctl + C_TAG) = v2u{(unsigned)epoch, (unsigned)(epoch >> 32)};
        arrive(ctl + C_QP, lane);
        wait_ge(ctl + C_QP, 4);
        STAMP(1);
        {
            const __amdgpu_buffer_rsrc_t rqg = __builtin_amdgcn_make_buffer_rsrc((void*)(ws + WS_QGRAN), 0, NH * NB * NB * DH * 8, 0x00020000);
#pragma unroll
            for (int r = 0; r < 2; ++r) {
                float s0 = 0.f, s1 = 0.f;
#pragma unroll
                for (int k = 0; k < 4; ++k) { const f32x2 v = *(const LAS f32x2*)(L + SC_OFF + ((k * 8 + 2 * w + r) * 128 + 2 * lane) * 4); s0 += v[0]; s1 += v[1]; }
                store_dev(v4u{gran(s0, qtag_lo), qtag_hi, gran(s1, qtag_lo), qtag_hi}, rqg, lane * 16, ((h * NB + 2 * w + r) * NB + b) * (DH * 8));
            }
        }
        arrive(ctl + C_QP, lane);
        STAMP(46);
        if (w == 3) {
            VMCNT(0);
            STAMP(55);
            const unsigned vo = lane * 16;
            v4u y[NB];
            for (unsigned s = 0;; ++s) {
#pragma unroll
                for (int p = 0; p < NB; ++p)
                    asm volatile("global_load_dwordx4 %0, %1, %2 offset:%3 sc1" : "=v"(y[p]) : "v"(vo), "s"(inbox + (p >> 2) * 4096), "i"((p & 3) * 1024) : "memory");
                if (s == 0) {
                    if (lane == 0) *(volatile LAS unsigned*)(ctl + C_POLL) = 1u;
                    fill(kb + toff(0), vk0, vk1, vk2, vk3, myslot_a);
                    VMCNT(16);
                } else {
                    VMCNT(0);
                }
                asm volatile("" : "+v"(y[0]), "+v"(y[1]), "+v"(y[2]), "+v"(y[3]), "+v"(y[4]), "+v"(y[5]), "+v"(y[6]), "+v"(y[7]) :: "memory");
                unsigned bad = 0u;
#pragma unroll
                for (int p = 0; p < NB; ++p) bad |= (y[p][1] ^ qtag_hi) | (y[p][3] ^ qtag_hi) | (((y[p][0] ^ qtag_lo) | (y[p][2] ^ qtag_lo)) & 0xffu);
                if (__all(bad == 0u) || s > SPIN_CAP) break;
                __builtin_amdgcn_s_sleep(POLL_SLEEP);
            }
            float s0 = 0.f, s1 = 0.f;
#pragma unroll
            for (int p = 0; p < NB; ++p) { s0 += __builtin_bit_cast(float, y[p][0] & 0xffffff00u); s1 += __builtin_bit_cast(float, y[p][2] & 0xffffff00u); }
            ((LAS unsigned*)(L + Q_OFF))[lane] = pack2(s0, s1);
            wait_ge(ctl + C_QP, 8);
            arrive(ctl + C_QR, lane);
        } else {
            if (w >= PRE) wait_ge(ctl + C_POLL, 1);
            fill(kb + toff(0), vk0, vk1, vk2, vk3, myslot_a);
        }
        STAMP(47);
        VMCNT(0);
        landed();
    } else {
        setup();
        if (w == 4 && lane < 16) ctl[lane] = lane == C_ISS ? 8u : 0u;
        unsigned pf = 0u;
        if (CODE_PF && w == 5 && lane < 16) pf = code_prefetch(lane);
        LGKM0();
        __builtin_amdgcn_s_barrier();
        fill(kb + toff(0), vk0, vk1, vk2, vk3, myslot_a);
        if (TLB_WARM && w == TLB_W) {
            const unsigned z = 0u;
            auto touch = [&](const unsigned char* p) { asm volatile("global_load_dword %0, %1, %2 sc1" : "+v"(pf) : "v"(z), "s"(p) : "memory"); };
            const unsigned char* v0 = vc + (size_t)bh * (SK * DH * 2);
            touch(v0); touch(v0 + (SK * DH * 2 - 128)); touch(kb + (SK * DH * 2 - 128));
            touch(wo + (size_t)h * DH * (DM * 2)); touch(wo + (size_t)(h + 1) * DH * (DM * 2) - 128);
            touch(wd + (size_t)fs * FS * (DM * 2)); touch(wd + (size_t)(fs + 1) * FS * (DM * 2) - 128);
            touch(wg + (size_t)(128 * b) * (FF * 2) + fs * (FS * 2)); touch(wg + (size_t)(128 * b + 127) * (FF * 2) + fs * (FS * 2));
            touch(wu + (size_t)(128 * b) * (FF * 2) + fs * (FS * 2)); touch(wu + (size_t)(128 * b + 127) * (FF * 2) + fs * (FS * 2));
            touch(ws + WS_GUGRAN + (size_t)h * (NB * NB * 2048)); touch(ws + WS_GRAN + (size_t)bh * (DM * 8));
            touch(out + (size_t)b * (DM * 2)); touch(gatew);
        }
        STAMP(43);
        asm volatile("s_waitcnt vmcnt(0)" : "+v"(pf)::"memory");
        landed();
        STAMP(2);
        wait_ge(ctl + C_QP, 4);
        const v2u e = *(volatile LAS v2u*)(ctl + C_TAG);
        const unsigned e0 = e[0], e1 = e[1];
        epoch = ((unsigned long long)rfl(e1) << 32) | rfl(e0);
    }
    const unsigned tag_hi = (unsigned)(epoch >> 8), tag_lo = (unsigned)epoch & 0xffu;
    const unsigned char* vb = vc + (size_t)bh * (SK * DH * 2);
    const __amdgpu_buffer_rsrc_t rgran = __builtin_amdgcn_make_buffer_rsrc((void*)(ws + WS_GRAN), 0, NB * NH * DM * 8, 0x00020000);
    const int gvo = (8 * c + 4 * (g & 1) + 2 * (g >> 1)) * 8, gso = bh * (DM * 8) + w * 1024;
    wait_ge(ctl + C_QR, 1);
    v4u qf[4];
#pragma unroll
    for (int s = 0; s < 4; ++s) qf[s] = *(const LAS v4u*)(L + Q_OFF + (32 * s + 8 * g) * 2);
    LGKM0();
    STAMP(3);

    float mx = -INFINITY;
    v4u wor[32];
    const __amdgpu_buffer_rsrc_t ro = __builtin_amdgcn_make_buffer_rsrc((void*)wo, 0, DM * NH * DH * 2, 0x00020000);
    const int soo = h * DH * (DM * 2) + w * 256;
#pragma unroll
    for (int t = 0; t < 8; ++t) {
        if (t > 0) { take_turn(8 * t + u); fill(kb + toff(t), vk0, vk1, vk2, vk3, myslot_a); issued(8 * t + u); }
        STAMP(8 + t);
        v4u xg[4][2], gw[16];
        if (t > 0) {
            const int l2 = fresh_lane();
            const int voo = 32 * (l2 >> 4) * (DM * 2) + (l2 & 15) * 16;
            v4u* wp = wor + 4 * (t - 1);
#pragma unroll
            for (int r = 0; r < 4; ++r) wp[r] = __builtin_amdgcn_raw_buffer_load_b128(ro, voo, soo + (4 * (t - 1) + r) * (DM * 2), WO_AUX);
            if (t == 1) {
                const __amdgpu_buffer_rsrc_t rx = __builtin_amdgcn_make_buffer_rsrc((void*)xin, 0, NB * DM * 2, 0x00020000);
                const __amdgpu_buffer_rsrc_t rw = __builtin_amdgcn_make_buffer_rsrc((void*)gatew, 0, DM * 2, 0x00020000);
                v4u xv = __builtin_amdgcn_raw_buffer_load_b128(rx, (l2 & 15) * 16, b * (DM * 2) + w * 256, 0);
                v4u gv = __builtin_amdgcn_raw_buffer_load_b128(rw, (l2 & 15) * 16, w * 256, 0);
                asm volatile("s_waitcnt vmcnt(0)" : "+v"(wp[0]), "+v"(wp[1]), "+v"(wp[2]), "+v"(wp[3]), "+v"(xv), "+v"(gv) :: "memory");
                landed();
                if (INVAL) store_dev(v4u{tag_lo ^ 1u, ~tag_hi, tag_lo ^ 1u, ~tag_hi}, rgran, gvo, gso);
                float s = 0.f;
#pragma unroll
                for (int k = 0; k < 4; ++k) s += rbf(bf_lo(xv[k]) * bf_lo(gv[k])) + rbf(bf_hi(xv[k]) * bf_hi(gv[k]));
                s = wave_sum(l2 < 16 ? s : 0.f);
                if (l2 == 0) ((LAS float*)ctl)[C_GP + w] = s;
            } else if (t == GU_T && w >= 4) {
                const int m = (w >> 1) & 1, hf = w & 1;
                const unsigned char* gs = (m ? wu : wg) + (size_t)(128 * b + 64 * hf) * (FF * 2) + fs * (FS * 2);
                const unsigned vog = 16 * (l2 >> 4) * (FF * 2) + ((l2 & 15) < 14 ? (l2 & 15) : 13) * 16;
#pragma unroll
                for (int k = 0; k < 16; ++k)
                    asm volatile("global_load_dwordx4 %0, %1, %2" GUNT : "=v"(gw[k]) : "v"(vog), "s"(gs + (size_t)k * (FF * 2)) : "memory");
                const unsigned char* xs = xin + (128 * b + 64 * hf) * 2;
                const unsigned vox = ((l2 & 15) >> 3) * (DM * 2) + (l2 >> 4) * 32;
#pragma unroll
                for (int j = 0; j < 4; ++j) {
                    asm volatile("global_load_dwordx4 %0, %1, %2" : "=v"(xg[j][0]) : "v"(vox), "s"(xs + j * (2 * DM * 2)) : "memory");
                    asm volatile("global_load_dwordx4 %0, %1, %2 offset:16" : "=v"(xg[j][1]) : "v"(vox), "s"(xs + j * (2 * DM * 2)) : "memory");
                }
                VMCNT(24);
            } else if (t == GU_T + 1 && w >= 4) {
                asm volatile("s_waitcnt vmcnt(0)" : "+v"(wp[0]), "+v"(wp[1]), "+v"(wp[2]), "+v"(wp[3]) :: "memory");
                landed();
                wait_ge(ctl + C_GU, 4);
                const __amdgpu_buffer_rsrc_t rgg = __builtin_amdgcn_make_buffer_rsrc((void*)(ws + WS_GUGRAN), 0, NH * NB * NB * 2048, 0x00020000);
#pragma unroll
                for (int r = 0; r < 2; ++r)
#pragma unroll
                    for (int q = 0; q < 2; ++q) {
                        const int p = q == 0 ? l2 : (l2 < 48 ? 64 + l2 : 111);
                        const int mm = p >= 56 ? 1 : 0, col = 2 * (p - 56 * mm), cons = 2 * (w - 4) + r;
                        const f32x2 v0 = *(const LAS f32x2*)(L + GU_OFF + (((2 * mm) * 8 + cons) * FS + col) * 4);
                        const f32x2 v1 = *(const LAS f32x2*)(L + GU_OFF + (((2 * mm + 1) * 8 + cons) * FS + col) * 4);
                        const float s0 = v0[0] + v1[0], s1 = v0[1] + v1[1];
                        if (q == 0 || l2 < 48)
                            store_dev(v4u{gran(s0, tag_lo), tag_hi, gran(s1, tag_lo), tag_hi}, rgg, p * 16, ((h * NB + cons) * NB + b) * 2048);
                    }
                arrive(ctl + C_GU, l2);
            } else if (t == 7) {
                const __amdgpu_buffer_rsrc_t rgg = __builtin_amdgcn_make_buffer_rsrc((void*)(ws + WS_GUGRAN), 0, NH * NB * NB * 2048, 0x00020000);
                const int sgg = ((h * NB + b) * NB + w) * 2048, v1o = 1024 + (l2 < 48 ? l2 : 47) * 16;
                v4u y0, y1;
                for (unsigned s = 0;; ++s) {
                    y0 = __builtin_amdgcn_raw_buffer_load_b128(rgg, l2 * 16, sgg, 16);
                    y1 = __builtin_amdgcn_raw_buffer_load_b128(rgg, v1o, sgg, 16);
                    asm volatile("s_waitcnt vmcnt(0)" : "+v"(wp[0]), "+v"(wp[1]), "+v"(wp[2]), "+v"(wp[3]), "+v"(y0), "+v"(y1) :: "memory");
                    const unsigned bad = (y0[1] ^ tag_hi) | (y0[3] ^ tag_hi) | (y1[1] ^ tag_hi) | (y1[3] ^ tag_hi) | (((y0[0] ^ tag_lo) | (y0[2] ^ tag_lo) | (y1[0] ^ tag_lo) | (y1[2] ^ tag_lo)) & 0xffu);
                    if (s == 0) landed();
                    if (__all(bad == 0u) || s > SPIN_CAP) break;
                    __builtin_amdgcn_s_sleep(1);
                }
                wait_ge(ctl + C_GU, 8);
                *(LAS v2u*)(L + GU_OFF + w * 896 + l2 * 8) = v2u{y0[0] & 0xffffff00u, y0[2] & 0xffffff00u};
                if (l2 < 48) *(LAS v2u*)(L + GU_OFF + w * 896 + 512 + l2 * 8) = v2u{y1[0] & 0xffffff00u, y1[2] & 0xffffff00u};
                arrive(ctl + C_FFN, l2);
            } else {
                asm volatile("s_waitcnt vmcnt(0)" : "+v"(wp[0]), "+v"(wp[1]), "+v"(wp[2]), "+v"(wp[3]) :: "memory");
            }
            if (t != 7 && t != 1 && !(t == GU_T + 1 && w >= 4)) landed();
        }
        STAMP(16 + t);
        f32x4 s4[4];
#pragma unroll
        for (int kg = 0; kg < 4; ++kg) {
            f32x4 a = {0.f, 0.f, 0.f, 0.f};
#pragma unroll
            for (int s = 0; s < 4; ++s) {
                const v4u kf = *(const LAS v4u*)(myslot + (16 * kg + c) * 256 + (((4 * s + g) ^ c) << 4));
                a = mfma(kf, qf[s], a);
            }
            s4[kg] = a;
        }
        f32x4 v;
#pragma unroll
        for (int i = 0; i < 4; ++i) {
            const float lo = (c & 1) ? s4[1][i] : s4[0][i], hi = (c & 1) ? s4[3][i] : s4[2][i];
            v[i] = rbf(rbf((c & 2) ? hi : lo) * 0.08838834764831845f);
            mx = fmaxf(mx, v[i]);
        }
        if (c < 4) *(LAS f32x4*)(sc + t * 64 + 16 * c + 4 * g) = v;
        __builtin_amdgcn_sched_barrier(0);
        if (t == GU_T && w >= 4) {
            v4u* wp = wor + 4 * (t - 1);
            asm volatile("s_waitcnt vmcnt(0)" : "+v"(gw[0]), "+v"(gw[1]), "+v"(gw[2]), "+v"(gw[3]), "+v"(gw[4]), "+v"(gw[5]), "+v"(gw[6]), "+v"(gw[7]),
                         "+v"(gw[8]), "+v"(gw[9]), "+v"(gw[10]), "+v"(gw[11]), "+v"(gw[12]), "+v"(gw[13]), "+v"(gw[14]), "+v"(gw[15]),
                         "+v"(xg[0][0]), "+v"(xg[0][1]), "+v"(xg[1][0]), "+v"(xg[1][1]), "+v"(xg[2][0]), "+v"(xg[2][1]), "+v"(xg[3][0]), "+v"(xg[3][1]) :: "memory");
            asm volatile("" : "+v"(wp[0]), "+v"(wp[1]), "+v"(wp[2]), "+v"(wp[3]));
            STAMP(44);
            f32x4 a[4];
#pragma unroll
            for (int j = 0; j < 4; ++j) a[j] = f32x4{0.f, 0.f, 0.f, 0.f};
#pragma unroll
            for (int k = 0; k < 16; ++k) {
#pragma unroll
                for (int j = 0; j < 4; ++j) a[j] = mfma(diagA(xg[j][k >> 3][(k >> 1) & 3], sel.s[k & 1]), gw[k], a[j]);
                PIN2(a[0], a[1]); PIN2(a[2], a[3]);
            }
            if (c < 14) {
#pragma unroll
                for (int j = 0; j < 4; ++j)
                    *(LAS f32x4*)(L + GU_OFF + ((((w - 4) * 8 + 2 * j + (g >> 1)) * FS) + 8 * c + 4 * (g & 1)) * 4) = a[j];
            }
            arrive(ctl + C_GU, lane);
            STAMP(56);
        }
    }
    STAMP(4);
    float lsum;
    mx = wave_max(mx);
    {
        const f32x4 sa = *(const LAS f32x4*)(sc + 8 * lane), sb = *(const LAS f32x4*)(sc + 8 * lane + 4);
        float e[8];
#pragma unroll
        for (int i = 0; i < 4; ++i) { e[i] = __expf(sa[i] - mx); e[4 + i] = __expf(sb[i] - mx); }
        lsum = wave_sum(((e[0] + e[1]) + (e[2] + e[3])) + ((e[4] + e[5]) + (e[6] + e[7])));
        v4u pk;
#pragma unroll
        for (int i = 0; i < 4; ++i) pk[i] = pack2(e[2 * i], e[2 * i + 1]);
        LGKM0();
        *(LAS v4u*)((LAS unsigned char*)sc + 16 * lane) = pk;
    }
    STAMP(5);

    f32x4 acco = {0.f, 0.f, 0.f, 0.f}, acco1 = {0.f, 0.f, 0.f, 0.f}, accb = {0.f, 0.f, 0.f, 0.f};
    {
        const unsigned vlin = lane * 16;
        {
            take_turn(64 + u);
            fill(vb + toff(0), vlin, vlin, vlin, vlin, myslot_a);
            issued(64 + u);
            STAMP(24);
            const int voo = 32 * g * (DM * 2) + c * 16;
#pragma unroll
            for (int r = 0; r < 4; ++r) wor[28 + r] = __builtin_amdgcn_raw_buffer_load_b128(ro, voo, soo + (28 + r) * (DM * 2), WO_AUX);
            asm volatile("s_waitcnt vmcnt(0)" : "+v"(wor[28]), "+v"(wor[29]), "+v"(wor[30]), "+v"(wor[31]) :: "memory");
            landed();
            STAMP(32);
            slot_rows64(myslot, (const LAS unsigned char*)sc, sel, c, g, acco, acco1);
        }
        const __amdgpu_buffer_rsrc_t rd = __builtin_amdgcn_make_buffer_rsrc((void*)wd, 0, FF * DM * 2, 0x00020000);
        const int vod = 28 * g * (DM * 2) + c * 16;
#pragma unroll 1
        for (int t = 1; t < 8; ++t) {
            take_turn(64 + 8 * t + u);
            fill(vb + toff(t), vlin, vlin, vlin, vlin, myslot_a);
            issued(64 + 8 * t + u);
            STAMP(24 + t);
            const int tt = t - 1;
            const int sod = (fs * FS + 4 * tt) * (DM * 2) + w * 256;
            v4u dw[4];
#pragma unroll
            for (int r = 0; r < 4; ++r) dw[r] = __builtin_amdgcn_raw_buffer_load_b128(rd, vod, sod + r * (DM * 2), WD_AUX);
            if (t == 1) {
                wait_ge(ctl + C_FFN, NW);
                const int l2 = fresh_lane();
                if (l2 < FS / 2) {
                    float g0 = 0.f, g1 = 0.f, u0 = 0.f, u1 = 0.f;
#pragma unroll
                    for (int ww = 0; ww < NW; ++ww) {
                        const f32x2 a = *(const LAS f32x2*)(L + GU_OFF + ww * 896 + 8 * l2), bb = *(const LAS f32x2*)(L + GU_OFF + ww * 896 + 448 + 8 * l2);
                        g0 += a[0]; g1 += a[1]; u0 += bb[0]; u1 += bb[1];
                    }
                    ((LAS unsigned*)(L + HH_OFF))[l2] = pack2(gelu_tanh(rbf(g0)) * rbf(u0), gelu_tanh(rbf(g1)) * rbf(u1));
                }
                LGKM0();
            }
            unsigned pf = 0u;
            if (CODE_PF2 && t == CODE_PF2 && w == 0 && lane < 16) pf = code_prefetch(lane);
            const v2u hs = *(const LAS v2u*)(L + HH_OFF + (28 * g + 4 * tt) * 2);
            asm volatile("s_waitcnt vmcnt(0)" : "+v"(dw[0]), "+v"(dw[1]), "+v"(dw[2]), "+v"(dw[3]), "+v"(pf) :: "memory");
            landed();
            STAMP(32 + t);
            slot_rows64(myslot, (const LAS unsigned char*)sc + t * 128, sel, c, g, acco, acco1);
#pragma unroll
            for (int r = 0; r < 4; ++r) accb = mfma(diagA(hs[r >> 1], sel.s[r & 1]), dw[r], accb);
        }
    }
    STAMP(6);
    acco += acco1;
    if (g < 2) *(LAS f32x4*)((LAS unsigned char*)sc + 1024 + (8 * c + 4 * g) * 4) = acco;
    if (lane == 0) { ((LAS float*)ctl)[C_WMAX + w] = mx; ((LAS float*)ctl)[C_WSUM + w] = lsum; }
    arrive(ctl + C_O, lane);
    wait_ge(ctl + C_O, NW);
    v4u of[4];
    {
        float m[NW], M = -INFINITY;
#pragma unroll
        for (int ww = 0; ww < NW; ++ww) { m[ww] = ((LAS float*)ctl)[C_WMAX + ww]; M = fmaxf(M, m[ww]); }
        float Ls = 0.f, s0 = 0.f, s1 = 0.f;
#pragma unroll
        for (int ww = 0; ww < NW; ++ww) {
            const float f = __expf(m[ww] - M);
            Ls += f * ((LAS float*)ctl)[C_WSUM + ww];
            const f32x2 v = *(const LAS f32x2*)(L + SC_OFF + ww * 2048 + 1024 + 8 * lane);
            s0 += f * v[0]; s1 += f * v[1];
        }
        const float inv = 1.0f / Ls;
        ((LAS unsigned*)(L + O_OFF))[lane] = pack2(s0 * inv, s1 * inv);
        LGKM0();
#pragma unroll
        for (int k = 0; k < 4; ++k) of[k] = *(const LAS v4u*)(L + O_OFF + 64 * g + 16 * k);
    }
    STAMP(7);
    f32x4 acca = {0.f, 0.f, 0.f, 0.f}, acca1 = {0.f, 0.f, 0.f, 0.f};
#pragma unroll
    for (int t = 0; t < 32; t += 2) {
        const unsigned S = of[t >> 3][(t >> 1) & 3];
        acca = mfma(diagA(S, sel.s[0]), wor[t], acca);
        acca1 = mfma(diagA(S, sel.s[1]), wor[t + 1], acca1);
    }
    acca += acca1;
    STAMP(40);
    {
        float z = 0.f;
#pragma unroll
        for (int ww = 0; ww < NW; ++ww) z += ((LAS float*)ctl)[C_GP + ww];
        z = rbf(z);
        const float gate = rbf(1.0f / (1.0f + __expf(-z)));
        const float omg = rbf(1.0f - gate);
        {
            const float r0 = gate * (g < 2 ? acca[0] : acca[2]) + omg * (g < 2 ? accb[0] : accb[2]);
            const float r1 = gate * (g < 2 ? acca[1] : acca[3]) + omg * (g < 2 ? accb[1] : accb[3]);
            store_dev(v4u{gran(r0, tag_lo), tag_hi, gran(r1, tag_lo), tag_hi}, rgran, gvo, gso);
        }
        const int l2 = fresh_lane();
        const int vo = ((l2 >> 4) * DM + 2 * (l2 & 15)) * 8, so = ((b * NH + 4 * w) * DM + 32 * h) * 8;
        v4u y;
        for (unsigned s = 0;; ++s) {
            y = __builtin_amdgcn_raw_buffer_load_b128(rgran, vo, so, 16);
            const unsigned bad = (y[1] ^ tag_hi) | (y[3] ^ tag_hi) | (((y[0] ^ tag_lo) | (y[2] ^ tag_lo)) & 0xffu);
            if (__all(bad == 0u) || s > SPIN_CAP) break;
            __builtin_amdgcn_s_sleep(1);
        }
        STAMP(41);
        float v0 = __builtin_bit_cast(float, y[0] & 0xffffff00u), v1 = __builtin_bit_cast(float, y[2] & 0xffffff00u);
        v0 += __shfl_xor(v0, 16); v1 += __shfl_xor(v1, 16);
        v0 += __shfl_xor(v0, 32); v1 += __shfl_xor(v1, 32);
        if (l2 < 16) *(LAS f32x2*)(L + GU_OFF + w * 128 + l2 * 8) = f32x2{v0, v1};
        LGKM0();
        unsigned old = 0u;
        if (l2 == 0) old = __hip_atomic_fetch_add(ctl + C_OUT, 1u, RLX_WG);
        if (rfl(old) == NW - 1) {
            CBAR();
            if (l2 < 16) {
                float s0 = 0.f, s1 = 0.f;
#pragma unroll
                for (int ww = 0; ww < NW; ++ww) { const f32x2 v = *(const LAS f32x2*)(L + GU_OFF + ww * 128 + l2 * 8); s0 += v[0]; s1 += v[1]; }
                __hip_atomic_store((gu32*)(out + ((size_t)b * DM + 32 * h + 2 * l2) * 2), pack2(s0, s1), __ATOMIC_RELAXED, __HIP_MEMORY_SCOPE_SYSTEM);
            }
            if (bh == 0 && l2 == 0) __hip_atomic_store((gu64*)(ws + WS_EPOCH), epoch, RLX_AGENT);
        }
        STAMP(57);
    }
    SDUMP();
    asm volatile(".Lfdk_end:");
}

}

extern "C" void kernel_launch(void* const* d_in, const int* in_sizes, int n_in, void* d_out, int out_size, void* d_ws, size_t ws_size,
                              hipStream_t stream) {
    (void)in_sizes; (void)n_in; (void)out_size; (void)ws_size;
    (void)hipFuncSetAttribute((const void*)fused_decode_kernel, hipFuncAttributeMaxDynamicSharedMemorySize, LDS_BYTES);
    int pace = PACE, tstart = TSTART, depth = DEPTH;
    fused_decode_kernel<<<dim3(NB * NH), dim3(NTHR), LDS_BYTES, stream>>>(
        (const unsigned char*)d_in[0], (const unsigned char*)d_in[1], (const unsigned char*)d_in[2], (const unsigned char*)d_in[3],
        (const unsigned char*)d_in[4], (const unsigned char*)d_in[6], (const unsigned char*)d_in[7],
        (const unsigned char*)d_in[8], (const unsigned char*)d_in[9], (unsigned char*)d_out, (unsigned char*)d_ws, pace, tstart, depth);
}
```

```cpp
#include <hip/hip_runtime.h>
#include <hip/hip_bf16.h>

#define GAS __attribute__((address_space(1)))
#define LAS __attribute__((address_space(3)))
typedef unsigned v4u __attribute__((ext_vector_type(4)));
typedef unsigned v2u __attribute__((ext_vector_type(2)));
typedef float f32x4 __attribute__((ext_vector_type(4)));
typedef float f32x2 __attribute__((ext_vector_type(2)));
typedef short bf16x8 __attribute__((ext_vector_type(8)));
typedef GAS unsigned gu32;
typedef GAS unsigned long long gu64;
#define RLX_AGENT __ATOMIC_RELAXED, __HIP_MEMORY_SCOPE_AGENT
#define RLX_WG __ATOMIC_RELAXED, __HIP_MEMORY_SCOPE_WORKGROUP

namespace {

constexpr int NB = 8, DM = 1024, NH = 32, SK = 4096, DH = 128, FF = 3584;
constexpr int FS = FF / NH;
constexpr int NW = 8, NTHR = 64 * NW;
constexpr int SLOT = 16384;
#define GUNT " nt"
#define ROTK 0
#define PRE 1
#define GU_T 2
#define WO_AUX 0
#define WD_AUX 0
#define POLL_SLEEP 1
#define QNT " nt"
#define CODE_PF 1
#define CODE_PF2 5
#define POLL_GAP 10
#define INVAL 0
#define PACE (16 * 61)
#define TSTART 600
#define DEPTH 4

constexpr int SC_OFF = NW * SLOT;
constexpr int GU_OFF = SC_OFF + NW * 2048;
constexpr int Q_OFF = GU_OFF + 14336;
constexpr int O_OFF = Q_OFF + 256;
constexpr int HH_OFF = O_OFF + 256;
constexpr int CTL_OFF = HH_OFF + 256;
constexpr int LDS_BYTES = CTL_OFF + 256;
constexpr int C_ISS = 0, C_LAND = 1, C_FFN = 2, C_O = 3, C_QP = 4, C_QR = 5, C_GU = 6, C_OUT = 7, C_TAG = 8, C_POLL = 10, C_SM = 11, C_WMAX = 16, C_WSUM = 24, C_GP = 32;
static_assert(LDS_BYTES <= 160 * 1024, "LDS");

constexpr size_t WS_EPOCH = 0, WS_GRAN = 4096, WS_QGRAN = WS_GRAN + (size_t)NB * NH * DM * 8, WS_GUGRAN = WS_QGRAN + (size_t)NH * NB * NB * DH * 8;

constexpr unsigned SPIN_CAP = 1u << 22;
#define STAMP(i) do {} while (0)
#define SDUMP() do {} while (0)

__device__ __forceinline__ f32x4 mfma(v4u a, v4u b, f32x4 c) {
    return __builtin_amdgcn_mfma_f32_16x16x32_bf16(__builtin_bit_cast(bf16x8, a), __builtin_bit_cast(bf16x8, b), c, 0, 0, 0);
}
__device__ __forceinline__ float bf_lo(unsigned u) { return __builtin_bit_cast(float, u << 16); }
__device__ __forceinline__ float bf_hi(unsigned u) { return __builtin_bit_cast(float, u & 0xffff0000u); }
__device__ __forceinline__ float rbf(float f) { return (float)(__bf16)f; }
__device__ __forceinline__ unsigned bf_bits(float f) { return (unsigned)__builtin_bit_cast(unsigned short, (__bf16)f); }
__device__ __forceinline__ unsigned pack2(float lo, float hi) { return bf_bits(lo) | (bf_bits(hi) << 16); }
__device__ __forceinline__ unsigned gran(float f, unsigned tag_lo) { return ((__builtin_bit_cast(unsigned, f) + 0x80u) & 0xffffff00u) | tag_lo; }
__device__ __forceinline__ void store_dev(v4u d, __amdgpu_buffer_rsrc_t r, int vo, int so) {
    __builtin_amdgcn_raw_buffer_store_b128(d, r, vo, so, 16);
    asm volatile("s_nop 1" ::"v"(d) : "memory");
}
__device__ __forceinline__ unsigned long long rt() { return __builtin_amdgcn_s_memrealtime(); }
__device__ __forceinline__ unsigned rfl(unsigned v) { return (unsigned)__builtin_amdgcn_readfirstlane((int)v); }
#define LGKM0() asm volatile("s_waitcnt lgkmcnt(0)" ::: "memory")
#define VMCNT(n) asm volatile("s_waitcnt vmcnt(%0)" ::"i"(n) : "memory")
#define CBAR() asm volatile("" ::: "memory")
#define PIN2(a, b) do { asm volatile("" : "+v"(a), "+v"(b)); __builtin_amdgcn_sched_barrier(0); } while (0)

__device__ __forceinline__ void glds4(const void* sbase, unsigned v0, unsigned v1, unsigned v2, unsigned v3, unsigned lds) {
    unsigned keep;
    asm volatile("s_mov_b32 %0, m0\n\ts_mov_b32 m0, %6\n\ts_nop 0\n\t"
                 "global_load_lds_dwordx4 %1, %5 nt\n\t"
                 "global_load_lds_dwordx4 %2, %5 offset:1024 nt\n\t"
                 "global_load_lds_dwordx4 %3, %5 offset:2048 nt\n\t"
                 "global_load_lds_dwordx4 %4, %5 offset:3072 nt\n\t"
                 "s_mov_b32 m0, %0"
                 : "=&s"(keep) : "v"(v0), "v"(v1), "v"(v2), "v"(v3), "s"(sbase), "s"(lds) : "memory");
}
__device__ __forceinline__ void fill(const unsigned char* src, unsigned v0, unsigned v1, unsigned v2, unsigned v3, unsigned lds) {
    LGKM0();
#pragma unroll
    for (int q = 0; q < 4; ++q) glds4(src + q * 4096, v0, v1, v2, v3, lds + q * 4096);
}

__device__ __forceinline__ void wait_ge(LAS unsigned* w, unsigned need) {
    for (unsigned s = 0; rfl(*(volatile LAS unsigned*)w) < need && s < SPIN_CAP; ++s) __builtin_amdgcn_s_sleep(0);
    CBAR();
}
__device__ __forceinline__ void arrive(LAS unsigned* w, int lane) {
    LGKM0();
    if (lane == 0) __hip_atomic_fetch_add(w, 1u, RLX_WG);
    CBAR();
}

struct Sel { unsigned s[2][4]; };
__device__ __forceinline__ v4u diagA(unsigned S, const unsigned (&sel)[4]) {
    return v4u{__builtin_amdgcn_perm(0u, S, sel[0]), __builtin_amdgcn_perm(0u, S, sel[1]), __builtin_amdgcn_perm(0u, S, sel[2]), __builtin_amdgcn_perm(0u, S, sel[3])};
}
__device__ __forceinline__ int fresh_lane() {
    int l;
    asm volatile("v_mbcnt_lo_u32_b32 %0, -1, 0\n\tv_mbcnt_hi_u32_b32 %0, -1, %0" : "=v"(l));
    __builtin_assume((unsigned)l < 64u);
    return l;
}
__device__ __forceinline__ Sel make_sel(int c) {
    Sel sel;
    const int dm = (c & 7) >> 1, hf = c & 1;
#pragma unroll
    for (int par = 0; par < 2; ++par)
#pragma unroll
        for (int d = 0; d < 4; ++d) {
            const unsigned lohi = (unsigned)(2 * par) | ((unsigned)(2 * par + 1) << 8);
            const unsigned v = hf ? (0x0c0cu | (lohi << 16)) : (0x0c0c0000u | lohi);
            sel.s[par][d] = (c < 8 && d == dm) ? v : 0x0c0c0c0cu;
        }
    return sel;
}

__device__ __forceinline__ void slot_rows64(const LAS unsigned char* slot, const LAS unsigned char* vec, const Sel& sel, int c, int g,
                                            f32x4& acc, f32x4& a1) {
    const LAS unsigned char* base = slot + g * 4096 + c * 16;
#pragma unroll
    for (int hf = 0; hf < 2; ++hf) {
        const v4u s = *(const LAS v4u*)(vec + g * 32 + hf * 16);
        v4u bb[8];
#pragma unroll
        for (int t = 0; t < 8; ++t) bb[t] = *(const LAS v4u*)(base + (8 * hf + t) * 256);
        __builtin_amdgcn_sched_barrier(0);
#pragma unroll
        for (int u = 0; u < 4; ++u) {
            acc = mfma(diagA(s[u], sel.s[0]), bb[2 * u], acc);
            a1 = mfma(diagA(s[u], sel.s[1]), bb[2 * u + 1], a1);
            PIN2(acc, a1);
        }
    }
}

__device__ __forceinline__ float gelu_tanh(float x) {
    const float y = 0.7978845608028654f * (x + 0.044715f * x * x * x);
    return rbf(x / (1.0f + __expf(-2.0f * y)));
}
__device__ __forceinline__ float wave_max(float v) {
#pragma unroll
    for (int o = 32; o >= 1; o >>= 1) v = fmaxf(v, __shfl_xor(v, o));
    return v;
}
__device__ __forceinline__ float wave_sum(float v) {
#pragma unroll
    for (int o = 32; o >= 1; o >>= 1) v += __shfl_xor(v, o);
    return v;
}

__global__ void __launch_bounds__(NTHR)
fused_decode_kernel(const unsigned char* __restrict__ xin, const unsigned char* __restrict__ kc, const unsigned char* __restrict__ vc,
                    const unsigned char* __restrict__ wq, const unsigned char* __restrict__ wo,
                    const unsigned char* __restrict__ wg, const unsigned char* __restrict__ wu, const unsigned char* __restrict__ wd,
                    const unsigned char* __restrict__ gatew, unsigned char* __restrict__ out, unsigned char* __restrict__ ws,
                    int pace, int tstart, int depth) {
    extern __shared__ __attribute__((aligned(16))) unsigned char smem[];
    LAS unsigned char* L = (LAS unsigned char*)smem;
    LAS unsigned* ctl = (LAS unsigned*)(L + CTL_OFF);
    asm volatile(".Lfdk_begin:");
    const unsigned long long t0 = rt();
    asm volatile("" ::"s"(xin), "s"(kc), "s"(vc), "s"(wq), "s"(wo), "s"(wg), "s"(wu), "s"(wd), "s"(gatew), "s"(out), "s"(ws), "s"(pace), "s"(tstart), "s"(depth));
    const int w = __builtin_amdgcn_readfirstlane((int)threadIdx.x >> 6);
    const int h = (blockIdx.x & 7) * 4 + ((blockIdx.x >> 3) & 3), b = blockIdx.x >> 5;
    const int fs = h, bh = b * NH + h;
    STAMP(0);
    const unsigned myslot_a = (unsigned)(size_t)L + w * SLOT;
    const LAS unsigned char* myslot = L + w * SLOT;
    const unsigned rot = (unsigned)(ROTK) + (w ^ 4);
    auto toff = [&](int t) { return (size_t)(((8u * t + rot) & 63u) * SLOT); };
    int lane, c, g;
    unsigned vk0, vk1, vk2, vk3;
    Sel sel;
    const unsigned char* kb;
    auto setup = [&]() {
        lane = fresh_lane(); c = lane & 15; g = lane >> 4;
        vk0 = g * 256 + ((c ^ g) << 4); vk1 = g * 256 + ((c ^ (4 + g)) << 4); vk2 = g * 256 + ((c ^ (8 + g)) << 4); vk3 = g * 256 + ((c ^ (12 + g)) << 4);
        sel = make_sel(c & 7);
        int bh2 = bh;
        asm volatile("" : "+s"(bh2));
        kb = kc + (size_t)bh2 * (SK * DH * 2);
    };
    auto code_prefetch = [&](int l) {
        unsigned long long pc;
        unsigned off, len, v = 0u;
        asm volatile("s_getpc_b64 %0\n.Lfdk_pc%=:\n\ts_mov_b32 %1, .Lfdk_pc%=-.Lfdk_begin\n\ts_mov_b32 %2, .Lfdk_end-.Lfdk_begin" : "=s"(pc), "=s"(off), "=s"(len));
        const unsigned o = (((blockIdx.x >> 3) & 31u) + 32u * (unsigned)l) * 128u;
        if (o < len) asm volatile("global_load_dword %0, %1, %2" : "+v"(v) : "v"(o), "s"(pc - off) : "memory");
        return v;
    };
    const int u = w ^ 4;
    auto take_turn = [&](int f) {
        const unsigned long long td = t0 + (unsigned)(tstart + (((f - 8) * pace) >> 4));
        while (rt() < td) __builtin_amdgcn_s_sleep(1);
        for (unsigned s = 0; s < SPIN_CAP; ++s) {
            const v2u st = *(volatile LAS v2u*)(ctl + C_ISS);
            if (rfl(st[0]) >= (unsigned)f && rfl(st[1]) + (unsigned)depth > (unsigned)f) break;
            __builtin_amdgcn_s_sleep(0);
        }
        CBAR();
    };
    auto issued = [&](int f) { if (lane == 0) *(volatile LAS unsigned*)(ctl + C_ISS) = (unsigned)f + 1u; CBAR(); };
    auto landed = [&]() { if (lane == 0) __hip_atomic_fetch_add(ctl + C_LAND, 1u, RLX_WG); CBAR(); };
    LAS float* sc = (LAS float*)(L + SC_OFF + w * 2048);
    unsigned long long epoch;

    if (__builtin_expect(w < 4, 1)) {
        v4u x8[4], wr[8];
        v2u ep;
        const unsigned l0 = threadIdx.x & 63;
        const unsigned char* qs = wq + (size_t)(128 * b + 32 * w) * (NH * DH * 2) + h * 256;
        const unsigned vq = (l0 >> 4) * 8 * (NH * DH * 2) + (l0 & 15) * 16;
#pragma unroll
        for (int t = 0; t < 8; ++t)
            asm volatile("global_load_dwordx4 %0, %1, %2" QNT : "=v"(wr[t]) : "v"(vq), "s"(qs + (size_t)t * (NH * DH * 2)) : "memory");
        __builtin_amdgcn_sched_barrier(0);
        const unsigned char* xs = xin + (128 * b + 32 * w) * 2;
        const unsigned vx = ((l0 >> 3) & 1) * (DM * 2) + (l0 >> 4) * 16;
#pragma unroll
        for (int j = 0; j < 4; ++j)
            asm volatile("global_load_dwordx4 %0, %1, %2" : "=v"(x8[j]) : "v"(vx), "s"(xs + j * (2 * DM * 2)) : "memory");
        asm volatile("global_load_dwordx2 %0, %1, %2 sc1" : "=v"(ep) : "v"(0u), "s"(ws + WS_EPOCH) : "memory");
        const unsigned char* inbox = ws + WS_QGRAN + (size_t)(h * NB + b) * NB * (DH * 8);
        unsigned warm;
        asm volatile("global_load_dword %0, %1, %2 sc1" : "=v"(warm) : "v"(l0 * 16), "s"(inbox) : "memory");
        STAMP(42);
        __builtin_amdgcn_s_barrier();
        setup();
        VMCNT(0);
        asm volatile("" : "+v"(x8[0]), "+v"(x8[1]), "+v"(x8[2]), "+v"(x8[3]), "+v"(wr[0]), "+v"(wr[1]), "+v"(wr[2]), "+v"(wr[3]),
                     "+v"(wr[4]), "+v"(wr[5]), "+v"(wr[6]), "+v"(wr[7]), "+v"(warm), "+v"(ep) :: "memory");
        STAMP(45);
        {
            const unsigned e0 = ep[0], e1 = ep[1];
            epoch = (((unsigned long long)rfl(e1) << 32) | rfl(e0)) + 1ull;
        }
        const unsigned qtag_hi = (unsigned)(epoch >> 8), qtag_lo = (unsigned)epoch & 0xffu;
#pragma unroll
        for (int j = 0; j < 4; ++j) {
            f32x4 a0 = {0.f, 0.f, 0.f, 0.f}, a1 = {0.f, 0.f, 0.f, 0.f};
#pragma unroll
            for (int k = 0; k < 4; ++k) {
                a0 = mfma(diagA(x8[j][k], sel.s[0]), wr[2 * k], a0);
                a1 = mfma(diagA(x8[j][k], sel.s[1]), wr[2 * k + 1], a1);
            }
            a0 += a1;
            *(LAS f32x4*)(L + SC_OFF + ((w * 8 + 2 * j + (g >> 1)) * 128 + 8 * c + 4 * (g & 1)) * 4) = a0;
        }
        if (w == 0 && lane == 0) *(LAS v2u*)(ctl + C_TAG) = v2u{(unsigned)epoch, (unsigned)(epoch >> 32)};
        arrive(ctl + C_QP, lane);
        wait_ge(ctl + C_QP, 4);
        STAMP(1);
        {
            const __amdgpu_buffer_rsrc_t rqg = __builtin_amdgcn_make_buffer_rsrc((void*)(ws + WS_QGRAN), 0, NH * NB * NB * DH * 8, 0x00020000);
#pragma unroll
            for (int r = 0; r < 2; ++r) {
                float s0 = 0.f, s1 = 0.f;
#pragma unroll
                for (int k = 0; k < 4; ++k) { const f32x2 v = *(const LAS f32x2*)(L + SC_OFF + ((k * 8 + 2 * w + r) * 128 + 2 * lane) * 4); s0 += v[0]; s1 += v[1]; }
                store_dev(v4u{gran(s0, qtag_lo), qtag_hi, gran(s1, qtag_lo), qtag_hi}, rqg, lane * 16, ((h * NB + 2 * w + r) * NB + b) * (DH * 8));
            }
        }
        arrive(ctl + C_QP, lane);
        STAMP(46);
        if (w == 3) {
            VMCNT(0);
            STAMP(55);
            const unsigned vo = lane * 16;
            v4u y[NB];
            for (unsigned s = 0;; ++s) {
#pragma unroll
                for (int p = 0; p < NB; ++p)
                    asm volatile("global_load_dwordx4 %0, %1, %2 offset:%3 sc1" : "=v"(y[p]) : "v"(vo), "s"(inbox + (p >> 2) * 4096), "i"((p & 3) * 1024) : "memory");
                if (s == 0) {
                    if (lane == 0) *(volatile LAS unsigned*)(ctl + C_POLL) = 1u;
                    fill(kb + toff(0), vk0, vk1, vk2, vk3, myslot_a);
                    VMCNT(16);
                } else {
                    VMCNT(0);
                }
                asm volatile("" : "+v"(y[0]), "+v"(y[1]), "+v"(y[2]), "+v"(y[3]), "+v"(y[4]), "+v"(y[5]), "+v"(y[6]), "+v"(y[7]) :: "memory");
                unsigned bad = 0u;
#pragma unroll
                for (int p = 0; p < NB; ++p) bad |= (y[p][1] ^ qtag_hi) | (y[p][3] ^ qtag_hi) | (((y[p][0] ^ qtag_lo) | (y[p][2] ^ qtag_lo)) & 0xffu);
                if (__all(bad == 0u) || s > SPIN_CAP) break;
                __builtin_amdgcn_s_sleep(POLL_SLEEP);
            }
            float s0 = 0.f, s1 = 0.f;
#pragma unroll
            for (int p = 0; p < NB; ++p) { s0 += __builtin_bit_cast(float, y[p][0] & 0xffffff00u); s1 += __builtin_bit_cast(float, y[p][2] & 0xffffff00u); }
            ((LAS unsigned*)(L + Q_OFF))[lane] = pack2(s0, s1);
            wait_ge(ctl + C_QP, 8);
            arrive(ctl + C_QR, lane);
        } else {
            if (w >= PRE) wait_ge(ctl + C_POLL, 1);
            fill(kb + toff(0), vk0, vk1, vk2, vk3, myslot_a);
        }
        STAMP(47);
        VMCNT(0);
        landed();
    } else {
        setup();
        if (w == 4 && lane < 16) ctl[lane] = lane == C_ISS ? 8u : 0u;
        unsigned pf = 0u;
        if (CODE_PF && w == 5 && lane < 16) pf = code_prefetch(lane);
        LGKM0();
        __builtin_amdgcn_s_barrier();
        fill(kb + toff(0), vk0, vk1, vk2, vk3, myslot_a);
        STAMP(43);
        asm volatile("s_waitcnt vmcnt(0)" : "+v"(pf)::"memory");
        landed();
        STAMP(2);
        wait_ge(ctl + C_QP, 4);
        const v2u e = *(volatile LAS v2u*)(ctl + C_TAG);
        const unsigned e0 = e[0], e1 = e[1];
        epoch = ((unsigned long long)rfl(e1) << 32) | rfl(e0);
    }
    const unsigned tag_hi = (unsigned)(epoch >> 8), tag_lo = (unsigned)epoch & 0xffu;
    const unsigned char* vb = vc + (size_t)bh * (SK * DH * 2);
    const __amdgpu_buffer_rsrc_t rgran = __builtin_amdgcn_make_buffer_rsrc((void*)(ws + WS_GRAN), 0, NB * NH * DM * 8, 0x00020000);
    const int gvo = (8 * c + 4 * (g & 1) + 2 * (g >> 1)) * 8, gso = bh * (DM * 8) + w * 1024;
    wait_ge(ctl + C_QR, 1);
    v4u qf[4];
#pragma unroll
    for (int s = 0; s < 4; ++s) qf[s] = *(const LAS v4u*)(L + Q_OFF + (32 * s + 8 * g) * 2);
    LGKM0();
    STAMP(3);

    float mx = -INFINITY;
    v4u wor[32];
    const __amdgpu_buffer_rsrc_t ro = __builtin_amdgcn_make_buffer_rsrc((void*)wo, 0, DM * NH * DH * 2, 0x00020000);
    const int soo = h * DH * (DM * 2) + w * 256;
#pragma unroll
    for (int t = 0; t < 8; ++t) {
        if (t > 0) { take_turn(8 * t + u); fill(kb + toff(t), vk0, vk1, vk2, vk3, myslot_a); issued(8 * t + u); }
        STAMP(8 + t);
        v4u xg[4][2], gw[16];
        if (t > 0) {
            const int l2 = fresh_lane();
            const int voo = 32 * (l2 >> 4) * (DM * 2) + (l2 & 15) * 16;
            v4u* wp = wor + 4 * (t - 1);
#pragma unroll
            for (int r = 0; r < 4; ++r) wp[r] = __builtin_amdgcn_raw_buffer_load_b128(ro, voo, soo + (4 * (t - 1) + r) * (DM * 2), WO_AUX);
            if (t == 1) {
                const __amdgpu_buffer_rsrc_t rx = __builtin_amdgcn_make_buffer_rsrc((void*)xin, 0, NB * DM * 2, 0x00020000);
                const __amdgpu_buffer_rsrc_t rw = __builtin_amdgcn_make_buffer_rsrc((void*)gatew, 0, DM * 2, 0x00020000);
                v4u xv = __builtin_amdgcn_raw_buffer_load_b128(rx, (l2 & 15) * 16, b * (DM * 2) + w * 256, 0);
                v4u gv = __builtin_amdgcn_raw_buffer_load_b128(rw, (l2 & 15) * 16, w * 256, 0);
                asm volatile("s_waitcnt vmcnt(0)" : "+v"(wp[0]), "+v"(wp[1]), "+v"(wp[2]), "+v"(wp[3]), "+v"(xv), "+v"(gv) :: "memory");
                landed();
                if (INVAL) store_dev(v4u{tag_lo ^ 1u, ~tag_hi, tag_lo ^ 1u, ~tag_hi}, rgran, gvo, gso);
                float s = 0.f;
#pragma unroll
                for (int k = 0; k < 4; ++k) s += rbf(bf_lo(xv[k]) * bf_lo(gv[k])) + rbf(bf_hi(xv[k]) * bf_hi(gv[k]));
                s = wave_sum(l2 < 16 ? s : 0.f);
                if (l2 == 0) ((LAS float*)ctl)[C_GP + w] = s;
            } else if (t == GU_T && w >= 4) {
                const int m = (w >> 1) & 1, hf = w & 1;
                const unsigned char* gs = (m ? wu : wg) + (size_t)(128 * b + 64 * hf) * (FF * 2) + fs * (FS * 2);
                const unsigned vog = 16 * (l2 >> 4) * (FF * 2) + ((l2 & 15) < 14 ? (l2 & 15) : 13) * 16;
#pragma unroll
                for (int k = 0; k < 16; ++k)
                    asm volatile("global_load_dwordx4 %0, %1, %2" GUNT : "=v"(gw[k]) : "v"(vog), "s"(gs + (size_t)k * (FF * 2)) : "memory");
                const unsigned char* xs = xin + (128 * b + 64 * hf) * 2;
                const unsigned vox = ((l2 & 15) >> 3) * (DM * 2) + (l2 >> 4) * 32;
#pragma unroll
                for (int j = 0; j < 4; ++j) {
                    asm volatile("global_load_dwordx4 %0, %1, %2" : "=v"(xg[j][0]) : "v"(vox), "s"(xs + j * (2 * DM * 2)) : "memory");
                    asm volatile("global_load_dwordx4 %0, %1, %2 offset:16" : "=v"(xg[j][1]) : "v"(vox), "s"(xs + j * (2 * DM * 2)) : "memory");
                }
                VMCNT(24);
            } else if (t == GU_T + 1 && w >= 4) {
                asm volatile("s_waitcnt vmcnt(0)" : "+v"(wp[0]), "+v"(wp[1]), "+v"(wp[2]), "+v"(wp[3]) :: "memory");
                landed();
                wait_ge(ctl + C_GU, 4);
                const __amdgpu_buffer_rsrc_t rgg = __builtin_amdgcn_make_buffer_rsrc((void*)(ws + WS_GUGRAN), 0, NH * NB * NB * 2048, 0x00020000);
#pragma unroll
                for (int r = 0; r < 2; ++r)
#pragma unroll
                    for (int q = 0; q < 2; ++q) {
                        const int p = q == 0 ? l2 : (l2 < 48 ? 64 + l2 : 111);
                        const int mm = p >= 56 ? 1 : 0, col = 2 * (p - 56 * mm), cons = 2 * (w - 4) + r;
                        const f32x2 v0 = *(const LAS f32x2*)(L + GU_OFF + (((2 * mm) * 8 + cons) * FS + col) * 4);
                        const f32x2 v1 = *(const LAS f32x2*)(L + GU_OFF + (((2 * mm + 1) * 8 + cons) * FS + col) * 4);
                        const float s0 = v0[0] + v1[0], s1 = v0[1] + v1[1];
                        if (q == 0 || l2 < 48)
                            store_dev(v4u{gran(s0, tag_lo), tag_hi, gran(s1, tag_lo), tag_hi}, rgg, p * 16, ((h * NB + cons) * NB + b) * 2048);
                    }
                arrive(ctl + C_GU, l2);
            } else if (t == 7) {
                const __amdgpu_buffer_rsrc_t rgg = __builtin_amdgcn_make_buffer_rsrc((void*)(ws + WS_GUGRAN), 0, NH * NB * NB * 2048, 0x00020000);
                const int sgg = ((h * NB + b) * NB + w) * 2048, v1o = 1024 + (l2 < 48 ? l2 : 47) * 16;
                v4u y0, y1;
                for (unsigned s = 0;; ++s) {
                    y0 = __builtin_amdgcn_raw_buffer_load_b128(rgg, l2 * 16, sgg, 16);
                    y1 = __builtin_amdgcn_raw_buffer_load_b128(rgg, v1o, sgg, 16);
                    asm volatile("s_waitcnt vmcnt(0)" : "+v"(wp[0]), "+v"(wp[1]), "+v"(wp[2]), "+v"(wp[3]), "+v"(y0), "+v"(y1) :: "memory");
                    const unsigned bad = (y0[1] ^ tag_hi) | (y0[3] ^ tag_hi) | (y1[1] ^ tag_hi) | (y1[3] ^ tag_hi) | (((y0[0] ^ tag_lo) | (y0[2] ^ tag_lo) | (y1[0] ^ tag_lo) | (y1[2] ^ tag_lo)) & 0xffu);
                    if (s == 0) landed();
                    if (__all(bad == 0u) || s > SPIN_CAP) break;
                    __builtin_amdgcn_s_sleep(1);
                }
                wait_ge(ctl + C_GU, 8);
                *(LAS v2u*)(L + GU_OFF + w * 896 + l2 * 8) = v2u{y0[0] & 0xffffff00u, y0[2] & 0xffffff00u};
                if (l2 < 48) *(LAS v2u*)(L + GU_OFF + w * 896 + 512 + l2 * 8) = v2u{y1[0] & 0xffffff00u, y1[2] & 0xffffff00u};
                arrive(ctl + C_FFN, l2);
            } else {
                asm volatile("s_waitcnt vmcnt(0)" : "+v"(wp[0]), "+v"(wp[1]), "+v"(wp[2]), "+v"(wp[3]) :: "memory");
            }
            if (t != 7 && t != 1 && !(t == GU_T + 1 && w >= 4)) landed();
        }
        STAMP(16 + t);
        f32x4 s4[4];
#pragma unroll
        for (int kg = 0; kg < 4; ++kg) {
            f32x4 a = {0.f, 0.f, 0.f, 0.f};
#pragma unroll
            for (int s = 0; s < 4; ++s) {
                const v4u kf = *(const LAS v4u*)(myslot + (16 * kg + c) * 256 + (((4 * s + g) ^ c) << 4));
                a = mfma(kf, qf[s], a);
            }
            s4[kg] = a;
        }
        f32x4 v;
#pragma unroll
        for (int i = 0; i < 4; ++i) {
            const float lo = (c & 1) ? s4[1][i] : s4[0][i], hi = (c & 1) ? s4[3][i] : s4[2][i];
            v[i] = rbf(rbf((c & 2) ? hi : lo) * 0.08838834764831845f);
            mx = fmaxf(mx, v[i]);
        }
        if (c < 4) *(LAS f32x4*)(sc + t * 64 + 16 * c + 4 * g) = v;
        __builtin_amdgcn_sched_barrier(0);
        if (t == GU_T && w >= 4) {
            v4u* wp = wor + 4 * (t - 1);
            asm volatile("s_waitcnt vmcnt(0)" : "+v"(gw[0]), "+v"(gw[1]), "+v"(gw[2]), "+v"(gw[3]), "+v"(gw[4]), "+v"(gw[5]), "+v"(gw[6]), "+v"(gw[7]),
                         "+v"(gw[8]), "+v"(gw[9]), "+v"(gw[10]), "+v"(gw[11]), "+v"(gw[12]), "+v"(gw[13]), "+v"(gw[14]), "+v"(gw[15]),
                         "+v"(xg[0][0]), "+v"(xg[0][1]), "+v"(xg[1][0]), "+v"(xg[1][1]), "+v"(xg[2][0]), "+v"(xg[2][1]), "+v"(xg[3][0]), "+v"(xg[3][1]) :: "memory");
            asm volatile("" : "+v"(wp[0]), "+v"(wp[1]), "+v"(wp[2]), "+v"(wp[3]));
            STAMP(44);
            f32x4 a[4];
#pragma unroll
            for (int j = 0; j < 4; ++j) a[j] = f32x4{0.f, 0.f, 0.f, 0.f};
#pragma unroll
            for (int k = 0; k < 16; ++k) {
#pragma unroll
                for (int j = 0; j < 4; ++j) a[j] = mfma(diagA(xg[j][k >> 3][(k >> 1) & 3], sel.s[k & 1]), gw[k], a[j]);
                PIN2(a[0], a[1]); PIN2(a[2], a[3]);
            }
            if (c < 14) {
#pragma unroll
                for (int j = 0; j < 4; ++j)
                    *(LAS f32x4*)(L + GU_OFF + ((((w - 4) * 8 + 2 * j + (g >> 1)) * FS) + 8 * c + 4 * (g & 1)) * 4) = a[j];
            }
            arrive(ctl + C_GU, lane);
            STAMP(56);
        }
    }
    STAMP(4);
    float lsum;
    mx = wave_max(mx);
    {
        const f32x4 sa = *(const LAS f32x4*)(sc + 8 * lane), sb = *(const LAS f32x4*)(sc + 8 * lane + 4);
        float e[8];
#pragma unroll
        for (int i = 0; i < 4; ++i) { e[i] = __expf(sa[i] - mx); e[4 + i] = __expf(sb[i] - mx); }
        lsum = wave_sum(((e[0] + e[1]) + (e[2] + e[3])) + ((e[4] + e[5]) + (e[6] + e[7])));
        v4u pk;
#pragma unroll
        for (int i = 0; i < 4; ++i) pk[i] = pack2(e[2 * i], e[2 * i + 1]);
        LGKM0();
        *(LAS v4u*)((LAS unsigned char*)sc + 16 * lane) = pk;
    }
    if (lane == 0) { ((LAS float*)ctl)[C_WMAX + w] = mx; ((LAS float*)ctl)[C_WSUM + w] = lsum; }
    arrive(ctl + C_SM, lane);
    STAMP(5);

    f32x4 acco = {0.f, 0.f, 0.f, 0.f}, acco1 = {0.f, 0.f, 0.f, 0.f}, accb = {0.f, 0.f, 0.f, 0.f};
    float fw = 0.f, gate = 0.f;
    {
        const unsigned vlin = lane * 16;
        {
            take_turn(64 + u);
            fill(vb + toff(0), vlin, vlin, vlin, vlin, myslot_a);
            issued(64 + u);
            STAMP(24);
            const int voo = 32 * g * (DM * 2) + c * 16;
#pragma unroll
            for (int r = 0; r < 4; ++r) wor[28 + r] = __builtin_amdgcn_raw_buffer_load_b128(ro, voo, soo + (28 + r) * (DM * 2), WO_AUX);
            asm volatile("s_waitcnt vmcnt(0)" : "+v"(wor[28]), "+v"(wor[29]), "+v"(wor[30]), "+v"(wor[31]) :: "memory");
            landed();
            STAMP(32);
            slot_rows64(myslot, (const LAS unsigned char*)sc, sel, c, g, acco, acco1);
        }
        const __amdgpu_buffer_rsrc_t rd = __builtin_amdgcn_make_buffer_rsrc((void*)wd, 0, FF * DM * 2, 0x00020000);
        const int vod = 28 * g * (DM * 2) + c * 16;
#pragma unroll 1
        for (int t = 1; t < 8; ++t) {
            take_turn(64 + 8 * t + u);
            fill(vb + toff(t), vlin, vlin, vlin, vlin, myslot_a);
            issued(64 + 8 * t + u);
            STAMP(24 + t);
            const int tt = t - 1;
            const int sod = (fs * FS + 4 * tt) * (DM * 2) + w * 256;
            v4u dw[4];
#pragma unroll
            for (int r = 0; r < 4; ++r) dw[r] = __builtin_amdgcn_raw_buffer_load_b128(rd, vod, sod + r * (DM * 2), WD_AUX);
            if (t == 1) {
                wait_ge(ctl + C_FFN, NW);
                const int l2 = fresh_lane();
                if (l2 < FS / 2) {
                    float g0 = 0.f, g1 = 0.f, u0 = 0.f, u1 = 0.f;
#pragma unroll
                    for (int ww = 0; ww < NW; ++ww) {
                        const f32x2 a = *(const LAS f32x2*)(L + GU_OFF + ww * 896 + 8 * l2), bb = *(const LAS f32x2*)(L + GU_OFF + ww * 896 + 448 + 8 * l2);
                        g0 += a[0]; g1 += a[1]; u0 += bb[0]; u1 += bb[1];
                    }
                    ((LAS unsigned*)(L + HH_OFF))[l2] = pack2(gelu_tanh(rbf(g0)) * rbf(u0), gelu_tanh(rbf(g1)) * rbf(u1));
                }
                wait_ge(ctl + C_SM, NW);
                float m[NW], M = -INFINITY, Ls = 0.f, z = 0.f;
#pragma unroll
                for (int ww = 0; ww < NW; ++ww) { m[ww] = ((LAS float*)ctl)[C_WMAX + ww]; M = fmaxf(M, m[ww]); }
#pragma unroll
                for (int ww = 0; ww < NW; ++ww) { Ls += __expf(m[ww] - M) * ((LAS float*)ctl)[C_WSUM + ww]; z += ((LAS float*)ctl)[C_GP + ww]; }
                fw = __expf(mx - M) / Ls;
                gate = rbf(1.0f / (1.0f + __expf(-rbf(z))));
                LGKM0();
            }
            unsigned pf = 0u;
            if (CODE_PF2 && t == CODE_PF2 && w == 0 && lane < 16) pf = code_prefetch(lane);
            const v2u hs = *(const LAS v2u*)(L + HH_OFF + (28 * g + 4 * tt) * 2);
            asm volatile("s_waitcnt vmcnt(0)" : "+v"(dw[0]), "+v"(dw[1]), "+v"(dw[2]), "+v"(dw[3]), "+v"(pf) :: "memory");
            landed();
            STAMP(32 + t);
            slot_rows64(myslot, (const LAS unsigned char*)sc + t * 128, sel, c, g, acco, acco1);
#pragma unroll
            for (int r = 0; r < 4; ++r) accb = mfma(diagA(hs[r >> 1], sel.s[r & 1]), dw[r], accb);
        }
    }
    STAMP(6);
    acco = (acco + acco1) * fw;
    if (g < 2) *(LAS f32x4*)((LAS unsigned char*)sc + 1024 + (8 * c + 4 * g) * 4) = acco;
    arrive(ctl + C_O, lane);
    wait_ge(ctl + C_O, NW);
    v4u of[4];
    {
        float s0 = 0.f, s1 = 0.f;
#pragma unroll
        for (int ww = 0; ww < NW; ++ww) {
            const f32x2 v = *(const LAS f32x2*)(L + SC_OFF + ww * 2048 + 1024 + 8 * lane);
            s0 += v[0]; s1 += v[1];
        }
        ((LAS unsigned*)(L + O_OFF))[lane] = pack2(s0, s1);
        LGKM0();
#pragma unroll
        for (int k = 0; k < 4; ++k) of[k] = *(const LAS v4u*)(L + O_OFF + 64 * g + 16 * k);
    }
    STAMP(7);
    f32x4 acca, ac[4];
#pragma unroll
    for (int k = 0; k < 4; ++k) ac[k] = f32x4{0.f, 0.f, 0.f, 0.f};
#pragma unroll
    for (int t = 0; t < 32; t += 4) {
#pragma unroll
        for (int k = 0; k < 4; ++k) ac[k] = mfma(diagA(of[t >> 3][((t + k) >> 1) & 3], sel.s[k & 1]), wor[t + k], ac[k]);
    }
    acca = (ac[0] + ac[1]) + (ac[2] + ac[3]);
    STAMP(40);
    {
        const float omg = rbf(1.0f - gate);
        {
            const float r0 = gate * (g < 2 ? acca[0] : acca[2]) + omg * (g < 2 ? accb[0] : accb[2]);
            const float r1 = gate * (g < 2 ? acca[1] : acca[3]) + omg * (g < 2 ? accb[1] : accb[3]);
            store_dev(v4u{gran(r0, tag_lo), tag_hi, gran(r1, tag_lo), tag_hi}, rgran, gvo, (int)rfl((unsigned)gso));
        }
        const int l2 = fresh_lane();
        const int vo = ((l2 >> 4) * DM + 2 * (l2 & 15)) * 8, so = ((b * NH + 4 * w) * DM + 32 * h) * 8;
        v4u y, ya, yb;
        auto poll = [&](v4u& d) { asm volatile("buffer_load_dwordx4 %0, %1, %2, %3 offen sc1" : "=v"(d) : "v"(vo), "s"(rgran), "s"(so) : "memory"); };
        auto good = [&](const v4u& d) { return __all(((d[1] ^ tag_hi) | (d[3] ^ tag_hi) | (((d[0] ^ tag_lo) | (d[2] ^ tag_lo)) & 0xffu)) == 0u); };
        poll(ya);
        __builtin_amdgcn_s_sleep(POLL_GAP);
        poll(yb);
        for (unsigned s = 0;; ++s) {
            asm volatile("s_waitcnt vmcnt(1)" : "+v"(ya)::"memory");
            if (good(ya) || s > SPIN_CAP) { y = ya; break; }
            poll(ya);
            asm volatile("s_waitcnt vmcnt(1)" : "+v"(yb)::"memory");
            if (good(yb)) { y = yb; break; }
            poll(yb);
        }
        STAMP(41);
        float v0 = __builtin_bit_cast(float, y[0] & 0xffffff00u), v1 = __builtin_bit_cast(float, y[2] & 0xffffff00u);
        v0 += __shfl_xor(v0, 16); v1 += __shfl_xor(v1, 16);
        v0 += __shfl_xor(v0, 32); v1 += __shfl_xor(v1, 32);
        if (l2 < 16) *(LAS f32x2*)(L + GU_OFF + w * 128 + l2 * 8) = f32x2{v0, v1};
        LGKM0();
        unsigned old = 0u;
        if (l2 == 0) old = __hip_atomic_fetch_add(ctl + C_OUT, 1u, RLX_WG);
        if (rfl(old) == NW - 1) {
            CBAR();
            if (l2 < 16) {
                float s0 = 0.f, s1 = 0.f;
#pragma unroll
                for (int ww = 0; ww < NW; ++ww) { const f32x2 v = *(const LAS f32x2*)(L + GU_OFF + ww * 128 + l2 * 8); s0 += v[0]; s1 += v[1]; }
                __hip_atomic_store((gu32*)(out + ((size_t)b * DM + 32 * h + 2 * l2) * 2), pack2(s0, s1), __ATOMIC_RELAXED, __HIP_MEMORY_SCOPE_SYSTEM);
            }
            if (bh == 0 && l2 == 0) __hip_atomic_store((gu64*)(ws + WS_EPOCH), epoch, RLX_AGENT);
        }
        STAMP(57);
        asm volatile("" ::"v"(ya), "v"(yb));
    }
    SDUMP();
    asm volatile(".Lfdk_end:");
}

}

extern "C" void kernel_launch(void* const* d_in, const int* in_sizes, int n_in, void* d_out, int out_size, void* d_ws, size_t ws_size,
                              hipStream_t stream) {
    (void)in_sizes; (void)n_in; (void)out_size; (void)ws_size;
    (void)hipFuncSetAttribute((const void*)fused_decode_kernel, hipFuncAttributeMaxDynamicSharedMemorySize, LDS_BYTES);
    int pace = PACE, tstart = TSTART, depth = DEPTH;
    fused_decode_kernel<<<dim3(NB * NH), dim3(NTHR), LDS_BYTES, stream>>>(
        (const unsigned char*)d_in[0], (const unsigned char*)d_in[1], (const unsigned char*)d_in[2], (const unsigned char*)d_in[3],
        (const unsigned char*)d_in[4], (const unsigned char*)d_in[6], (const unsigned char*)d_in[7],
        (const unsigned char*)d_in[8], (const unsigned char*)d_in[9], (unsigned char*)d_out, (unsigned char*)d_ws, pace, tstart, depth);
}
```

```cpp
#define PACE (16*62)
#include <hip/hip_runtime.h>
#include <hip/hip_bf16.h>

#define GAS __attribute__((address_space(1)))
#define LAS __attribute__((address_space(3)))
typedef unsigned v4u __attribute__((ext_vector_type(4)));
typedef unsigned v2u __attribute__((ext_vector_type(2)));
typedef float f32x4 __attribute__((ext_vector_type(4)));
typedef float f32x2 __attribute__((ext_vector_type(2)));
typedef short bf16x8 __attribute__((ext_vector_type(8)));
typedef GAS unsigned gu32;
typedef GAS unsigned long long gu64;
#define RLX_AGENT __ATOMIC_RELAXED, __HIP_MEMORY_SCOPE_AGENT
#define RLX_WG __ATOMIC_RELAXED, __HIP_MEMORY_SCOPE_WORKGROUP

namespace {

constexpr int NB = 8, DM = 1024, NH = 32, SK = 4096, DH = 128, FF = 3584;
constexpr int FS = FF / NH;
constexpr int NW = 8, NTHR = 64 * NW;
constexpr int SLOT = 16384;
#define GUNT " nt"
#define ROTK 0
#define PRE 1
#define GU_T 2
#define WO_AUX 0
#define WD_AUX 0
#define POLL_SLEEP 1
#define QNT " nt"
#define CODE_PF 1
#define CODE_PF2 5
#define POLL_GAP 10
#define INVAL 0
#define TSTART 600
#define DEPTH 4

constexpr int SC_OFF = NW * SLOT;
constexpr int GU_OFF = SC_OFF + NW * 2048;
constexpr int Q_OFF = GU_OFF + 14336;
constexpr int O_OFF = Q_OFF + 256;
constexpr int HH_OFF = O_OFF + 256;
constexpr int CTL_OFF = HH_OFF + 256;
constexpr int LDS_BYTES = CTL_OFF + 256;
constexpr int C_ISS = 0, C_LAND = 1, C_FFN = 2, C_O = 3, C_QP = 4, C_QR = 5, C_GU = 6, C_OUT = 7, C_TAG = 8, C_POLL = 10, C_SM = 11, C_WMAX = 16, C_WSUM = 24, C_GP = 32;
static_assert(LDS_BYTES <= 160 * 1024, "LDS");

constexpr size_t WS_EPOCH = 0, WS_GRAN = 4096, WS_QGRAN = WS_GRAN + (size_t)NB * NH * DM * 8, WS_GUGRAN = WS_QGRAN + (size_t)NH * NB * NB * DH * 8;

constexpr unsigned SPIN_CAP = 1u << 22;
#define STAMP(i) do {} while (0)
#define SDUMP() do {} while (0)

__device__ __forceinline__ f32x4 mfma(v4u a, v4u b, f32x4 c) {
    return __builtin_amdgcn_mfma_f32_16x16x32_bf16(__builtin_bit_cast(bf16x8, a), __builtin_bit_cast(bf16x8, b), c, 0, 0, 0);
}
__device__ __forceinline__ float bf_lo(unsigned u) { return __builtin_bit_cast(float, u << 16); }
__device__ __forceinline__ float bf_hi(unsigned u) { return __builtin_bit_cast(float, u & 0xffff0000u); }
__device__ __forceinline__ float rbf(float f) { return (float)(__bf16)f; }
__device__ __forceinline__ unsigned bf_bits(float f) { return (unsigned)__builtin_bit_cast(unsigned short, (__bf16)f); }
__device__ __forceinline__ unsigned pack2(float lo, float hi) { return bf_bits(lo) | (bf_bits(hi) << 16); }
__device__ __forceinline__ unsigned gran(float f, unsigned tag_lo) { return ((__builtin_bit_cast(unsigned, f) + 0x80u) & 0xffffff00u) | tag_lo; }
__device__ __forceinline__ void store_dev(v4u d, __amdgpu_buffer_rsrc_t r, int vo, int so) {
    __builtin_amdgcn_raw_buffer_store_b128(d, r, vo, so, 16);
    asm volatile("s_nop 1" ::"v"(d) : "memory");
}
__device__ __forceinline__ unsigned long long rt() { return __builtin_amdgcn_s_memrealtime(); }
__device__ __forceinline__ unsigned rfl(unsigned v) { return (unsigned)__builtin_amdgcn_readfirstlane((int)v); }
#define LGKM0() asm volatile("s_waitcnt lgkmcnt(0)" ::: "memory")
#define VMCNT(n) asm volatile("s_waitcnt vmcnt(%0)" ::"i"(n) : "memory")
#define CBAR() asm volatile("" ::: "memory")
#define PIN2(a, b) do { asm volatile("" : "+v"(a), "+v"(b)); __builtin_amdgcn_sched_barrier(0); } while (0)

__device__ __forceinline__ void glds4(const void* sbase, unsigned v0, unsigned v1, unsigned v2, unsigned v3, unsigned lds) {
    unsigned keep;
    asm volatile("s_mov_b32 %0, m0\n\ts_mov_b32 m0, %6\n\ts_nop 0\n\t"
                 "global_load_lds_dwordx4 %1, %5 nt\n\t"
                 "global_load_lds_dwordx4 %2, %5 offset:1024 nt\n\t"
                 "global_load_lds_dwordx4 %3, %5 offset:2048 nt\n\t"
                 "global_load_lds_dwordx4 %4, %5 offset:3072 nt\n\t"
                 "s_mov_b32 m0, %0"
                 : "=&s"(keep) : "v"(v0), "v"(v1), "v"(v2), "v"(v3), "s"(sbase), "s"(lds) : "memory");
}
__device__ __forceinline__ void fill(const unsigned char* src, unsigned v0, unsigned v1, unsigned v2, unsigned v3, unsigned lds) {
    LGKM0();
#pragma unroll
    for (int q = 0; q < 4; ++q) glds4(src + q * 4096, v0, v1, v2, v3, lds + q * 4096);
}

__device__ __forceinline__ void wait_ge(LAS unsigned* w, unsigned need) {
    for (unsigned s = 0; rfl(*(volatile LAS unsigned*)w) < need && s < SPIN_CAP; ++s) __builtin_amdgcn_s_sleep(0);
    CBAR();
}
__device__ __forceinline__ void arrive(LAS unsigned* w, int lane) {
    LGKM0();
    if (lane == 0) __hip_atomic_fetch_add(w, 1u, RLX_WG);
    CBAR();
}

struct Sel { unsigned s[2][4]; };
__device__ __forceinline__ v4u diagA(unsigned S, const unsigned (&sel)[4]) {
    return v4u{__builtin_amdgcn_perm(0u, S, sel[0]), __builtin_amdgcn_perm(0u, S, sel[1]), __builtin_amdgcn_perm(0u, S, sel[2]), __builtin_amdgcn_perm(0u, S, sel[3])};
}
__device__ __forceinline__ int fresh_lane() {
    int l;
    asm volatile("v_mbcnt_lo_u32_b32 %0, -1, 0\n\tv_mbcnt_hi_u32_b32 %0, -1, %0" : "=v"(l));
    __builtin_assume((unsigned)l < 64u);
    return l;
}
__device__ __forceinline__ Sel make_sel(int c) {
    Sel sel;
    const int dm = (c & 7) >> 1, hf = c & 1;
#pragma unroll
    for (int par = 0; par < 2; ++par)
#pragma unroll
        for (int d = 0; d < 4; ++d) {
            const unsigned lohi = (unsigned)(2 * par) | ((unsigned)(2 * par + 1) << 8);
            const unsigned v = hf ? (0x0c0cu | (lohi << 16)) : (0x0c0c0000u | lohi);
            sel.s[par][d] = (c < 8 && d == dm) ? v : 0x0c0c0c0cu;
        }
    return sel;
}

template <int LATER, typename F, typename G>
__device__ __forceinline__ void slot_rows64(const LAS unsigned char* slot, const LAS unsigned char* vec, const Sel& sel, int c, int g,
                                            f32x4& acc, f32x4& a1, F&& last_wait, G&& all_landed) {
    const LAS unsigned char* base = slot + g * 256 + c * 16;
    const v4u s0 = *(const LAS v4u*)(vec + g * 32), s1 = *(const LAS v4u*)(vec + g * 32 + 16);
#pragma unroll
    for (int q = 0; q < 4; ++q) {
        VMCNT(LATER + 12 - 4 * q);
        v4u bb[4];
#pragma unroll
        for (int t = 0; t < 4; ++t) bb[t] = *(const LAS v4u*)(base + (4 * q + t) * 1024);
        if (q == 3) { last_wait(); all_landed(); }
        __builtin_amdgcn_sched_barrier(0);
#pragma unroll
        for (int u = 0; u < 2; ++u) {
            const unsigned S = (q < 2 ? s0 : s1)[2 * (q & 1) + u];
            acc = mfma(diagA(S, sel.s[0]), bb[2 * u], acc);
            a1 = mfma(diagA(S, sel.s[1]), bb[2 * u + 1], a1);
            PIN2(acc, a1);
        }
    }
}

__device__ __forceinline__ float gelu_tanh(float x) {
    const float y = 0.7978845608028654f * (x + 0.044715f * x * x * x);
    return rbf(x / (1.0f + __expf(-2.0f * y)));
}
__device__ __forceinline__ float wave_max(float v) {
#pragma unroll
    for (int o = 32; o >= 1; o >>= 1) v = fmaxf(v, __shfl_xor(v, o));
    return v;
}
__device__ __forceinline__ float wave_sum(float v) {
#pragma unroll
    for (int o = 32; o >= 1; o >>= 1) v += __shfl_xor(v, o);
    return v;
}

__global__ void __launch_bounds__(NTHR)
fused_decode_kernel(const unsigned char* __restrict__ xin, const unsigned char* __restrict__ kc, const unsigned char* __restrict__ vc,
                    const unsigned char* __restrict__ wq, const unsigned char* __restrict__ wo,
                    const unsigned char* __restrict__ wg, const unsigned char* __restrict__ wu, const unsigned char* __restrict__ wd,
                    const unsigned char* __restrict__ gatew, unsigned char* __restrict__ out, unsigned char* __restrict__ ws,
                    int pace, int tstart, int depth) {
    extern __shared__ __attribute__((aligned(16))) unsigned char smem[];
    LAS unsigned char* L = (LAS unsigned char*)smem;
    LAS unsigned* ctl = (LAS unsigned*)(L + CTL_OFF);
    asm volatile(".Lfdk_begin:");
    const unsigned long long t0 = rt();
    asm volatile("" ::"s"(xin), "s"(kc), "s"(vc), "s"(wq), "s"(wo), "s"(wg), "s"(wu), "s"(wd), "s"(gatew), "s"(out), "s"(ws), "s"(pace), "s"(tstart), "s"(depth));
    const int w = __builtin_amdgcn_readfirstlane((int)threadIdx.x >> 6);
    const int h = (blockIdx.x & 7) * 4 + ((blockIdx.x >> 3) & 3), b = blockIdx.x >> 5;
    const int fs = h, bh = b * NH + h;
    STAMP(0);
    const unsigned myslot_a = (unsigned)(size_t)L + w * SLOT;
    const LAS unsigned char* myslot = L + w * SLOT;
    const unsigned rot = (unsigned)(ROTK) + (w ^ 4);
    auto toff = [&](int t) { return (size_t)(((8u * t + rot) & 63u) * SLOT); };
    int lane, c, g;
    unsigned vk0, vk1, vk2, vk3;
    Sel sel;
    const unsigned char* kb;
    auto setup = [&]() {
        lane = fresh_lane(); c = lane & 15; g = lane >> 4;
        vk0 = g * 256 + ((c ^ g) << 4); vk1 = g * 256 + ((c ^ (4 + g)) << 4); vk2 = g * 256 + ((c ^ (8 + g)) << 4); vk3 = g * 256 + ((c ^ (12 + g)) << 4);
        sel = make_sel(c & 7);
        int bh2 = bh;
        asm volatile("" : "+s"(bh2));
        kb = kc + (size_t)bh2 * (SK * DH * 2);
    };
    auto code_prefetch = [&](int l) {
        unsigned long long pc;
        unsigned off, len, v = 0u;
        asm volatile("s_getpc_b64 %0\n.Lfdk_pc%=:\n\ts_mov_b32 %1, .Lfdk_pc%=-.Lfdk_begin\n\ts_mov_b32 %2, .Lfdk_end-.Lfdk_begin" : "=s"(pc), "=s"(off), "=s"(len));
        const unsigned o = (((blockIdx.x >> 3) & 31u) + 32u * (unsigned)l) * 128u;
        if (o < len) asm volatile("global_load_dword %0, %1, %2" : "+v"(v) : "v"(o), "s"(pc - off) : "memory");
        return v;
    };
    const int u = w ^ 4;
    auto take_turn = [&](int f) {
        const unsigned long long td = t0 + (unsigned)(tstart + (((f - 8) * pace) >> 4));
        while (rt() < td) __builtin_amdgcn_s_sleep(1);
        for (unsigned s = 0; s < SPIN_CAP; ++s) {
            const v2u st = *(volatile LAS v2u*)(ctl + C_ISS);
            if (rfl(st[0]) >= (unsigned)f && rfl(st[1]) + (unsigned)depth > (unsigned)f) break;
            __builtin_amdgcn_s_sleep(0);
        }
        CBAR();
    };
    auto issued = [&](int f) { if (lane == 0) *(volatile LAS unsigned*)(ctl + C_ISS) = (unsigned)f + 1u; CBAR(); };
    auto landed = [&]() { if (lane == 0) __hip_atomic_fetch_add(ctl + C_LAND, 1u, RLX_WG); CBAR(); };
    LAS float* sc = (LAS float*)(L + SC_OFF + w * 2048);
    unsigned long long epoch;

    if (__builtin_expect(w < 4, 1)) {
        v4u x8[4], wr[8];
        v2u ep;
        const unsigned l0 = threadIdx.x & 63;
        const unsigned char* qs = wq + (size_t)(128 * b + 32 * w) * (NH * DH * 2) + h * 256;
        const unsigned vq = (l0 >> 4) * 8 * (NH * DH * 2) + (l0 & 15) * 16;
#pragma unroll
        for (int t = 0; t < 8; ++t)
            asm volatile("global_load_dwordx4 %0, %1, %2" QNT : "=v"(wr[t]) : "v"(vq), "s"(qs + (size_t)t * (NH * DH * 2)) : "memory");
        __builtin_amdgcn_sched_barrier(0);
        const unsigned char* xs = xin + (128 * b + 32 * w) * 2;
        const unsigned vx = ((l0 >> 3) & 1) * (DM * 2) + (l0 >> 4) * 16;
#pragma unroll
        for (int j = 0; j < 4; ++j)
            asm volatile("global_load_dwordx4 %0, %1, %2" : "=v"(x8[j]) : "v"(vx), "s"(xs + j * (2 * DM * 2)) : "memory");
        asm volatile("global_load_dwordx2 %0, %1, %2 sc1" : "=v"(ep) : "v"(0u), "s"(ws + WS_EPOCH) : "memory");
        const unsigned char* inbox = ws + WS_QGRAN + (size_t)(h * NB + b) * NB * (DH * 8);
        unsigned warm;
        asm volatile("global_load_dword %0, %1, %2 sc1" : "=v"(warm) : "v"(l0 * 16), "s"(inbox) : "memory");
        STAMP(42);
        __builtin_amdgcn_s_barrier();
        setup();
        VMCNT(0);
        asm volatile("" : "+v"(x8[0]), "+v"(x8[1]), "+v"(x8[2]), "+v"(x8[3]), "+v"(wr[0]), "+v"(wr[1]), "+v"(wr[2]), "+v"(wr[3]),
                     "+v"(wr[4]), "+v"(wr[5]), "+v"(wr[6]), "+v"(wr[7]), "+v"(warm), "+v"(ep) :: "memory");
        STAMP(45);
        {
            const unsigned e0 = ep[0], e1 = ep[1];
            epoch = (((unsigned long long)rfl(e1) << 32) | rfl(e0)) + 1ull;
        }
        const unsigned qtag_hi = (unsigned)(epoch >> 8), qtag_lo = (unsigned)epoch & 0xffu;
#pragma unroll
        for (int j = 0; j < 4; ++j) {
            f32x4 a0 = {0.f, 0.f, 0.f, 0.f}, a1 = {0.f, 0.f, 0.f, 0.f};
#pragma unroll
            for (int k = 0; k < 4; ++k) {
                a0 = mfma(diagA(x8[j][k], sel.s[0]), wr[2 * k], a0);
                a1 = mfma(diagA(x8[j][k], sel.s[1]), wr[2 * k + 1], a1);
            }
            a0 += a1;
            *(LAS f32x4*)(L + SC_OFF + ((w * 8 + 2 * j + (g >> 1)) * 128 + 8 * c + 4 * (g & 1)) * 4) = a0;
        }
        if (w == 0 && lane == 0) *(LAS v2u*)(ctl + C_TAG) = v2u{(unsigned)epoch, (unsigned)(epoch >> 32)};
        arrive(ctl + C_QP, lane);
        wait_ge(ctl + C_QP, 4);
        STAMP(1);
        {
            const __amdgpu_buffer_rsrc_t rqg = __builtin_amdgcn_make_buffer_rsrc((void*)(ws + WS_QGRAN), 0, NH * NB * NB * DH * 8, 0x00020000);
#pragma unroll
            for (int r = 0; r < 2; ++r) {
                float s0 = 0.f, s1 = 0.f;
#pragma unroll
                for (int k = 0; k < 4; ++k) { const f32x2 v = *(const LAS f32x2*)(L + SC_OFF + ((k * 8 + 2 * w + r) * 128 + 2 * lane) * 4); s0 += v[0]; s1 += v[1]; }
                store_dev(v4u{gran(s0, qtag_lo), qtag_hi, gran(s1, qtag_lo), qtag_hi}, rqg, lane * 16, ((h * NB + 2 * w + r) * NB + b) * (DH * 8));
            }
        }
        arrive(ctl + C_QP, lane);
        STAMP(46);
        if (w == 3) {
            VMCNT(0);
            STAMP(55);
            const unsigned vo = lane * 16;
            v4u y[NB];
            for (unsigned s = 0;; ++s) {
#pragma unroll
                for (int p = 0; p < NB; ++p)
                    asm volatile("global_load_dwordx4 %0, %1, %2 offset:%3 sc1" : "=v"(y[p]) : "v"(vo), "s"(inbox + (p >> 2) * 4096), "i"((p & 3) * 1024) : "memory");
                if (s == 0) {
                    if (lane == 0) *(volatile LAS unsigned*)(ctl + C_POLL) = 1u;
                    fill(kb + toff(0), vk0, vk1, vk2, vk3, myslot_a);
                    VMCNT(16);
                } else {
                    VMCNT(0);
                }
                asm volatile("" : "+v"(y[0]), "+v"(y[1]), "+v"(y[2]), "+v"(y[3]), "+v"(y[4]), "+v"(y[5]), "+v"(y[6]), "+v"(y[7]) :: "memory");
                unsigned bad = 0u;
#pragma unroll
                for (int p = 0; p < NB; ++p) bad |= (y[p][1] ^ qtag_hi) | (y[p][3] ^ qtag_hi) | (((y[p][0] ^ qtag_lo) | (y[p][2] ^ qtag_lo)) & 0xffu);
                if (__all(bad == 0u) || s > SPIN_CAP) break;
                __builtin_amdgcn_s_sleep(POLL_SLEEP);
            }
            float s0 = 0.f, s1 = 0.f;
#pragma unroll
            for (int p = 0; p < NB; ++p) { s0 += __builtin_bit_cast(float, y[p][0] & 0xffffff00u); s1 += __builtin_bit_cast(float, y[p][2] & 0xffffff00u); }
            ((LAS unsigned*)(L + Q_OFF))[lane] = pack2(s0, s1);
            wait_ge(ctl + C_QP, 8);
            arrive(ctl + C_QR, lane);
        } else {
            if (w >= PRE) wait_ge(ctl + C_POLL, 1);
            fill(kb + toff(0), vk0, vk1, vk2, vk3, myslot_a);
        }
        STAMP(47);
        VMCNT(0);
        landed();
    } else {
        setup();
        if (w == 4 && lane < 16) ctl[lane] = lane == C_ISS ? 8u : 0u;
        unsigned pf = 0u;
        if (CODE_PF && w == 5 && lane < 16) pf = code_prefetch(lane);
        LGKM0();
        __builtin_amdgcn_s_barrier();
        fill(kb + toff(0), vk0, vk1, vk2, vk3, myslot_a);
        STAMP(43);
        asm volatile("s_waitcnt vmcnt(0)" : "+v"(pf)::"memory");
        landed();
        STAMP(2);
        wait_ge(ctl + C_QP, 4);
        const v2u e = *(volatile LAS v2u*)(ctl + C_TAG);
        const unsigned e0 = e[0], e1 = e[1];
        epoch = ((unsigned long long)rfl(e1) << 32) | rfl(e0);
    }
    const unsigned tag_hi = (unsigned)(epoch >> 8), tag_lo = (unsigned)epoch & 0xffu;
    const unsigned char* vb = vc + (size_t)bh * (SK * DH * 2);
    const __amdgpu_buffer_rsrc_t rgran = __builtin_amdgcn_make_buffer_rsrc((void*)(ws + WS_GRAN), 0, NB * NH * DM * 8, 0x00020000);
    const int gvo = (8 * c + 4 * (g & 1) + 2 * (g >> 1)) * 8, gso = bh * (DM * 8) + w * 1024;
    wait_ge(ctl + C_QR, 1);
    v4u qf[4];
#pragma unroll
    for (int s = 0; s < 4; ++s) qf[s] = *(const LAS v4u*)(L + Q_OFF + (32 * s + 8 * g) * 2);
    LGKM0();
    STAMP(3);

    float mx = -INFINITY;
    v4u wor[32];
    const __amdgpu_buffer_rsrc_t ro = __builtin_amdgcn_make_buffer_rsrc((void*)wo, 0, DM * NH * DH * 2, 0x00020000);
    const int soo = h * DH * (DM * 2) + w * 256;
#pragma unroll
    for (int t = 0; t < 8; ++t) {
        if (t > 0) { take_turn(8 * t + u); fill(kb + toff(t), vk0, vk1, vk2, vk3, myslot_a); issued(8 * t + u); }
        STAMP(8 + t);
        v4u xg[4][2], gw[16];
        if (t > 0) {
            const int l2 = fresh_lane();
            const int voo = 32 * (l2 >> 4) * (DM * 2) + (l2 & 15) * 16;
            v4u* wp = wor + 4 * (t - 1);
#pragma unroll
            for (int r = 0; r < 4; ++r) wp[r] = __builtin_amdgcn_raw_buffer_load_b128(ro, voo, soo + (4 * (t - 1) + r) * (DM * 2), WO_AUX);
            if (t == 1) {
                const __amdgpu_buffer_rsrc_t rx = __builtin_amdgcn_make_buffer_rsrc((void*)xin, 0, NB * DM * 2, 0x00020000);
                const __amdgpu_buffer_rsrc_t rw = __builtin_amdgcn_make_buffer_rsrc((void*)gatew, 0, DM * 2, 0x00020000);
                v4u xv = __builtin_amdgcn_raw_buffer_load_b128(rx, (l2 & 15) * 16, b * (DM * 2) + w * 256, 0);
                v4u gv = __builtin_amdgcn_raw_buffer_load_b128(rw, (l2 & 15) * 16, w * 256, 0);
                asm volatile("s_waitcnt vmcnt(0)" : "+v"(wp[0]), "+v"(wp[1]), "+v"(wp[2]), "+v"(wp[3]), "+v"(xv), "+v"(gv) :: "memory");
                landed();
                if (INVAL) store_dev(v4u{tag_lo ^ 1u, ~tag_hi, tag_lo ^ 1u, ~tag_hi}, rgran, gvo, gso);
                float s = 0.f;
#pragma unroll
                for (int k = 0; k < 4; ++k) s += rbf(bf_lo(xv[k]) * bf_lo(gv[k])) + rbf(bf_hi(xv[k]) * bf_hi(gv[k]));
                s = wave_sum(l2 < 16 ? s : 0.f);
                if (l2 == 0) ((LAS float*)ctl)[C_GP + w] = s;
            } else if (t == GU_T && w >= 4) {
                const int m = (w >> 1) & 1, hf = w & 1;
                const unsigned char* gs = (m ? wu : wg) + (size_t)(128 * b + 64 * hf) * (FF * 2) + fs * (FS * 2);
                const unsigned vog = 16 * (l2 >> 4) * (FF * 2) + ((l2 & 15) < 14 ? (l2 & 15) : 13) * 16;
#pragma unroll
                for (int k = 0; k < 16; ++k)
                    asm volatile("global_load_dwordx4 %0, %1, %2" GUNT : "=v"(gw[k]) : "v"(vog), "s"(gs + (size_t)k * (FF * 2)) : "memory");
                const unsigned char* xs = xin + (128 * b + 64 * hf) * 2;
                const unsigned vox = ((l2 & 15) >> 3) * (DM * 2) + (l2 >> 4) * 32;
#pragma unroll
                for (int j = 0; j < 4; ++j) {
                    asm volatile("global_load_dwordx4 %0, %1, %2" : "=v"(xg[j][0]) : "v"(vox), "s"(xs + j * (2 * DM * 2)) : "memory");
                    asm volatile("global_load_dwordx4 %0, %1, %2 offset:16" : "=v"(xg[j][1]) : "v"(vox), "s"(xs + j * (2 * DM * 2)) : "memory");
                }
                VMCNT(24);
            } else if (t == GU_T + 1 && w >= 4) {
                asm volatile("s_waitcnt vmcnt(0)" : "+v"(wp[0]), "+v"(wp[1]), "+v"(wp[2]), "+v"(wp[3]) :: "memory");
                landed();
                wait_ge(ctl + C_GU, 4);
                const __amdgpu_buffer_rsrc_t rgg = __builtin_amdgcn_make_buffer_rsrc((void*)(ws + WS_GUGRAN), 0, NH * NB * NB * 2048, 0x00020000);
#pragma unroll
                for (int r = 0; r < 2; ++r)
#pragma unroll
                    for (int q = 0; q < 2; ++q) {
                        const int p = q == 0 ? l2 : (l2 < 48 ? 64 + l2 : 111);
                        const int mm = p >= 56 ? 1 : 0, col = 2 * (p - 56 * mm), cons = 2 * (w - 4) + r;
                        const f32x2 v0 = *(const LAS f32x2*)(L + GU_OFF + (((2 * mm) * 8 + cons) * FS + col) * 4);
                        const f32x2 v1 = *(const LAS f32x2*)(L + GU_OFF + (((2 * mm + 1) * 8 + cons) * FS + col) * 4);
                        const float s0 = v0[0] + v1[0], s1 = v0[1] + v1[1];
                        if (q == 0 || l2 < 48)
                            store_dev(v4u{gran(s0, tag_lo), tag_hi, gran(s1, tag_lo), tag_hi}, rgg, p * 16, ((h * NB + cons) * NB + b) * 2048);
                    }
                arrive(ctl + C_GU, l2);
            } else if (t == 7) {
                const __amdgpu_buffer_rsrc_t rgg = __builtin_amdgcn_make_buffer_rsrc((void*)(ws + WS_GUGRAN), 0, NH * NB * NB * 2048, 0x00020000);
                const int sgg = ((h * NB + b) * NB + w) * 2048, v1o = 1024 + (l2 < 48 ? l2 : 47) * 16;
                v4u y0, y1;
                for (unsigned s = 0;; ++s) {
                    y0 = __builtin_amdgcn_raw_buffer_load_b128(rgg, l2 * 16, sgg, 16);
                    y1 = __builtin_amdgcn_raw_buffer_load_b128(rgg, v1o, sgg, 16);
                    asm volatile("s_waitcnt vmcnt(0)" : "+v"(wp[0]), "+v"(wp[1]), "+v"(wp[2]), "+v"(wp[3]), "+v"(y0), "+v"(y1) :: "memory");
                    const unsigned bad = (y0[1] ^ tag_hi) | (y0[3] ^ tag_hi) | (y1[1] ^ tag_hi) | (y1[3] ^ tag_hi) | (((y0[0] ^ tag_lo) | (y0[2] ^ tag_lo) | (y1[0] ^ tag_lo) | (y1[2] ^ tag_lo)) & 0xffu);
                    if (s == 0) landed();
                    if (__all(bad == 0u) || s > SPIN_CAP) break;
                    __builtin_amdgcn_s_sleep(1);
                }
                wait_ge(ctl + C_GU, 8);
                *(LAS v2u*)(L + GU_OFF + w * 896 + l2 * 8) = v2u{y0[0] & 0xffffff00u, y0[2] & 0xffffff00u};
                if (l2 < 48) *(LAS v2u*)(L + GU_OFF + w * 896 + 512 + l2 * 8) = v2u{y1[0] & 0xffffff00u, y1[2] & 0xffffff00u};
                arrive(ctl + C_FFN, l2);
            } else {
                asm volatile("s_waitcnt vmcnt(0)" : "+v"(wp[0]), "+v"(wp[1]), "+v"(wp[2]), "+v"(wp[3]) :: "memory");
            }
            if (t != 7 && t != 1 && !(t == GU_T + 1 && w >= 4)) landed();
        }
        STAMP(16 + t);
        f32x4 s4[4];
#pragma unroll
        for (int kg = 0; kg < 4; ++kg) {
            f32x4 a = {0.f, 0.f, 0.f, 0.f};
#pragma unroll
            for (int s = 0; s < 4; ++s) {
                const v4u kf = *(const LAS v4u*)(myslot + (16 * kg + c) * 256 + (((4 * s + g) ^ c) << 4));
                a = mfma(kf, qf[s], a);
            }
            s4[kg] = a;
        }
        f32x4 v;
#pragma unroll
        for (int i = 0; i < 4; ++i) {
            const float lo = (c & 1) ? s4[1][i] : s4[0][i], hi = (c & 1) ? s4[3][i] : s4[2][i];
            v[i] = rbf(rbf((c & 2) ? hi : lo) * 0.08838834764831845f);
            mx = fmaxf(mx, v[i]);
        }
        if (c < 4) *(LAS f32x4*)(sc + t * 64 + 16 * c + 4 * g) = v;
        __builtin_amdgcn_sched_barrier(0);
        if (t == GU_T && w >= 4) {
            v4u* wp = wor + 4 * (t - 1);
            asm volatile("s_waitcnt vmcnt(0)" : "+v"(gw[0]), "+v"(gw[1]), "+v"(gw[2]), "+v"(gw[3]), "+v"(gw[4]), "+v"(gw[5]), "+v"(gw[6]), "+v"(gw[7]),
                         "+v"(gw[8]), "+v"(gw[9]), "+v"(gw[10]), "+v"(gw[11]), "+v"(gw[12]), "+v"(gw[13]), "+v"(gw[14]), "+v"(gw[15]),
                         "+v"(xg[0][0]), "+v"(xg[0][1]), "+v"(xg[1][0]), "+v"(xg[1][1]), "+v"(xg[2][0]), "+v"(xg[2][1]), "+v"(xg[3][0]), "+v"(xg[3][1]) :: "memory");
            asm volatile("" : "+v"(wp[0]), "+v"(wp[1]), "+v"(wp[2]), "+v"(wp[3]));
            STAMP(44);
            f32x4 a[4];
#pragma unroll
            for (int j = 0; j < 4; ++j) a[j] = f32x4{0.f, 0.f, 0.f, 0.f};
#pragma unroll
            for (int k = 0; k < 16; ++k) {
#pragma unroll
                for (int j = 0; j < 4; ++j) a[j] = mfma(diagA(xg[j][k >> 3][(k >> 1) & 3], sel.s[k & 1]), gw[k], a[j]);
                PIN2(a[0], a[1]); PIN2(a[2], a[3]);
            }
            if (c < 14) {
#pragma unroll
                for (int j = 0; j < 4; ++j)
                    *(LAS f32x4*)(L + GU_OFF + ((((w - 4) * 8 + 2 * j + (g >> 1)) * FS) + 8 * c + 4 * (g & 1)) * 4) = a[j];
            }
            arrive(ctl + C_GU, lane);
            STAMP(56);
        }
    }
    STAMP(4);
    float lsum;
    mx = wave_max(mx);
    {
        const f32x4 sa = *(const LAS f32x4*)(sc + 8 * lane), sb = *(const LAS f32x4*)(sc + 8 * lane + 4);
        float e[8];
#pragma unroll
        for (int i = 0; i < 4; ++i) { e[i] = __expf(sa[i] - mx); e[4 + i] = __expf(sb[i] - mx); }
        lsum = wave_sum(((e[0] + e[1]) + (e[2] + e[3])) + ((e[4] + e[5]) + (e[6] + e[7])));
        unsigned pk[4];
#pragma unroll
        for (int i = 0; i < 4; ++i) pk[i] = pack2(e[i], e[4 + i]);
        LGKM0();
#pragma unroll
        for (int i = 0; i < 4; ++i) *(LAS unsigned*)((LAS unsigned char*)sc + (lane >> 3) * 128 + i * 32 + (lane & 7) * 4) = pk[i];
    }
    if (lane == 0) { ((LAS float*)ctl)[C_WMAX + w] = mx; ((LAS float*)ctl)[C_WSUM + w] = lsum; }
    arrive(ctl + C_SM, lane);
    STAMP(5);

    f32x4 acco = {0.f, 0.f, 0.f, 0.f}, acco1 = {0.f, 0.f, 0.f, 0.f}, accb = {0.f, 0.f, 0.f, 0.f};
    float fw = 0.f, gate = 0.f;
    {
        const unsigned vlin = lane * 16;
        {
            take_turn(64 + u);
            fill(vb + toff(0), vlin, vlin, vlin, vlin, myslot_a);
            issued(64 + u);
            STAMP(24);
            const int voo = 32 * g * (DM * 2) + c * 16;
#pragma unroll
            for (int r = 0; r < 4; ++r) wor[28 + r] = __builtin_amdgcn_raw_buffer_load_b128(ro, voo, soo + (28 + r) * (DM * 2), WO_AUX);
            slot_rows64<4>(myslot, (const LAS unsigned char*)sc, sel, c, g, acco, acco1, []() {}, landed);
            asm volatile("s_waitcnt vmcnt(0)" : "+v"(wor[28]), "+v"(wor[29]), "+v"(wor[30]), "+v"(wor[31]) :: "memory");
            STAMP(32);
        }
        const __amdgpu_buffer_rsrc_t rd = __builtin_amdgcn_make_buffer_rsrc((void*)wd, 0, FF * DM * 2, 0x00020000);
        const int vod = 28 * g * (DM * 2) + c * 16;
#pragma unroll 1
        for (int t = 1; t < 8; ++t) {
            take_turn(64 + 8 * t + u);
            fill(vb + toff(t), vlin, vlin, vlin, vlin, myslot_a);
            issued(64 + 8 * t + u);
            STAMP(24 + t);
            const int tt = t - 1;
            const int sod = (fs * FS + 4 * tt) * (DM * 2) + w * 256;
            v4u dw[4];
#pragma unroll
            for (int r = 0; r < 4; ++r) dw[r] = __builtin_amdgcn_raw_buffer_load_b128(rd, vod, sod + r * (DM * 2), WD_AUX);
            if (t == 1) {
                wait_ge(ctl + C_FFN, NW);
                const int l2 = fresh_lane();
                if (l2 < FS / 2) {
                    float g0 = 0.f, g1 = 0.f, u0 = 0.f, u1 = 0.f;
#pragma unroll
                    for (int ww = 0; ww < NW; ++ww) {
                        const f32x2 a = *(const LAS f32x2*)(L + GU_OFF + ww * 896 + 8 * l2), bb = *(const LAS f32x2*)(L + GU_OFF + ww * 896 + 448 + 8 * l2);
                        g0 += a[0]; g1 += a[1]; u0 += bb[0]; u1 += bb[1];
                    }
                    ((LAS unsigned*)(L + HH_OFF))[l2] = pack2(gelu_tanh(rbf(g0)) * rbf(u0), gelu_tanh(rbf(g1)) * rbf(u1));
                }
                wait_ge(ctl + C_SM, NW);
                float m[NW], M = -INFINITY, Ls = 0.f, z = 0.f;
#pragma unroll
                for (int ww = 0; ww < NW; ++ww) { m[ww] = ((LAS float*)ctl)[C_WMAX + ww]; M = fmaxf(M, m[ww]); }
#pragma unroll
                for (int ww = 0; ww < NW; ++ww) { Ls += __expf(m[ww] - M) * ((LAS float*)ctl)[C_WSUM + ww]; z += ((LAS float*)ctl)[C_GP + ww]; }
                fw = __expf(mx - M) / Ls;
                gate = rbf(1.0f / (1.0f + __expf(-rbf(z))));
                LGKM0();
            }
            unsigned pf = 0u;
            if (CODE_PF2 && t == CODE_PF2 && w == 0 && lane < 16) pf = code_prefetch(lane);
            const v2u hs = *(const LAS v2u*)(L + HH_OFF + (28 * g + 4 * tt) * 2);
            slot_rows64<4>(myslot, (const LAS unsigned char*)sc + t * 128, sel, c, g, acco, acco1, []() {}, landed);
            asm volatile("s_waitcnt vmcnt(0)" : "+v"(dw[0]), "+v"(dw[1]), "+v"(dw[2]), "+v"(dw[3]), "+v"(pf) :: "memory");
            STAMP(32 + t);
#pragma unroll
            for (int r = 0; r < 4; ++r) accb = mfma(diagA(hs[r >> 1], sel.s[r & 1]), dw[r], accb);
        }
    }
    STAMP(6);
    acco = (acco + acco1) * fw;
    if (g < 2) *(LAS f32x4*)((LAS unsigned char*)sc + 1024 + (8 * c + 4 * g) * 4) = acco;
    arrive(ctl + C_O, lane);
    wait_ge(ctl + C_O, NW);
    v4u of[4];
    {
        float s0 = 0.f, s1 = 0.f;
#pragma unroll
        for (int ww = 0; ww < NW; ++ww) {
            const f32x2 v = *(const LAS f32x2*)(L + SC_OFF + ww * 2048 + 1024 + 8 * lane);
            s0 += v[0]; s1 += v[1];
        }
        ((LAS unsigned*)(L + O_OFF))[lane] = pack2(s0, s1);
        LGKM0();
#pragma unroll
        for (int k = 0; k < 4; ++k) of[k] = *(const LAS v4u*)(L + O_OFF + 64 * g + 16 * k);
    }
    STAMP(7);
    f32x4 acca, ac[4];
#pragma unroll
    for (int k = 0; k < 4; ++k) ac[k] = f32x4{0.f, 0.f, 0.f, 0.f};
#pragma unroll
    for (int t = 0; t < 32; t += 4) {
#pragma unroll
        for (int k = 0; k < 4; ++k) ac[k] = mfma(diagA(of[t >> 3][((t + k) >> 1) & 3], sel.s[k & 1]), wor[t + k], ac[k]);
    }
    acca = (ac[0] + ac[1]) + (ac[2] + ac[3]);
    STAMP(40);
    {
        const float omg = rbf(1.0f - gate);
        {
            const float r0 = gate * (g < 2 ? acca[0] : acca[2]) + omg * (g < 2 ? accb[0] : accb[2]);
            const float r1 = gate * (g < 2 ? acca[1] : acca[3]) + omg * (g < 2 ? accb[1] : accb[3]);
            store_dev(v4u{gran(r0, tag_lo), tag_hi, gran(r1, tag_lo), tag_hi}, rgran, gvo, (int)rfl((unsigned)gso));
        }
        const int l2 = fresh_lane();
        const int vo = ((l2 >> 4) * DM + 2 * (l2 & 15)) * 8, so = ((b * NH + 4 * w) * DM + 32 * h) * 8;
        v4u y, ya, yb;
        auto poll = [&](v4u& d) { asm volatile("buffer_load_dwordx4 %0, %1, %2, %3 offen sc1" : "=v"(d) : "v"(vo), "s"(rgran), "s"(so) : "memory"); };
        auto good = [&](const v4u& d) { return __all(((d[1] ^ tag_hi) | (d[3] ^ tag_hi) | (((d[0] ^ tag_lo) | (d[2] ^ tag_lo)) & 0xffu)) == 0u); };
        poll(ya);
        __builtin_amdgcn_s_sleep(POLL_GAP);
        poll(yb);
        for (unsigned s = 0;; ++s) {
            asm volatile("s_waitcnt vmcnt(1)" : "+v"(ya)::"memory");
            if (good(ya) || s > SPIN_CAP) { y = ya; break; }
            poll(ya);
            asm volatile("s_waitcnt vmcnt(1)" : "+v"(yb)::"memory");
            if (good(yb)) { y = yb; break; }
            poll(yb);
        }
        STAMP(41);
        float v0 = __builtin_bit_cast(float, y[0] & 0xffffff00u), v1 = __builtin_bit_cast(float, y[2] & 0xffffff00u);
        v0 += __shfl_xor(v0, 16); v1 += __shfl_xor(v1, 16);
        v0 += __shfl_xor(v0, 32); v1 += __shfl_xor(v1, 32);
        if (l2 < 16) *(LAS f32x2*)(L + GU_OFF + w * 128 + l2 * 8) = f32x2{v0, v1};
        LGKM0();
        unsigned old = 0u;
        if (l2 == 0) old = __hip_atomic_fetch_add(ctl + C_OUT, 1u, RLX_WG);
        if (rfl(old) == NW - 1) {
            CBAR();
            if (l2 < 16) {
                float s0 = 0.f, s1 = 0.f;
#pragma unroll
                for (int ww = 0; ww < NW; ++ww) { const f32x2 v = *(const LAS f32x2*)(L + GU_OFF + ww * 128 + l2 * 8); s0 += v[0]; s1 += v[1]; }
                __hip_atomic_store((gu32*)(out + ((size_t)b * DM + 32 * h + 2 * l2) * 2), pack2(s0, s1), __ATOMIC_RELAXED, __HIP_MEMORY_SCOPE_SYSTEM);
            }
            if (bh == 0 && l2 == 0) __hip_atomic_store((gu64*)(ws + WS_EPOCH), epoch, RLX_AGENT);
        }
        STAMP(57);
        asm volatile("" ::"v"(ya), "v"(yb));
    }
    SDUMP();
    asm volatile(".Lfdk_end:");
}

}

extern "C" void kernel_launch(void* const* d_in, const int* in_sizes, int n_in, void* d_out, int out_size, void* d_ws, size_t ws_size,
                              hipStream_t stream) {
    (void)in_sizes; (void)n_in; (void)out_size; (void)ws_size;
    (void)hipFuncSetAttribute((const void*)fused_decode_kernel, hipFuncAttributeMaxDynamicSharedMemorySize, LDS_BYTES);
    int pace = PACE, tstart = TSTART, depth = DEPTH;
    fused_decode_kernel<<<dim3(NB * NH), dim3(NTHR), LDS_BYTES, stream>>>(
        (const unsigned char*)d_in[0], (const unsigned char*)d_in[1], (const unsigned char*)d_in[2], (const unsigned char*)d_in[3],
        (const unsigned char*)d_in[4], (const unsigned char*)d_in[6], (const unsigned char*)d_in[7],
        (const unsigned char*)d_in[8], (const unsigned char*)d_in[9], (unsigned char*)d_out, (unsigned char*)d_ws, pace, tstart, depth);
}
```

```cpp
#define PACE 936
#define HOLE1 65
#define HOLE2 5
#include <hip/hip_runtime.h>
#include <hip/hip_bf16.h>

#define GAS __attribute__((address_space(1)))
#define LAS __attribute__((address_space(3)))
typedef unsigned v4u __attribute__((ext_vector_type(4)));
typedef unsigned v2u __attribute__((ext_vector_type(2)));
typedef float f32x4 __attribute__((ext_vector_type(4)));
typedef float f32x2 __attribute__((ext_vector_type(2)));
typedef short bf16x8 __attribute__((ext_vector_type(8)));
typedef GAS unsigned gu32;
typedef GAS unsigned long long gu64;
#define RLX_AGENT __ATOMIC_RELAXED, __HIP_MEMORY_SCOPE_AGENT
#define RLX_WG __ATOMIC_RELAXED, __HIP_MEMORY_SCOPE_WORKGROUP

namespace {

constexpr int NB = 8, DM = 1024, NH = 32, SK = 4096, DH = 128, FF = 3584;
constexpr int FS = FF / NH;
constexpr int NW = 8, NTHR = 64 * NW;
constexpr int SLOT = 16384;
#define GUNT " nt"
#define ROTK 0
#define PRE 1
#define GU_T 2
#define WO_AUX 0
#define WD_AUX 0
#define POLL_SLEEP 1
#define QNT " nt"
#define CODE_PF 1
#define CODE_PF2 5
#define POLL_GAP 10
#define INVAL 0
#define TSTART 600
#define DEPTH 4

constexpr int SC_OFF = NW * SLOT;
constexpr int GU_OFF = SC_OFF + NW * 2048;
constexpr int Q_OFF = GU_OFF + 14336;
constexpr int O_OFF = Q_OFF + 256;
constexpr int HH_OFF = O_OFF + 256;
constexpr int CTL_OFF = HH_OFF + 256;
constexpr int LDS_BYTES = CTL_OFF + 256;
constexpr int C_ISS = 0, C_LAND = 1, C_FFN = 2, C_O = 3, C_QP = 4, C_QR = 5, C_GU = 6, C_OUT = 7, C_TAG = 8, C_POLL = 10, C_SM = 11, C_WMAX = 16, C_WSUM = 24, C_GP = 32;
static_assert(LDS_BYTES <= 160 * 1024, "LDS");

constexpr size_t WS_EPOCH = 0, WS_GRAN = 4096, WS_QGRAN = WS_GRAN + (size_t)NB * NH * DM * 8, WS_GUGRAN = WS_QGRAN + (size_t)NH * NB * NB * DH * 8;

constexpr unsigned SPIN_CAP = 1u << 22;
#define STAMP(i) do {} while (0)
#define SDUMP() do {} while (0)

__device__ __forceinline__ f32x4 mfma(v4u a, v4u b, f32x4 c) {
    return __builtin_amdgcn_mfma_f32_16x16x32_bf16(__builtin_bit_cast(bf16x8, a), __builtin_bit_cast(bf16x8, b), c, 0, 0, 0);
}
__device__ __forceinline__ float bf_lo(unsigned u) { return __builtin_bit_cast(float, u << 16); }
__device__ __forceinline__ float bf_hi(unsigned u) { return __builtin_bit_cast(float, u & 0xffff0000u); }
__device__ __forceinline__ float rbf(float f) { return (float)(__bf16)f; }
__device__ __forceinline__ unsigned bf_bits(float f) { return (unsigned)__builtin_bit_cast(unsigned short, (__bf16)f); }
__device__ __forceinline__ unsigned pack2(float lo, float hi) { return bf_bits(lo) | (bf_bits(hi) << 16); }
__device__ __forceinline__ unsigned gran(float f, unsigned tag_lo) { return ((__builtin_bit_cast(unsigned, f) + 0x80u) & 0xffffff00u) | tag_lo; }
__device__ __forceinline__ void store_dev(v4u d, __amdgpu_buffer_rsrc_t r, int vo, int so) {
    __builtin_amdgcn_raw_buffer_store_b128(d, r, vo, so, 16);
    asm volatile("s_nop 1" ::"v"(d) : "memory");
}
__device__ __forceinline__ unsigned long long rt() { return __builtin_amdgcn_s_memrealtime(); }
__device__ __forceinline__ unsigned rfl(unsigned v) { return (unsigned)__builtin_amdgcn_readfirstlane((int)v); }
#define LGKM0() asm volatile("s_waitcnt lgkmcnt(0)" ::: "memory")
#define VMCNT(n) asm volatile("s_waitcnt vmcnt(%0)" ::"i"(n) : "memory")
#define CBAR() asm volatile("" ::: "memory")
#define PIN2(a, b) do { asm volatile("" : "+v"(a), "+v"(b)); __builtin_amdgcn_sched_barrier(0); } while (0)

__device__ __forceinline__ void glds4(const void* sbase, unsigned v0, unsigned v1, unsigned v2, unsigned v3, unsigned lds) {
    unsigned keep;
    asm volatile("s_mov_b32 %0, m0\n\ts_mov_b32 m0, %6\n\ts_nop 0\n\t"
                 "global_load_lds_dwordx4 %1, %5 nt\n\t"
                 "global_load_lds_dwordx4 %2, %5 offset:1024 nt\n\t"
                 "global_load_lds_dwordx4 %3, %5 offset:2048 nt\n\t"
                 "global_load_lds_dwordx4 %4, %5 offset:3072 nt\n\t"
                 "s_mov_b32 m0, %0"
                 : "=&s"(keep) : "v"(v0), "v"(v1), "v"(v2), "v"(v3), "s"(sbase), "s"(lds) : "memory");
}
__device__ __forceinline__ void fill(const unsigned char* src, unsigned v0, unsigned v1, unsigned v2, unsigned v3, unsigned lds) {
    LGKM0();
#pragma unroll
    for (int q = 0; q < 4; ++q) glds4(src + q * 4096, v0, v1, v2, v3, lds + q * 4096);
}

__device__ __forceinline__ void wait_ge(LAS unsigned* w, unsigned need) {
    for (unsigned s = 0; rfl(*(volatile LAS unsigned*)w) < need && s < SPIN_CAP; ++s) __builtin_amdgcn_s_sleep(0);
    CBAR();
}
__device__ __forceinline__ void arrive(LAS unsigned* w, int lane) {
    LGKM0();
    if (lane == 0) __hip_atomic_fetch_add(w, 1u, RLX_WG);
    CBAR();
}

struct Sel { unsigned s[2][4]; };
__device__ __forceinline__ v4u diagA(unsigned S, const unsigned (&sel)[4]) {
    return v4u{__builtin_amdgcn_perm(0u, S, sel[0]), __builtin_amdgcn_perm(0u, S, sel[1]), __builtin_amdgcn_perm(0u, S, sel[2]), __builtin_amdgcn_perm(0u, S, sel[3])};
}
__device__ __forceinline__ int fresh_lane() {
    int l;
    asm volatile("v_mbcnt_lo_u32_b32 %0, -1, 0\n\tv_mbcnt_hi_u32_b32 %0, -1, %0" : "=v"(l));
    __builtin_assume((unsigned)l < 64u);
    return l;
}
__device__ __forceinline__ Sel make_sel(int c) {
    Sel sel;
    const int dm = (c & 7) >> 1, hf = c & 1;
#pragma unroll
    for (int par = 0; par < 2; ++par)
#pragma unroll
        for (int d = 0; d < 4; ++d) {
            const unsigned lohi = (unsigned)(2 * par) | ((unsigned)(2 * par + 1) << 8);
            const unsigned v = hf ? (0x0c0cu | (lohi << 16)) : (0x0c0c0000u | lohi);
            sel.s[par][d] = (c < 8 && d == dm) ? v : 0x0c0c0c0cu;
        }
    return sel;
}

template <int LATER, typename F, typename G>
__device__ __forceinline__ void slot_rows64(const LAS unsigned char* slot, const LAS unsigned char* vec, const Sel& sel, int c, int g,
                                            f32x4& acc, f32x4& a1, F&& last_wait, G&& all_landed) {
    const LAS unsigned char* base = slot + g * 256 + c * 16;
    const v4u s0 = *(const LAS v4u*)(vec + g * 32), s1 = *(const LAS v4u*)(vec + g * 32 + 16);
#pragma unroll
    for (int q = 0; q < 4; ++q) {
        VMCNT(LATER + 12 - 4 * q);
        v4u bb[4];
#pragma unroll
        for (int t = 0; t < 4; ++t) bb[t] = *(const LAS v4u*)(base + (4 * q + t) * 1024);
        if (q == 3) { last_wait(); all_landed(); }
        __builtin_amdgcn_sched_barrier(0);
#pragma unroll
        for (int u = 0; u < 2; ++u) {
            const unsigned S = (q < 2 ? s0 : s1)[2 * (q & 1) + u];
            acc = mfma(diagA(S, sel.s[0]), bb[2 * u], acc);
            a1 = mfma(diagA(S, sel.s[1]), bb[2 * u + 1], a1);
            PIN2(acc, a1);
        }
    }
}

__device__ __forceinline__ float gelu_tanh(float x) {
    const float y = 0.7978845608028654f * (x + 0.044715f * x * x * x);
    return rbf(x / (1.0f + __expf(-2.0f * y)));
}
__device__ __forceinline__ float wave_max(float v) {
#pragma unroll
    for (int o = 32; o >= 1; o >>= 1) v = fmaxf(v, __shfl_xor(v, o));
    return v;
}
__device__ __forceinline__ float wave_sum(float v) {
#pragma unroll
    for (int o = 32; o >= 1; o >>= 1) v += __shfl_xor(v, o);
    return v;
}

__global__ void __launch_bounds__(NTHR)
fused_decode_kernel(const unsigned char* __restrict__ xin, const unsigned char* __restrict__ kc, const unsigned char* __restrict__ vc,
                    const unsigned char* __restrict__ wq, const unsigned char* __restrict__ wo,
                    const unsigned char* __restrict__ wg, const unsigned char* __restrict__ wu, const unsigned char* __restrict__ wd,
                    const unsigned char* __restrict__ gatew, unsigned char* __restrict__ out, unsigned char* __restrict__ ws,
                    int pace, int tstart, int depth) {
    extern __shared__ __attribute__((aligned(16))) unsigned char smem[];
    LAS unsigned char* L = (LAS unsigned char*)smem;
    LAS unsigned* ctl = (LAS unsigned*)(L + CTL_OFF);
    asm volatile(".Lfdk_begin:");
    const unsigned long long t0 = rt();
    asm volatile("" ::"s"(xin), "s"(kc), "s"(vc), "s"(wq), "s"(wo), "s"(wg), "s"(wu), "s"(wd), "s"(gatew), "s"(out), "s"(ws), "s"(pace), "s"(tstart), "s"(depth));
    const int w = __builtin_amdgcn_readfirstlane((int)threadIdx.x >> 6);
    const int h = (blockIdx.x & 7) * 4 + ((blockIdx.x >> 3) & 3), b = blockIdx.x >> 5;
    const int fs = h, bh = b * NH + h;
    STAMP(0);
    const unsigned myslot_a = (unsigned)(size_t)L + w * SLOT;
    const LAS unsigned char* myslot = L + w * SLOT;
    const unsigned rot = (unsigned)(ROTK) + (w ^ 4);
    auto toff = [&](int t) { return (size_t)(((8u * t + rot) & 63u) * SLOT); };
    int lane, c, g;
    unsigned vk0, vk1, vk2, vk3;
    Sel sel;
    const unsigned char* kb;
    auto setup = [&]() {
        lane = fresh_lane(); c = lane & 15; g = lane >> 4;
        vk0 = g * 256 + ((c ^ g) << 4); vk1 = g * 256 + ((c ^ (4 + g)) << 4); vk2 = g * 256 + ((c ^ (8 + g)) << 4); vk3 = g * 256 + ((c ^ (12 + g)) << 4);
        sel = make_sel(c & 7);
        int bh2 = bh;
        asm volatile("" : "+s"(bh2));
        kb = kc + (size_t)bh2 * (SK * DH * 2);
    };
    auto code_prefetch = [&](int l) {
        unsigned long long pc;
        unsigned off, len, v = 0u;
        asm volatile("s_getpc_b64 %0\n.Lfdk_pc%=:\n\ts_mov_b32 %1, .Lfdk_pc%=-.Lfdk_begin\n\ts_mov_b32 %2, .Lfdk_end-.Lfdk_begin" : "=s"(pc), "=s"(off), "=s"(len));
        const unsigned o = (((blockIdx.x >> 3) & 31u) + 32u * (unsigned)l) * 128u;
        if (o < len) asm volatile("global_load_dword %0, %1, %2" : "+v"(v) : "v"(o), "s"(pc - off) : "memory");
        return v;
    };
    const int u = w ^ 4;
    auto take_turn = [&](int f) {
        const int hole = HOLE1 * min(max(f - 8 * GU_T, 0), 4) + HOLE2 * min(max(f - 56, 0), 8);
        const unsigned long long td = t0 + (unsigned)(tstart + hole + (((f - 8) * pace) >> 4));
        while (rt() < td) __builtin_amdgcn_s_sleep(1);
        for (unsigned s = 0; s < SPIN_CAP; ++s) {
            const v2u st = *(volatile LAS v2u*)(ctl + C_ISS);
            if (rfl(st[0]) >= (unsigned)f && rfl(st[1]) + (unsigned)depth > (unsigned)f) break;
            __builtin_amdgcn_s_sleep(0);
        }
        CBAR();
    };
    auto issued = [&](int f) { if (lane == 0) *(volatile LAS unsigned*)(ctl + C_ISS) = (unsigned)f + 1u; CBAR(); };
    auto landed = [&]() { if (lane == 0) __hip_atomic_fetch_add(ctl + C_LAND, 1u, RLX_WG); CBAR(); };
    LAS float* sc = (LAS float*)(L + SC_OFF + w * 2048);
    unsigned long long epoch;

    if (__builtin_expect(w < 4, 1)) {
        v4u x8[4], wr[8];
        v2u ep;
        const unsigned l0 = threadIdx.x & 63;
        const unsigned char* qs = wq + (size_t)(128 * b + 32 * w) * (NH * DH * 2) + h * 256;
        const unsigned vq = (l0 >> 4) * 8 * (NH * DH * 2) + (l0 & 15) * 16;
#pragma unroll
        for (int t = 0; t < 8; ++t)
            asm volatile("global_load_dwordx4 %0, %1, %2" QNT : "=v"(wr[t]) : "v"(vq), "s"(qs + (size_t)t * (NH * DH * 2)) : "memory");
        __builtin_amdgcn_sched_barrier(0);
        const unsigned char* xs = xin + (128 * b + 32 * w) * 2;
        const unsigned vx = ((l0 >> 3) & 1) * (DM * 2) + (l0 >> 4) * 16;
#pragma unroll
        for (int j = 0; j < 4; ++j)
            asm volatile("global_load_dwordx4 %0, %1, %2" : "=v"(x8[j]) : "v"(vx), "s"(xs + j * (2 * DM * 2)) : "memory");
        asm volatile("global_load_dwordx2 %0, %1, %2 sc1" : "=v"(ep) : "v"(0u), "s"(ws + WS_EPOCH) : "memory");
        const unsigned char* inbox = ws + WS_QGRAN + (size_t)(h * NB + b) * NB * (DH * 8);
        unsigned warm;
        asm volatile("global_load_dword %0, %1, %2 sc1" : "=v"(warm) : "v"(l0 * 16), "s"(inbox) : "memory");
        STAMP(42);
        __builtin_amdgcn_s_barrier();
        setup();
        VMCNT(0);
        asm volatile("" : "+v"(x8[0]), "+v"(x8[1]), "+v"(x8[2]), "+v"(x8[3]), "+v"(wr[0]), "+v"(wr[1]), "+v"(wr[2]), "+v"(wr[3]),
                     "+v"(wr[4]), "+v"(wr[5]), "+v"(wr[6]), "+v"(wr[7]), "+v"(warm), "+v"(ep) :: "memory");
        STAMP(45);
        {
            const unsigned e0 = ep[0], e1 = ep[1];
            epoch = (((unsigned long long)rfl(e1) << 32) | rfl(e0)) + 1ull;
        }
        const unsigned qtag_hi = (unsigned)(epoch >> 8), qtag_lo = (unsigned)epoch & 0xffu;
#pragma unroll
        for (int j = 0; j < 4; ++j) {
            f32x4 a0 = {0.f, 0.f, 0.f, 0.f}, a1 = {0.f, 0.f, 0.f, 0.f};
#pragma unroll
            for (int k = 0; k < 4; ++k) {
                a0 = mfma(diagA(x8[j][k], sel.s[0]), wr[2 * k], a0);
                a1 = mfma(diagA(x8[j][k], sel.s[1]), wr[2 * k + 1], a1);
            }
            a0 += a1;
            *(LAS f32x4*)(L + SC_OFF + ((w * 8 + 2 * j + (g >> 1)) * 128 + 8 * c + 4 * (g & 1)) * 4) = a0;
        }
        if (w == 0 && lane == 0) *(LAS v2u*)(ctl + C_TAG) = v2u{(unsigned)epoch, (unsigned)(epoch >> 32)};
        arrive(ctl + C_QP, lane);
        wait_ge(ctl + C_QP, 4);
        STAMP(1);
        {
            const __amdgpu_buffer_rsrc_t rqg = __builtin_amdgcn_make_buffer_rsrc((void*)(ws + WS_QGRAN), 0, NH * NB * NB * DH * 8, 0x00020000);
#pragma unroll
            for (int r = 0; r < 2; ++r) {
                float s0 = 0.f, s1 = 0.f;
#pragma unroll
                for (int k = 0; k < 4; ++k) { const f32x2 v = *(const LAS f32x2*)(L + SC_OFF + ((k * 8 + 2 * w + r) * 128 + 2 * lane) * 4); s0 += v[0]; s1 += v[1]; }
                store_dev(v4u{gran(s0, qtag_lo), qtag_hi, gran(s1, qtag_lo), qtag_hi}, rqg, lane * 16, ((h * NB + 2 * w + r) * NB + b) * (DH * 8));
            }
        }
        arrive(ctl + C_QP, lane);
        STAMP(46);
        if (w == 3) {
            VMCNT(0);
            STAMP(55);
            const unsigned vo = lane * 16;
            v4u y[NB];
            for (unsigned s = 0;; ++s) {
#pragma unroll
                for (int p = 0; p < NB; ++p)
                    asm volatile("global_load_dwordx4 %0, %1, %2 offset:%3 sc1" : "=v"(y[p]) : "v"(vo), "s"(inbox + (p >> 2) * 4096), "i"((p & 3) * 1024) : "memory");
                if (s == 0) {
                    if (lane == 0) *(volatile LAS unsigned*)(ctl + C_POLL) = 1u;
                    fill(kb + toff(0), vk0, vk1, vk2, vk3, myslot_a);
                    VMCNT(16);
                } else {
                    VMCNT(0);
                }
                asm volatile("" : "+v"(y[0]), "+v"(y[1]), "+v"(y[2]), "+v"(y[3]), "+v"(y[4]), "+v"(y[5]), "+v"(y[6]), "+v"(y[7]) :: "memory");
                unsigned bad = 0u;
#pragma unroll
                for (int p = 0; p < NB; ++p) bad |= (y[p][1] ^ qtag_hi) | (y[p][3] ^ qtag_hi) | (((y[p][0] ^ qtag_lo) | (y[p][2] ^ qtag_lo)) & 0xffu);
                if (__all(bad == 0u) || s > SPIN_CAP) break;
                __builtin_amdgcn_s_sleep(POLL_SLEEP);
            }
            float s0 = 0.f, s1 = 0.f;
#pragma unroll
            for (int p = 0; p < NB; ++p) { s0 += __builtin_bit_cast(float, y[p][0] & 0xffffff00u); s1 += __builtin_bit_cast(float, y[p][2] & 0xffffff00u); }
            ((LAS unsigned*)(L + Q_OFF))[lane] = pack2(s0, s1);
            wait_ge(ctl + C_QP, 8);
            arrive(ctl + C_QR, lane);
        } else {
            if (w >= PRE) wait_ge(ctl + C_POLL, 1);
            fill(kb + toff(0), vk0, vk1, vk2, vk3, myslot_a);
        }
        STAMP(47);
        VMCNT(0);
        landed();
    } else {
        setup();
        if (w == 4 && lane < 16) ctl[lane] = lane == C_ISS ? 8u : 0u;
        unsigned pf = 0u;
        if (CODE_PF && w == 5 && lane < 16) pf = code_prefetch(lane);
        LGKM0();
        __builtin_amdgcn_s_barrier();
        fill(kb + toff(0), vk0, vk1, vk2, vk3, myslot_a);
        STAMP(43);
        asm volatile("s_waitcnt vmcnt(0)" : "+v"(pf)::"memory");
        landed();
        STAMP(2);
        wait_ge(ctl + C_QP, 4);
        const v2u e = *(volatile LAS v2u*)(ctl + C_TAG);
        const unsigned e0 = e[0], e1 = e[1];
        epoch = ((unsigned long long)rfl(e1) << 32) | rfl(e0);
    }
    const unsigned tag_hi = (unsigned)(epoch >> 8), tag_lo = (unsigned)epoch & 0xffu;
    const unsigned char* vb = vc + (size_t)bh * (SK * DH * 2);
    const __amdgpu_buffer_rsrc_t rgran = __builtin_amdgcn_make_buffer_rsrc((void*)(ws + WS_GRAN), 0, NB * NH * DM * 8, 0x00020000);
    const int gvo = (8 * c + 4 * (g & 1) + 2 * (g >> 1)) * 8, gso = bh * (DM * 8) + w * 1024;
    wait_ge(ctl + C_QR, 1);
    v4u qf[4];
#pragma unroll
    for (int s = 0; s < 4; ++s) qf[s] = *(const LAS v4u*)(L + Q_OFF + (32 * s + 8 * g) * 2);
    LGKM0();
    STAMP(3);

    float mx = -INFINITY;
    v4u wor[32];
    const __amdgpu_buffer_rsrc_t ro = __builtin_amdgcn_make_buffer_rsrc((void*)wo, 0, DM * NH * DH * 2, 0x00020000);
    const int soo = h * DH * (DM * 2) + w * 256;
#pragma unroll
    for (int t = 0; t < 8; ++t) {
        if (t > 0) { take_turn(8 * t + u); fill(kb + toff(t), vk0, vk1, vk2, vk3, myslot_a); issued(8 * t + u); }
        STAMP(8 + t);
        v4u xg[4][2], gw[16];
        if (t > 0) {
            const int l2 = fresh_lane();
            const int voo = 32 * (l2 >> 4) * (DM * 2) + (l2 & 15) * 16;
            v4u* wp = wor + 4 * (t - 1);
#pragma unroll
            for (int r = 0; r < 4; ++r) wp[r] = __builtin_amdgcn_raw_buffer_load_b128(ro, voo, soo + (4 * (t - 1) + r) * (DM * 2), WO_AUX);
            if (t == 1) {
                const __amdgpu_buffer_rsrc_t rx = __builtin_amdgcn_make_buffer_rsrc((void*)xin, 0, NB * DM * 2, 0x00020000);
                const __amdgpu_buffer_rsrc_t rw = __builtin_amdgcn_make_buffer_rsrc((void*)gatew, 0, DM * 2, 0x00020000);
                v4u xv = __builtin_amdgcn_raw_buffer_load_b128(rx, (l2 & 15) * 16, b * (DM * 2) + w * 256, 0);
                v4u gv = __builtin_amdgcn_raw_buffer_load_b128(rw, (l2 & 15) * 16, w * 256, 0);
                asm volatile("s_waitcnt vmcnt(0)" : "+v"(wp[0]), "+v"(wp[1]), "+v"(wp[2]), "+v"(wp[3]), "+v"(xv), "+v"(gv) :: "memory");
                landed();
                if (INVAL) store_dev(v4u{tag_lo ^ 1u, ~tag_hi, tag_lo ^ 1u, ~tag_hi}, rgran, gvo, gso);
                float s = 0.f;
#pragma unroll
                for (int k = 0; k < 4; ++k) s += rbf(bf_lo(xv[k]) * bf_lo(gv[k])) + rbf(bf_hi(xv[k]) * bf_hi(gv[k]));
                s = wave_sum(l2 < 16 ? s : 0.f);
                if (l2 == 0) ((LAS float*)ctl)[C_GP + w] = s;
            } else if (t == GU_T && w >= 4) {
                const int m = (w >> 1) & 1, hf = w & 1;
                const unsigned char* gs = (m ? wu : wg) + (size_t)(128 * b + 64 * hf) * (FF * 2) + fs * (FS * 2);
                const unsigned vog = 16 * (l2 >> 4) * (FF * 2) + ((l2 & 15) < 14 ? (l2 & 15) : 13) * 16;
#pragma unroll
                for (int k = 0; k < 16; ++k)
                    asm volatile("global_load_dwordx4 %0, %1, %2" GUNT : "=v"(gw[k]) : "v"(vog), "s"(gs + (size_t)k * (FF * 2)) : "memory");
                const unsigned char* xs = xin + (128 * b + 64 * hf) * 2;
                const unsigned vox = ((l2 & 15) >> 3) * (DM * 2) + (l2 >> 4) * 32;
#pragma unroll
                for (int j = 0; j < 4; ++j) {
                    asm volatile("global_load_dwordx4 %0, %1, %2" : "=v"(xg[j][0]) : "v"(vox), "s"(xs + j * (2 * DM * 2)) : "memory");
                    asm volatile("global_load_dwordx4 %0, %1, %2 offset:16" : "=v"(xg[j][1]) : "v"(vox), "s"(xs + j * (2 * DM * 2)) : "memory");
                }
                VMCNT(24);
            } else if (t == GU_T + 1 && w >= 4) {
                asm volatile("s_waitcnt vmcnt(0)" : "+v"(wp[0]), "+v"(wp[1]), "+v"(wp[2]), "+v"(wp[3]) :: "memory");
                landed();
                wait_ge(ctl + C_GU, 4);
                const __amdgpu_buffer_rsrc_t rgg = __builtin_amdgcn_make_buffer_rsrc((void*)(ws + WS_GUGRAN), 0, NH * NB * NB * 2048, 0x00020000);
#pragma unroll
                for (int r = 0; r < 2; ++r)
#pragma unroll
                    for (int q = 0; q < 2; ++q) {
                        const int p = q == 0 ? l2 : (l2 < 48 ? 64 + l2 : 111);
                        const int mm = p >= 56 ? 1 : 0, col = 2 * (p - 56 * mm), cons = 2 * (w - 4) + r;
                        const f32x2 v0 = *(const LAS f32x2*)(L + GU_OFF + (((2 * mm) * 8 + cons) * FS + col) * 4);
                        const f32x2 v1 = *(const LAS f32x2*)(L + GU_OFF + (((2 * mm + 1) * 8 + cons) * FS + col) * 4);
                        const float s0 = v0[0] + v1[0], s1 = v0[1] + v1[1];
                        if (q == 0 || l2 < 48)
                            store_dev(v4u{gran(s0, tag_lo), tag_hi, gran(s1, tag_lo), tag_hi}, rgg, p * 16, ((h * NB + cons) * NB + b) * 2048);
                    }
                arrive(ctl + C_GU, l2);
            } else if (t == 7) {
                const __amdgpu_buffer_rsrc_t rgg = __builtin_amdgcn_make_buffer_rsrc((void*)(ws + WS_GUGRAN), 0, NH * NB * NB * 2048, 0x00020000);
                const int sgg = ((h * NB + b) * NB + w) * 2048, v1o = 1024 + (l2 < 48 ? l2 : 47) * 16;
                v4u y0, y1;
                for (unsigned s = 0;; ++s) {
                    y0 = __builtin_amdgcn_raw_buffer_load_b128(rgg, l2 * 16, sgg, 16);
                    y1 = __builtin_amdgcn_raw_buffer_load_b128(rgg, v1o, sgg, 16);
                    asm volatile("s_waitcnt vmcnt(0)" : "+v"(wp[0]), "+v"(wp[1]), "+v"(wp[2]), "+v"(wp[3]), "+v"(y0), "+v"(y1) :: "memory");
                    const unsigned bad = (y0[1] ^ tag_hi) | (y0[3] ^ tag_hi) | (y1[1] ^ tag_hi) | (y1[3] ^ tag_hi) | (((y0[0] ^ tag_lo) | (y0[2] ^ tag_lo) | (y1[0] ^ tag_lo) | (y1[2] ^ tag_lo)) & 0xffu);
                    if (s == 0) landed();
                    if (__all(bad == 0u) || s > SPIN_CAP) break;
                    __builtin_amdgcn_s_sleep(1);
                }
                wait_ge(ctl + C_GU, 8);
                *(LAS v2u*)(L + GU_OFF + w * 896 + l2 * 8) = v2u{y0[0] & 0xffffff00u, y0[2] & 0xffffff00u};
                if (l2 < 48) *(LAS v2u*)(L + GU_OFF + w * 896 + 512 + l2 * 8) = v2u{y1[0] & 0xffffff00u, y1[2] & 0xffffff00u};
                arrive(ctl + C_FFN, l2);
            } else {
                asm volatile("s_waitcnt vmcnt(0)" : "+v"(wp[0]), "+v"(wp[1]), "+v"(wp[2]), "+v"(wp[3]) :: "memory");
            }
            if (t != 7 && t != 1 && !(t == GU_T + 1 && w >= 4)) landed();
        }
        STAMP(16 + t);
        f32x4 s4[4];
#pragma unroll
        for (int kg = 0; kg < 4; ++kg) {
            f32x4 a = {0.f, 0.f, 0.f, 0.f};
#pragma unroll
            for (int s = 0; s < 4; ++s) {
                const v4u kf = *(const LAS v4u*)(myslot + (16 * kg + c) * 256 + (((4 * s + g) ^ c) << 4));
                a = mfma(kf, qf[s], a);
            }
            s4[kg] = a;
        }
        f32x4 v;
#pragma unroll
        for (int i = 0; i < 4; ++i) {
            const float lo = (c & 1) ? s4[1][i] : s4[0][i], hi = (c & 1) ? s4[3][i] : s4[2][i];
            v[i] = rbf(rbf((c & 2) ? hi : lo) * 0.08838834764831845f);
            mx = fmaxf(mx, v[i]);
        }
        if (c < 4) *(LAS f32x4*)(sc + t * 64 + 16 * c + 4 * g) = v;
        __builtin_amdgcn_sched_barrier(0);
        if (t == GU_T && w >= 4) {
            v4u* wp = wor + 4 * (t - 1);
            asm volatile("s_waitcnt vmcnt(0)" : "+v"(gw[0]), "+v"(gw[1]), "+v"(gw[2]), "+v"(gw[3]), "+v"(gw[4]), "+v"(gw[5]), "+v"(gw[6]), "+v"(gw[7]),
                         "+v"(gw[8]), "+v"(gw[9]), "+v"(gw[10]), "+v"(gw[11]), "+v"(gw[12]), "+v"(gw[13]), "+v"(gw[14]), "+v"(gw[15]),
                         "+v"(xg[0][0]), "+v"(xg[0][1]), "+v"(xg[1][0]), "+v"(xg[1][1]), "+v"(xg[2][0]), "+v"(xg[2][1]), "+v"(xg[3][0]), "+v"(xg[3][1]) :: "memory");
            asm volatile("" : "+v"(wp[0]), "+v"(wp[1]), "+v"(wp[2]), "+v"(wp[3]));
            STAMP(44);
            f32x4 a[4];
#pragma unroll
            for (int j = 0; j < 4; ++j) a[j] = f32x4{0.f, 0.f, 0.f, 0.f};
#pragma unroll
            for (int k = 0; k < 16; ++k) {
#pragma unroll
                for (int j = 0; j < 4; ++j) a[j] = mfma(diagA(xg[j][k >> 3][(k >> 1) & 3], sel.s[k & 1]), gw[k], a[j]);
                PIN2(a[0], a[1]); PIN2(a[2], a[3]);
            }
            if (c < 14) {
#pragma unroll
                for (int j = 0; j < 4; ++j)
                    *(LAS f32x4*)(L + GU_OFF + ((((w - 4) * 8 + 2 * j + (g >> 1)) * FS) + 8 * c + 4 * (g & 1)) * 4) = a[j];
            }
            arrive(ctl + C_GU, lane);
            STAMP(56);
        }
    }
    STAMP(4);
    float lsum;
    mx = wave_max(mx);
    {
        const f32x4 sa = *(const LAS f32x4*)(sc + 8 * lane), sb = *(const LAS f32x4*)(sc + 8 * lane + 4);
        float e[8];
#pragma unroll
        for (int i = 0; i < 4; ++i) { e[i] = __expf(sa[i] - mx); e[4 + i] = __expf(sb[i] - mx); }
        lsum = wave_sum(((e[0] + e[1]) + (e[2] + e[3])) + ((e[4] + e[5]) + (e[6] + e[7])));
        unsigned pk[4];
#pragma unroll
        for (int i = 0; i < 4; ++i) pk[i] = pack2(e[i], e[4 + i]);
        LGKM0();
#pragma unroll
        for (int i = 0; i < 4; ++i) *(LAS unsigned*)((LAS unsigned char*)sc + (lane >> 3) * 128 + i * 32 + (lane & 7) * 4) = pk[i];
    }
    if (lane == 0) { ((LAS float*)ctl)[C_WMAX + w] = mx; ((LAS float*)ctl)[C_WSUM + w] = lsum; }
    arrive(ctl + C_SM, lane);
    STAMP(5);

    f32x4 acco = {0.f, 0.f, 0.f, 0.f}, acco1 = {0.f, 0.f, 0.f, 0.f}, accb = {0.f, 0.f, 0.f, 0.f};
    float fw = 0.f, gate = 0.f;
    {
        const unsigned vlin = lane * 16;
        {
            take_turn(64 + u);
            fill(vb + toff(0), vlin, vlin, vlin, vlin, myslot_a);
            issued(64 + u);
            STAMP(24);
            const int voo = 32 * g * (DM * 2) + c * 16;
#pragma unroll
            for (int r = 0; r < 4; ++r) wor[28 + r] = __builtin_amdgcn_raw_buffer_load_b128(ro, voo, soo + (28 + r) * (DM * 2), WO_AUX);
            slot_rows64<4>(myslot, (const LAS unsigned char*)sc, sel, c, g, acco, acco1, []() {}, landed);
            asm volatile("s_waitcnt vmcnt(0)" : "+v"(wor[28]), "+v"(wor[29]), "+v"(wor[30]), "+v"(wor[31]) :: "memory");
            STAMP(32);
        }
        const __amdgpu_buffer_rsrc_t rd = __builtin_amdgcn_make_buffer_rsrc((void*)wd, 0, FF * DM * 2, 0x00020000);
        const int vod = 28 * g * (DM * 2) + c * 16;
#pragma unroll 1
        for (int t = 1; t < 8; ++t) {
            take_turn(64 + 8 * t + u);
            fill(vb + toff(t), vlin, vlin, vlin, vlin, myslot_a);
            issued(64 + 8 * t + u);
            STAMP(24 + t);
            const int tt = t - 1;
            const int sod = (fs * FS + 4 * tt) * (DM * 2) + w * 256;
            v4u dw[4];
#pragma unroll
            for (int r = 0; r < 4; ++r) dw[r] = __builtin_amdgcn_raw_buffer_load_b128(rd, vod, sod + r * (DM * 2), WD_AUX);
            if (t == 1) {
                wait_ge(ctl + C_FFN, NW);
                const int l2 = fresh_lane();
                if (l2 < FS / 2) {
                    float g0 = 0.f, g1 = 0.f, u0 = 0.f, u1 = 0.f;
#pragma unroll
                    for (int ww = 0; ww < NW; ++ww) {
                        const f32x2 a = *(const LAS f32x2*)(L + GU_OFF + ww * 896 + 8 * l2), bb = *(const LAS f32x2*)(L + GU_OFF + ww * 896 + 448 + 8 * l2);
                        g0 += a[0]; g1 += a[1]; u0 += bb[0]; u1 += bb[1];
                    }
                    ((LAS unsigned*)(L + HH_OFF))[l2] = pack2(gelu_tanh(rbf(g0)) * rbf(u0), gelu_tanh(rbf(g1)) * rbf(u1));
                }
                wait_ge(ctl + C_SM, NW);
                float m[NW], M = -INFINITY, Ls = 0.f, z = 0.f;
#pragma unroll
                for (int ww = 0; ww < NW; ++ww) { m[ww] = ((LAS float*)ctl)[C_WMAX + ww]; M = fmaxf(M, m[ww]); }
#pragma unroll
                for (int ww = 0; ww < NW; ++ww) { Ls += __expf(m[ww] - M) * ((LAS float*)ctl)[C_WSUM + ww]; z += ((LAS float*)ctl)[C_GP + ww]; }
                fw = __expf(mx - M) / Ls;
                gate = rbf(1.0f / (1.0f + __expf(-rbf(z))));
                LGKM0();
            }
            unsigned pf = 0u;
            if (CODE_PF2 && t == CODE_PF2 && w == 0 && lane < 16) pf = code_prefetch(lane);
            const v2u hs = *(const LAS v2u*)(L + HH_OFF + (28 * g + 4 * tt) * 2);
            slot_rows64<4>(myslot, (const LAS unsigned char*)sc + t * 128, sel, c, g, acco, acco1, []() {}, landed);
            asm volatile("s_waitcnt vmcnt(0)" : "+v"(dw[0]), "+v"(dw[1]), "+v"(dw[2]), "+v"(dw[3]), "+v"(pf) :: "memory");
            STAMP(32 + t);
#pragma unroll
            for (int r = 0; r < 4; ++r) accb = mfma(diagA(hs[r >> 1], sel.s[r & 1]), dw[r], accb);
        }
    }
    STAMP(6);
    acco = (acco + acco1) * fw;
    if (g < 2) *(LAS f32x4*)((LAS unsigned char*)sc + 1024 + (8 * c + 4 * g) * 4) = acco;
    arrive(ctl + C_O, lane);
    wait_ge(ctl + C_O, NW);
    v4u of[4];
    {
        float s0 = 0.f, s1 = 0.f;
#pragma unroll
        for (int ww = 0; ww < NW; ++ww) {
            const f32x2 v = *(const LAS f32x2*)(L + SC_OFF + ww * 2048 + 1024 + 8 * lane);
            s0 += v[0]; s1 += v[1];
        }
        ((LAS unsigned*)(L + O_OFF))[lane] = pack2(s0, s1);
        LGKM0();
#pragma unroll
        for (int k = 0; k < 4; ++k) of[k] = *(const LAS v4u*)(L + O_OFF + 64 * g + 16 * k);
    }
    STAMP(7);
    f32x4 acca, ac[4];
#pragma unroll
    for (int k = 0; k < 4; ++k) ac[k] = f32x4{0.f, 0.f, 0.f, 0.f};
#pragma unroll
    for (int t = 0; t < 32; t += 4) {
#pragma unroll
        for (int k = 0; k < 4; ++k) ac[k] = mfma(diagA(of[t >> 3][((t + k) >> 1) & 3], sel.s[k & 1]), wor[t + k], ac[k]);
    }
    acca = (ac[0] + ac[1]) + (ac[2] + ac[3]);
    STAMP(40);
    {
        const float omg = rbf(1.0f - gate);
        {
            const float r0 = gate * (g < 2 ? acca[0] : acca[2]) + omg * (g < 2 ? accb[0] : accb[2]);
            const float r1 = gate * (g < 2 ? acca[1] : acca[3]) + omg * (g < 2 ? accb[1] : accb[3]);
            store_dev(v4u{gran(r0, tag_lo), tag_hi, gran(r1, tag_lo), tag_hi}, rgran, gvo, (int)rfl((unsigned)gso));
        }
        const int l2 = fresh_lane();
        const int vo = ((l2 >> 4) * DM + 2 * (l2 & 15)) * 8, so = ((b * NH + 4 * w) * DM + 32 * h) * 8;
        v4u y, ya, yb;
        auto poll = [&](v4u& d) { asm volatile("buffer_load_dwordx4 %0, %1, %2, %3 offen sc1" : "=v"(d) : "v"(vo), "s"(rgran), "s"(so) : "memory"); };
        auto good = [&](const v4u& d) { return __all(((d[1] ^ tag_hi) | (d[3] ^ tag_hi) | (((d[0] ^ tag_lo) | (d[2] ^ tag_lo)) & 0xffu)) == 0u); };
        poll(ya);
        __builtin_amdgcn_s_sleep(POLL_GAP);
        poll(yb);
        for (unsigned s = 0;; ++s) {
            asm volatile("s_waitcnt vmcnt(1)" : "+v"(ya)::"memory");
            if (good(ya) || s > SPIN_CAP) { y = ya; break; }
            poll(ya);
            asm volatile("s_waitcnt vmcnt(1)" : "+v"(yb)::"memory");
            if (good(yb)) { y = yb; break; }
            poll(yb);
        }
        STAMP(41);
        float v0 = __builtin_bit_cast(float, y[0] & 0xffffff00u), v1 = __builtin_bit_cast(float, y[2] & 0xffffff00u);
        v0 += __shfl_xor(v0, 16); v1 += __shfl_xor(v1, 16);
        v0 += __shfl_xor(v0, 32); v1 += __shfl_xor(v1, 32);
        if (l2 < 16) *(LAS f32x2*)(L + GU_OFF + w * 128 + l2 * 8) = f32x2{v0, v1};
        LGKM0();
        unsigned old = 0u;
        if (l2 == 0) old = __hip_atomic_fetch_add(ctl + C_OUT, 1u, RLX_WG);
        if (rfl(old) == NW - 1) {
            CBAR();
            if (l2 < 16) {
                float s0 = 0.f, s1 = 0.f;
#pragma unroll
                for (int ww = 0; ww < NW; ++ww) { const f32x2 v = *(const LAS f32x2*)(L + GU_OFF + ww * 128 + l2 * 8); s0 += v[0]; s1 += v[1]; }
                __hip_atomic_store((gu32*)(out + ((size_t)b * DM + 32 * h + 2 * l2) * 2), pack2(s0, s1), __ATOMIC_RELAXED, __HIP_MEMORY_SCOPE_SYSTEM);
            }
            if (bh == 0 && l2 == 0) __hip_atomic_store((gu64*)(ws + WS_EPOCH), epoch, RLX_AGENT);
        }
        STAMP(57);
        asm volatile("" ::"v"(ya), "v"(yb));
    }
    SDUMP();
    asm volatile(".Lfdk_end:");
}

}

extern "C" void kernel_launch(void* const* d_in, const int* in_sizes, int n_in, void* d_out, int out_size, void* d_ws, size_t ws_size,
                              hipStream_t stream) {
    (void)in_sizes; (void)n_in; (void)out_size; (void)ws_size;
    (void)hipFuncSetAttribute((const void*)fused_decode_kernel, hipFuncAttributeMaxDynamicSharedMemorySize, LDS_BYTES);
    int pace = PACE, tstart = TSTART, depth = DEPTH;
    fused_decode_kernel<<<dim3(NB * NH), dim3(NTHR), LDS_BYTES, stream>>>(
        (const unsigned char*)d_in[0], (const unsigned char*)d_in[1], (const unsigned char*)d_in[2], (const unsigned char*)d_in[3],
        (const unsigned char*)d_in[4], (const unsigned char*)d_in[6], (const unsigned char*)d_in[7],
        (const unsigned char*)d_in[8], (const unsigned char*)d_in[9], (unsigned char*)d_out, (unsigned char*)d_ws, pace, tstart, depth);
}
```

```cpp
#define LAGSTEP 64
#define LAGMAX 3
#define PACE 928
#include <hip/hip_runtime.h>
#include <hip/hip_bf16.h>

#define GAS __attribute__((address_space(1)))
#define LAS __attribute__((address_space(3)))
typedef unsigned v4u __attribute__((ext_vector_type(4)));
typedef unsigned v2u __attribute__((ext_vector_type(2)));
typedef float f32x4 __attribute__((ext_vector_type(4)));
typedef float f32x2 __attribute__((ext_vector_type(2)));
typedef short bf16x8 __attribute__((ext_vector_type(8)));
typedef GAS unsigned gu32;
typedef GAS unsigned long long gu64;
#define RLX_AGENT __ATOMIC_RELAXED, __HIP_MEMORY_SCOPE_AGENT
#define RLX_WG __ATOMIC_RELAXED, __HIP_MEMORY_SCOPE_WORKGROUP

namespace {

constexpr int NB = 8, DM = 1024, NH = 32, SK = 4096, DH = 128, FF = 3584;
constexpr int FS = FF / NH;
constexpr int NW = 8, NTHR = 64 * NW;
constexpr int SLOT = 16384;
#define GUNT " nt"
#define ROTK 0
#define PRE 1
#define GU_T 2
#define WO_AUX 0
#define WD_AUX 0
#define POLL_SLEEP 1
#define QNT " nt"
#define CODE_PF 1
#define CODE_PF2 5
#define POLL_GAP 10
#define HOLE1 65
#define HOLE2 5
#define INVAL 0
#define TSTART 600
#define DEPTH 4

constexpr int SC_OFF = NW * SLOT;
constexpr int GU_OFF = SC_OFF + NW * 2048;
constexpr int Q_OFF = GU_OFF + 14336;
constexpr int O_OFF = Q_OFF + 256;
constexpr int HH_OFF = O_OFF + 256;
constexpr int CTL_OFF = HH_OFF + 256;
constexpr int LDS_BYTES = CTL_OFF + 256;
constexpr int C_ISS = 0, C_LAND = 1, C_FFN = 2, C_O = 3, C_QP = 4, C_QR = 5, C_GU = 6, C_OUT = 7, C_TAG = 8, C_POLL = 10, C_SM = 11, C_WMAX = 16, C_WSUM = 24, C_GP = 32;
static_assert(LDS_BYTES <= 160 * 1024, "LDS");

constexpr size_t WS_EPOCH = 0, WS_GRAN = 4096, WS_QGRAN = WS_GRAN + (size_t)NB * NH * DM * 8, WS_GUGRAN = WS_QGRAN + (size_t)NH * NB * NB * DH * 8;

constexpr unsigned SPIN_CAP = 1u << 22;
#define STAMP(i) do {} while (0)
#define SDUMP() do {} while (0)

__device__ __forceinline__ f32x4 mfma(v4u a, v4u b, f32x4 c) {
    return __builtin_amdgcn_mfma_f32_16x16x32_bf16(__builtin_bit_cast(bf16x8, a), __builtin_bit_cast(bf16x8, b), c, 0, 0, 0);
}
__device__ __forceinline__ float bf_lo(unsigned u) { return __builtin_bit_cast(float, u << 16); }
__device__ __forceinline__ float bf_hi(unsigned u) { return __builtin_bit_cast(float, u & 0xffff0000u); }
__device__ __forceinline__ float rbf(float f) { return (float)(__bf16)f; }
__device__ __forceinline__ unsigned bf_bits(float f) { return (unsigned)__builtin_bit_cast(unsigned short, (__bf16)f); }
__device__ __forceinline__ unsigned pack2(float lo, float hi) { return bf_bits(lo) | (bf_bits(hi) << 16); }
__device__ __forceinline__ unsigned gran(float f, unsigned tag_lo) { return ((__builtin_bit_cast(unsigned, f) + 0x80u) & 0xffffff00u) | tag_lo; }
__device__ __forceinline__ void store_dev(v4u d, __amdgpu_buffer_rsrc_t r, int vo, int so) {
    __builtin_amdgcn_raw_buffer_store_b128(d, r, vo, so, 16);
    asm volatile("s_nop 1" ::"v"(d) : "memory");
}
__device__ __forceinline__ unsigned long long rt() { return __builtin_amdgcn_s_memrealtime(); }
__device__ __forceinline__ unsigned rfl(unsigned v) { return (unsigned)__builtin_amdgcn_readfirstlane((int)v); }
#define LGKM0() asm volatile("s_waitcnt lgkmcnt(0)" ::: "memory")
#define VMCNT(n) asm volatile("s_waitcnt vmcnt(%0)" ::"i"(n) : "memory")
#define CBAR() asm volatile("" ::: "memory")
#define PIN2(a, b) do { asm volatile("" : "+v"(a), "+v"(b)); __builtin_amdgcn_sched_barrier(0); } while (0)

__device__ __forceinline__ void glds4(const void* sbase, unsigned v0, unsigned v1, unsigned v2, unsigned v3, unsigned lds) {
    unsigned keep;
    asm volatile("s_mov_b32 %0, m0\n\ts_mov_b32 m0, %6\n\ts_nop 0\n\t"
                 "global_load_lds_dwordx4 %1, %5 nt\n\t"
                 "global_load_lds_dwordx4 %2, %5 offset:1024 nt\n\t"
                 "global_load_lds_dwordx4 %3, %5 offset:2048 nt\n\t"
                 "global_load_lds_dwordx4 %4, %5 offset:3072 nt\n\t"
                 "s_mov_b32 m0, %0"
                 : "=&s"(keep) : "v"(v0), "v"(v1), "v"(v2), "v"(v3), "s"(sbase), "s"(lds) : "memory");
}
__device__ __forceinline__ void fill(const unsigned char* src, unsigned v0, unsigned v1, unsigned v2, unsigned v3, unsigned lds) {
    LGKM0();
#pragma unroll
    for (int q = 0; q < 4; ++q) glds4(src + q * 4096, v0, v1, v2, v3, lds + q * 4096);
}

__device__ __forceinline__ void wait_ge(LAS unsigned* w, unsigned need) {
    for (unsigned s = 0; rfl(*(volatile LAS unsigned*)w) < need && s < SPIN_CAP; ++s) __builtin_amdgcn_s_sleep(0);
    CBAR();
}
__device__ __forceinline__ void arrive(LAS unsigned* w, int lane) {
    LGKM0();
    if (lane == 0) __hip_atomic_fetch_add(w, 1u, RLX_WG);
    CBAR();
}

struct Sel { unsigned s[2][4]; };
__device__ __forceinline__ v4u diagA(unsigned S, const unsigned (&sel)[4]) {
    return v4u{__builtin_amdgcn_perm(0u, S, sel[0]), __builtin_amdgcn_perm(0u, S, sel[1]), __builtin_amdgcn_perm(0u, S, sel[2]), __builtin_amdgcn_perm(0u, S, sel[3])};
}
__device__ __forceinline__ int fresh_lane() {
    int l;
    asm volatile("v_mbcnt_lo_u32_b32 %0, -1, 0\n\tv_mbcnt_hi_u32_b32 %0, -1, %0" : "=v"(l));
    __builtin_assume((unsigned)l < 64u);
    return l;
}
__device__ __forceinline__ Sel make_sel(int c) {
    Sel sel;
    const int dm = (c & 7) >> 1, hf = c & 1;
#pragma unroll
    for (int par = 0; par < 2; ++par)
#pragma unroll
        for (int d = 0; d < 4; ++d) {
            const unsigned lohi = (unsigned)(2 * par) | ((unsigned)(2 * par + 1) << 8);
            const unsigned v = hf ? (0x0c0cu | (lohi << 16)) : (0x0c0c0000u | lohi);
            sel.s[par][d] = (c < 8 && d == dm) ? v : 0x0c0c0c0cu;
        }
    return sel;
}

template <int LATER, typename F, typename G>
__device__ __forceinline__ void slot_rows64(const LAS unsigned char* slot, const LAS unsigned char* vec, const Sel& sel, int c, int g,
                                            f32x4& acc, f32x4& a1, F&& last_wait, G&& all_landed) {
    const LAS unsigned char* base = slot + g * 256 + c * 16;
    const v4u s0 = *(const LAS v4u*)(vec + g * 32), s1 = *(const LAS v4u*)(vec + g * 32 + 16);
#pragma unroll
    for (int q = 0; q < 4; ++q) {
        VMCNT(LATER + 12 - 4 * q);
        v4u bb[4];
#pragma unroll
        for (int t = 0; t < 4; ++t) bb[t] = *(const LAS v4u*)(base + (4 * q + t) * 1024);
        if (q == 3) { last_wait(); all_landed(); }
        __builtin_amdgcn_sched_barrier(0);
#pragma unroll
        for (int u = 0; u < 2; ++u) {
            const unsigned S = (q < 2 ? s0 : s1)[2 * (q & 1) + u];
            acc = mfma(diagA(S, sel.s[0]), bb[2 * u], acc);
            a1 = mfma(diagA(S, sel.s[1]), bb[2 * u + 1], a1);
            PIN2(acc, a1);
        }
    }
}

__device__ __forceinline__ float gelu_tanh(float x) {
    const float y = 0.7978845608028654f * (x + 0.044715f * x * x * x);
    return rbf(x / (1.0f + __expf(-2.0f * y)));
}
__device__ __forceinline__ float wave_max(float v) {
#pragma unroll
    for (int o = 32; o >= 1; o >>= 1) v = fmaxf(v, __shfl_xor(v, o));
    return v;
}
__device__ __forceinline__ float wave_sum(float v) {
#pragma unroll
    for (int o = 32; o >= 1; o >>= 1) v += __shfl_xor(v, o);
    return v;
}

__global__ void __launch_bounds__(NTHR)
fused_decode_kernel(const unsigned char* __restrict__ xin, const unsigned char* __restrict__ kc, const unsigned char* __restrict__ vc,
                    const unsigned char* __restrict__ wq, const unsigned char* __restrict__ wo,
                    const unsigned char* __restrict__ wg, const unsigned char* __restrict__ wu, const unsigned char* __restrict__ wd,
                    const unsigned char* __restrict__ gatew, unsigned char* __restrict__ out, unsigned char* __restrict__ ws,
                    int pace, int tstart, int depth) {
    extern __shared__ __attribute__((aligned(16))) unsigned char smem[];
    LAS unsigned char* L = (LAS unsigned char*)smem;
    LAS unsigned* ctl = (LAS unsigned*)(L + CTL_OFF);
    asm volatile(".Lfdk_begin:");
    const unsigned long long t0 = rt();
    asm volatile("" ::"s"(xin), "s"(kc), "s"(vc), "s"(wq), "s"(wo), "s"(wg), "s"(wu), "s"(wd), "s"(gatew), "s"(out), "s"(ws), "s"(pace), "s"(tstart), "s"(depth));
    const int w = __builtin_amdgcn_readfirstlane((int)threadIdx.x >> 6);
    const int h = (blockIdx.x & 7) * 4 + ((blockIdx.x >> 3) & 3), b = blockIdx.x >> 5;
    const int fs = h, bh = b * NH + h;
    STAMP(0);
    const unsigned myslot_a = (unsigned)(size_t)L + w * SLOT;
    const LAS unsigned char* myslot = L + w * SLOT;
    const unsigned rot = (unsigned)(ROTK) + (w ^ 4);
    auto toff = [&](int t) { return (size_t)(((8u * t + rot) & 63u) * SLOT); };
    int lane, c, g;
    unsigned vk0, vk1, vk2, vk3;
    Sel sel;
    const unsigned char* kb;
    auto setup = [&]() {
        lane = fresh_lane(); c = lane & 15; g = lane >> 4;
        vk0 = g * 256 + ((c ^ g) << 4); vk1 = g * 256 + ((c ^ (4 + g)) << 4); vk2 = g * 256 + ((c ^ (8 + g)) << 4); vk3 = g * 256 + ((c ^ (12 + g)) << 4);
        sel = make_sel(c & 7);
        int bh2 = bh;
        asm volatile("" : "+s"(bh2));
        kb = kc + (size_t)bh2 * (SK * DH * 2);
    };
    auto code_prefetch = [&](int l) {
        unsigned long long pc;
        unsigned off, len, v = 0u;
        asm volatile("s_getpc_b64 %0\n.Lfdk_pc%=:\n\ts_mov_b32 %1, .Lfdk_pc%=-.Lfdk_begin\n\ts_mov_b32 %2, .Lfdk_end-.Lfdk_begin" : "=s"(pc), "=s"(off), "=s"(len));
        const unsigned o = (((blockIdx.x >> 3) & 31u) + 32u * (unsigned)l) * 128u;
        if (o < len) asm volatile("global_load_dword %0, %1, %2" : "+v"(v) : "v"(o), "s"(pc - off) : "memory");
        return v;
    };
    const int u = w ^ 4;
    auto take_turn = [&](int f) {
        const int hole = HOLE1 * min(max(f - 8 * GU_T, 0), 4) + HOLE2 * min(max(f - 56, 0), 8);
        const unsigned long long td = t0 + (unsigned)(tstart + hole + (((f - 8) * pace) >> 4));
        while (rt() < td) __builtin_amdgcn_s_sleep(1);
        for (unsigned s = 0; s < SPIN_CAP; ++s) {
            const v2u st = *(volatile LAS v2u*)(ctl + C_ISS);
            const unsigned extra = min(((unsigned)rt() - (unsigned)td) / (unsigned)LAGSTEP, (unsigned)LAGMAX);
            if (rfl(st[0]) >= (unsigned)f && rfl(st[1]) + (unsigned)depth + extra > (unsigned)f) break;
            __builtin_amdgcn_s_sleep(0);
        }
        CBAR();
    };
    auto issued = [&](int f) { if (lane == 0) *(volatile LAS unsigned*)(ctl + C_ISS) = (unsigned)f + 1u; CBAR(); };
    auto landed = [&]() { if (lane == 0) __hip_atomic_fetch_add(ctl + C_LAND, 1u, RLX_WG); CBAR(); };
    LAS float* sc = (LAS float*)(L + SC_OFF + w * 2048);
    unsigned long long epoch;

    if (__builtin_expect(w < 4, 1)) {
        v4u x8[4], wr[8];
        v2u ep;
        const unsigned l0 = threadIdx.x & 63;
        const unsigned char* qs = wq + (size_t)(128 * b + 32 * w) * (NH * DH * 2) + h * 256;
        const unsigned vq = (l0 >> 4) * 8 * (NH * DH * 2) + (l0 & 15) * 16;
#pragma unroll
        for (int t = 0; t < 8; ++t)
            asm volatile("global_load_dwordx4 %0, %1, %2" QNT : "=v"(wr[t]) : "v"(vq), "s"(qs + (size_t)t * (NH * DH * 2)) : "memory");
        __builtin_amdgcn_sched_barrier(0);
        const unsigned char* xs = xin + (128 * b + 32 * w) * 2;
        const unsigned vx = ((l0 >> 3) & 1) * (DM * 2) + (l0 >> 4) * 16;
#pragma unroll
        for (int j = 0; j < 4; ++j)
            asm volatile("global_load_dwordx4 %0, %1, %2" : "=v"(x8[j]) : "v"(vx), "s"(xs + j * (2 * DM * 2)) : "memory");
        asm volatile("global_load_dwordx2 %0, %1, %2 sc1" : "=v"(ep) : "v"(0u), "s"(ws + WS_EPOCH) : "memory");
        const unsigned char* inbox = ws + WS_QGRAN + (size_t)(h * NB + b) * NB * (DH * 8);
        unsigned warm;
        asm volatile("global_load_dword %0, %1, %2 sc1" : "=v"(warm) : "v"(l0 * 16), "s"(inbox) : "memory");
        STAMP(42);
        __builtin_amdgcn_s_barrier();
        setup();
        VMCNT(0);
        asm volatile("" : "+v"(x8[0]), "+v"(x8[1]), "+v"(x8[2]), "+v"(x8[3]), "+v"(wr[0]), "+v"(wr[1]), "+v"(wr[2]), "+v"(wr[3]),
                     "+v"(wr[4]), "+v"(wr[5]), "+v"(wr[6]), "+v"(wr[7]), "+v"(warm), "+v"(ep) :: "memory");
        STAMP(45);
        {
            const unsigned e0 = ep[0], e1 = ep[1];
            epoch = (((unsigned long long)rfl(e1) << 32) | rfl(e0)) + 1ull;
        }
        const unsigned qtag_hi = (unsigned)(epoch >> 8), qtag_lo = (unsigned)epoch & 0xffu;
#pragma unroll
        for (int j = 0; j < 4; ++j) {
            f32x4 a0 = {0.f, 0.f, 0.f, 0.f}, a1 = {0.f, 0.f, 0.f, 0.f};
#pragma unroll
            for (int k = 0; k < 4; ++k) {
                a0 = mfma(diagA(x8[j][k], sel.s[0]), wr[2 * k], a0);
                a1 = mfma(diagA(x8[j][k], sel.s[1]), wr[2 * k + 1], a1);
            }
            a0 += a1;
            *(LAS f32x4*)(L + SC_OFF + ((w * 8 + 2 * j + (g >> 1)) * 128 + 8 * c + 4 * (g & 1)) * 4) = a0;
        }
        if (w == 0 && lane == 0) *(LAS v2u*)(ctl + C_TAG) = v2u{(unsigned)epoch, (unsigned)(epoch >> 32)};
        arrive(ctl + C_QP, lane);
        wait_ge(ctl + C_QP, 4);
        STAMP(1);
        {
            const __amdgpu_buffer_rsrc_t rqg = __builtin_amdgcn_make_buffer_rsrc((void*)(ws + WS_QGRAN), 0, NH * NB * NB * DH * 8, 0x00020000);
#pragma unroll
            for (int r = 0; r < 2; ++r) {
                float s0 = 0.f, s1 = 0.f;
#pragma unroll
                for (int k = 0; k < 4; ++k) { const f32x2 v = *(const LAS f32x2*)(L + SC_OFF + ((k * 8 + 2 * w + r) * 128 + 2 * lane) * 4); s0 += v[0]; s1 += v[1]; }
                store_dev(v4u{gran(s0, qtag_lo), qtag_hi, gran(s1, qtag_lo), qtag_hi}, rqg, lane * 16, ((h * NB + 2 * w + r) * NB + b) * (DH * 8));
            }
        }
        arrive(ctl + C_QP, lane);
        STAMP(46);
        if (w == 3) {
            VMCNT(0);
            STAMP(55);
            const unsigned vo = lane * 16;
            v4u y[NB];
            for (unsigned s = 0;; ++s) {
#pragma unroll
                for (int p = 0; p < NB; ++p)
                    asm volatile("global_load_dwordx4 %0, %1, %2 offset:%3 sc1" : "=v"(y[p]) : "v"(vo), "s"(inbox + (p >> 2) * 4096), "i"((p & 3) * 1024) : "memory");
                if (s == 0) {
                    if (lane == 0) *(volatile LAS unsigned*)(ctl + C_POLL) = 1u;
                    fill(kb + toff(0), vk0, vk1, vk2, vk3, myslot_a);
                    VMCNT(16);
                } else {
                    VMCNT(0);
                }
                asm volatile("" : "+v"(y[0]), "+v"(y[1]), "+v"(y[2]), "+v"(y[3]), "+v"(y[4]), "+v"(y[5]), "+v"(y[6]), "+v"(y[7]) :: "memory");
                unsigned bad = 0u;
#pragma unroll
                for (int p = 0; p < NB; ++p) bad |= (y[p][1] ^ qtag_hi) | (y[p][3] ^ qtag_hi) | (((y[p][0] ^ qtag_lo) | (y[p][2] ^ qtag_lo)) & 0xffu);
                if (__all(bad == 0u) || s > SPIN_CAP) break;
                __builtin_amdgcn_s_sleep(POLL_SLEEP);
            }
            float s0 = 0.f, s1 = 0.f;
#pragma unroll
            for (int p = 0; p < NB; ++p) { s0 += __builtin_bit_cast(float, y[p][0] & 0xffffff00u); s1 += __builtin_bit_cast(float, y[p][2] & 0xffffff00u); }
            ((LAS unsigned*)(L + Q_OFF))[lane] = pack2(s0, s1);
            wait_ge(ctl + C_QP, 8);
            arrive(ctl + C_QR, lane);
        } else {
            if (w >= PRE) wait_ge(ctl + C_POLL, 1);
            fill(kb + toff(0), vk0, vk1, vk2, vk3, myslot_a);
        }
        STAMP(47);
        VMCNT(0);
        landed();
    } else {
        setup();
        if (w == 4 && lane < 16) ctl[lane] = lane == C_ISS ? 8u : 0u;
        unsigned pf = 0u;
        if (CODE_PF && w == 5 && lane < 16) pf = code_prefetch(lane);
        LGKM0();
        __builtin_amdgcn_s_barrier();
        fill(kb + toff(0), vk0, vk1, vk2, vk3, myslot_a);
        STAMP(43);
        asm volatile("s_waitcnt vmcnt(0)" : "+v"(pf)::"memory");
        landed();
        STAMP(2);
        wait_ge(ctl + C_QP, 4);
        const v2u e = *(volatile LAS v2u*)(ctl + C_TAG);
        const unsigned e0 = e[0], e1 = e[1];
        epoch = ((unsigned long long)rfl(e1) << 32) | rfl(e0);
    }
    const unsigned tag_hi = (unsigned)(epoch >> 8), tag_lo = (unsigned)epoch & 0xffu;
    const unsigned char* vb = vc + (size_t)bh * (SK * DH * 2);
    const __amdgpu_buffer_rsrc_t rgran = __builtin_amdgcn_make_buffer_rsrc((void*)(ws + WS_GRAN), 0, NB * NH * DM * 8, 0x00020000);
    const int gvo = (8 * c + 4 * (g & 1) + 2 * (g >> 1)) * 8, gso = bh * (DM * 8) + w * 1024;
    wait_ge(ctl + C_QR, 1);
    v4u qf[4];
#pragma unroll
    for (int s = 0; s < 4; ++s) qf[s] = *(const LAS v4u*)(L + Q_OFF + (32 * s + 8 * g) * 2);
    LGKM0();
    STAMP(3);

    float mx = -INFINITY;
    v4u wor[32];
    const __amdgpu_buffer_rsrc_t ro = __builtin_amdgcn_make_buffer_rsrc((void*)wo, 0, DM * NH * DH * 2, 0x00020000);
    const int soo = h * DH * (DM * 2) + w * 256;
#pragma unroll
    for (int t = 0; t < 8; ++t) {
        if (t > 0) { take_turn(8 * t + u); fill(kb + toff(t), vk0, vk1, vk2, vk3, myslot_a); issued(8 * t + u); }
        STAMP(8 + t);
        v4u xg[4][2], gw[16];
        if (t > 0) {
            const int l2 = fresh_lane();
            const int voo = 32 * (l2 >> 4) * (DM * 2) + (l2 & 15) * 16;
            v4u* wp = wor + 4 * (t - 1);
#pragma unroll
            for (int r = 0; r < 4; ++r) wp[r] = __builtin_amdgcn_raw_buffer_load_b128(ro, voo, soo + (4 * (t - 1) + r) * (DM * 2), WO_AUX);
            if (t == 1) {
                const __amdgpu_buffer_rsrc_t rx = __builtin_amdgcn_make_buffer_rsrc((void*)xin, 0, NB * DM * 2, 0x00020000);
                const __amdgpu_buffer_rsrc_t rw = __builtin_amdgcn_make_buffer_rsrc((void*)gatew, 0, DM * 2, 0x00020000);
                v4u xv = __builtin_amdgcn_raw_buffer_load_b128(rx, (l2 & 15) * 16, b * (DM * 2) + w * 256, 0);
                v4u gv = __builtin_amdgcn_raw_buffer_load_b128(rw, (l2 & 15) * 16, w * 256, 0);
                asm volatile("s_waitcnt vmcnt(0)" : "+v"(wp[0]), "+v"(wp[1]), "+v"(wp[2]), "+v"(wp[3]), "+v"(xv), "+v"(gv) :: "memory");
                landed();
                if (INVAL) store_dev(v4u{tag_lo ^ 1u, ~tag_hi, tag_lo ^ 1u, ~tag_hi}, rgran, gvo, gso);
                float s = 0.f;
#pragma unroll
                for (int k = 0; k < 4; ++k) s += rbf(bf_lo(xv[k]) * bf_lo(gv[k])) + rbf(bf_hi(xv[k]) * bf_hi(gv[k]));
                s = wave_sum(l2 < 16 ? s : 0.f);
                if (l2 == 0) ((LAS float*)ctl)[C_GP + w] = s;
            } else if (t == GU_T && w >= 4) {
                const int m = (w >> 1) & 1, hf = w & 1;
                const unsigned char* gs = (m ? wu : wg) + (size_t)(128 * b + 64 * hf) * (FF * 2) + fs * (FS * 2);
                const unsigned vog = 16 * (l2 >> 4) * (FF * 2) + ((l2 & 15) < 14 ? (l2 & 15) : 13) * 16;
#pragma unroll
                for (int k = 0; k < 16; ++k)
                    asm volatile("global_load_dwordx4 %0, %1, %2" GUNT : "=v"(gw[k]) : "v"(vog), "s"(gs + (size_t)k * (FF * 2)) : "memory");
                const unsigned char* xs = xin + (128 * b + 64 * hf) * 2;
                const unsigned vox = ((l2 & 15) >> 3) * (DM * 2) + (l2 >> 4) * 32;
#pragma unroll
                for (int j = 0; j < 4; ++j) {
                    asm volatile("global_load_dwordx4 %0, %1, %2" : "=v"(xg[j][0]) : "v"(vox), "s"(xs + j * (2 * DM * 2)) : "memory");
                    asm volatile("global_load_dwordx4 %0, %1, %2 offset:16" : "=v"(xg[j][1]) : "v"(vox), "s"(xs + j * (2 * DM * 2)) : "memory");
                }
                VMCNT(24);
            } else if (t == GU_T + 1 && w >= 4) {
                asm volatile("s_waitcnt vmcnt(0)" : "+v"(wp[0]), "+v"(wp[1]), "+v"(wp[2]), "+v"(wp[3]) :: "memory");
                landed();
                wait_ge(ctl + C_GU, 4);
                const __amdgpu_buffer_rsrc_t rgg = __builtin_amdgcn_make_buffer_rsrc((void*)(ws + WS_GUGRAN), 0, NH * NB * NB * 2048, 0x00020000);
#pragma unroll
                for (int r = 0; r < 2; ++r)
#pragma unroll
                    for (int q = 0; q < 2; ++q) {
                        const int p = q == 0 ? l2 : (l2 < 48 ? 64 + l2 : 111);
                        const int mm = p >= 56 ? 1 : 0, col = 2 * (p - 56 * mm), cons = 2 * (w - 4) + r;
                        const f32x2 v0 = *(const LAS f32x2*)(L + GU_OFF + (((2 * mm) * 8 + cons) * FS + col) * 4);
                        const f32x2 v1 = *(const LAS f32x2*)(L + GU_OFF + (((2 * mm + 1) * 8 + cons) * FS + col) * 4);
                        const float s0 = v0[0] + v1[0], s1 = v0[1] + v1[1];
                        if (q == 0 || l2 < 48)
                            store_dev(v4u{gran(s0, tag_lo), tag_hi, gran(s1, tag_lo), tag_hi}, rgg, p * 16, ((h * NB + cons) * NB + b) * 2048);
                    }
                arrive(ctl + C_GU, l2);
            } else if (t == 7) {
                const __amdgpu_buffer_rsrc_t rgg = __builtin_amdgcn_make_buffer_rsrc((void*)(ws + WS_GUGRAN), 0, NH * NB * NB * 2048, 0x00020000);
                const int sgg = ((h * NB + b) * NB + w) * 2048, v1o = 1024 + (l2 < 48 ? l2 : 47) * 16;
                v4u y0, y1;
                for (unsigned s = 0;; ++s) {
                    y0 = __builtin_amdgcn_raw_buffer_load_b128(rgg, l2 * 16, sgg, 16);
                    y1 = __builtin_amdgcn_raw_buffer_load_b128(rgg, v1o, sgg, 16);
                    asm volatile("s_waitcnt vmcnt(0)" : "+v"(wp[0]), "+v"(wp[1]), "+v"(wp[2]), "+v"(wp[3]), "+v"(y0), "+v"(y1) :: "memory");
                    const unsigned bad = (y0[1] ^ tag_hi) | (y0[3] ^ tag_hi) | (y1[1] ^ tag_hi) | (y1[3] ^ tag_hi) | (((y0[0] ^ tag_lo) | (y0[2] ^ tag_lo) | (y1[0] ^ tag_lo) | (y1[2] ^ tag_lo)) & 0xffu);
                    if (s == 0) landed();
                    if (__all(bad == 0u) || s > SPIN_CAP) break;
                    __builtin_amdgcn_s_sleep(1);
                }
                wait_ge(ctl + C_GU, 8);
                *(LAS v2u*)(L + GU_OFF + w * 896 + l2 * 8) = v2u{y0[0] & 0xffffff00u, y0[2] & 0xffffff00u};
                if (l2 < 48) *(LAS v2u*)(L + GU_OFF + w * 896 + 512 + l2 * 8) = v2u{y1[0] & 0xffffff00u, y1[2] & 0xffffff00u};
                arrive(ctl + C_FFN, l2);
            } else {
                asm volatile("s_waitcnt vmcnt(0)" : "+v"(wp[0]), "+v"(wp[1]), "+v"(wp[2]), "+v"(wp[3]) :: "memory");
            }
            if (t != 7 && t != 1 && !(t == GU_T + 1 && w >= 4)) landed();
        }
        STAMP(16 + t);
        f32x4 s4[4];
#pragma unroll
        for (int kg = 0; kg < 4; ++kg) {
            f32x4 a = {0.f, 0.f, 0.f, 0.f};
#pragma unroll
            for (int s = 0; s < 4; ++s) {
                const v4u kf = *(const LAS v4u*)(myslot + (16 * kg + c) * 256 + (((4 * s + g) ^ c) << 4));
                a = mfma(kf, qf[s], a);
            }
            s4[kg] = a;
        }
        f32x4 v;
#pragma unroll
        for (int i = 0; i < 4; ++i) {
            const float lo = (c & 1) ? s4[1][i] : s4[0][i], hi = (c & 1) ? s4[3][i] : s4[2][i];
            v[i] = rbf(rbf((c & 2) ? hi : lo) * 0.08838834764831845f);
            mx = fmaxf(mx, v[i]);
        }
        if (c < 4) *(LAS f32x4*)(sc + t * 64 + 16 * c + 4 * g) = v;
        __builtin_amdgcn_sched_barrier(0);
        if (t == GU_T && w >= 4) {
            v4u* wp = wor + 4 * (t - 1);
            asm volatile("s_waitcnt vmcnt(0)" : "+v"(gw[0]), "+v"(gw[1]), "+v"(gw[2]), "+v"(gw[3]), "+v"(gw[4]), "+v"(gw[5]), "+v"(gw[6]), "+v"(gw[7]),
                         "+v"(gw[8]), "+v"(gw[9]), "+v"(gw[10]), "+v"(gw[11]), "+v"(gw[12]), "+v"(gw[13]), "+v"(gw[14]), "+v"(gw[15]),
                         "+v"(xg[0][0]), "+v"(xg[0][1]), "+v"(xg[1][0]), "+v"(xg[1][1]), "+v"(xg[2][0]), "+v"(xg[2][1]), "+v"(xg[3][0]), "+v"(xg[3][1]) :: "memory");
            asm volatile("" : "+v"(wp[0]), "+v"(wp[1]), "+v"(wp[2]), "+v"(wp[3]));
            STAMP(44);
            f32x4 a[4];
#pragma unroll
            for (int j = 0; j < 4; ++j) a[j] = f32x4{0.f, 0.f, 0.f, 0.f};
#pragma unroll
            for (int k = 0; k < 16; ++k) {
#pragma unroll
                for (int j = 0; j < 4; ++j) a[j] = mfma(diagA(xg[j][k >> 3][(k >> 1) & 3], sel.s[k & 1]), gw[k], a[j]);
                PIN2(a[0], a[1]); PIN2(a[2], a[3]);
            }
            if (c < 14) {
#pragma unroll
                for (int j = 0; j < 4; ++j)
                    *(LAS f32x4*)(L + GU_OFF + ((((w - 4) * 8 + 2 * j + (g >> 1)) * FS) + 8 * c + 4 * (g & 1)) * 4) = a[j];
            }
            arrive(ctl + C_GU, lane);
            STAMP(56);
        }
    }
    STAMP(4);
    float lsum;
    mx = wave_max(mx);
    {
        const f32x4 sa = *(const LAS f32x4*)(sc + 8 * lane), sb = *(const LAS f32x4*)(sc + 8 * lane + 4);
        float e[8];
#pragma unroll
        for (int i = 0; i < 4; ++i) { e[i] = __expf(sa[i] - mx); e[4 + i] = __expf(sb[i] - mx); }
        lsum = wave_sum(((e[0] + e[1]) + (e[2] + e[3])) + ((e[4] + e[5]) + (e[6] + e[7])));
        unsigned pk[4];
#pragma unroll
        for (int i = 0; i < 4; ++i) pk[i] = pack2(e[i], e[4 + i]);
        LGKM0();
#pragma unroll
        for (int i = 0; i < 4; ++i) *(LAS unsigned*)((LAS unsigned char*)sc + (lane >> 3) * 128 + i * 32 + (lane & 7) * 4) = pk[i];
    }
    if (lane == 0) { ((LAS float*)ctl)[C_WMAX + w] = mx; ((LAS float*)ctl)[C_WSUM + w] = lsum; }
    arrive(ctl + C_SM, lane);
    STAMP(5);

    f32x4 acco = {0.f, 0.f, 0.f, 0.f}, acco1 = {0.f, 0.f, 0.f, 0.f}, accb = {0.f, 0.f, 0.f, 0.f};
    float fw = 0.f, gate = 0.f;
    {
        const unsigned vlin = lane * 16;
        {
            take_turn(64 + u);
            fill(vb + toff(0), vlin, vlin, vlin, vlin, myslot_a);
            issued(64 + u);
            STAMP(24);
            const int voo = 32 * g * (DM * 2) + c * 16;
#pragma unroll
            for (int r = 0; r < 4; ++r) wor[28 + r] = __builtin_amdgcn_raw_buffer_load_b128(ro, voo, soo + (28 + r) * (DM * 2), WO_AUX);
            slot_rows64<4>(myslot, (const LAS unsigned char*)sc, sel, c, g, acco, acco1, []() {}, landed);
            asm volatile("s_waitcnt vmcnt(0)" : "+v"(wor[28]), "+v"(wor[29]), "+v"(wor[30]), "+v"(wor[31]) :: "memory");
            STAMP(32);
        }
        const __amdgpu_buffer_rsrc_t rd = __builtin_amdgcn_make_buffer_rsrc((void*)wd, 0, FF * DM * 2, 0x00020000);
        const int vod = 28 * g * (DM * 2) + c * 16;
#pragma unroll 1
        for (int t = 1; t < 8; ++t) {
            take_turn(64 + 8 * t + u);
            fill(vb + toff(t), vlin, vlin, vlin, vlin, myslot_a);
            issued(64 + 8 * t + u);
            STAMP(24 + t);
            const int tt = t - 1;
            const int sod = (fs * FS + 4 * tt) * (DM * 2) + w * 256;
            v4u dw[4];
#pragma unroll
            for (int r = 0; r < 4; ++r) dw[r] = __builtin_amdgcn_raw_buffer_load_b128(rd, vod, sod + r * (DM * 2), WD_AUX);
            if (t == 1) {
                wait_ge(ctl + C_FFN, NW);
                const int l2 = fresh_lane();
                if (l2 < FS / 2) {
                    float g0 = 0.f, g1 = 0.f, u0 = 0.f, u1 = 0.f;
#pragma unroll
                    for (int ww = 0; ww < NW; ++ww) {
                        const f32x2 a = *(const LAS f32x2*)(L + GU_OFF + ww * 896 + 8 * l2), bb = *(const LAS f32x2*)(L + GU_OFF + ww * 896 + 448 + 8 * l2);
                        g0 += a[0]; g1 += a[1]; u0 += bb[0]; u1 += bb[1];
                    }
                    ((LAS unsigned*)(L + HH_OFF))[l2] = pack2(gelu_tanh(rbf(g0)) * rbf(u0), gelu_tanh(rbf(g1)) * rbf(u1));
                }
                wait_ge(ctl + C_SM, NW);
                float m[NW], M = -INFINITY, Ls = 0.f, z = 0.f;
#pragma unroll
                for (int ww = 0; ww < NW; ++ww) { m[ww] = ((LAS float*)ctl)[C_WMAX + ww]; M = fmaxf(M, m[ww]); }
#pragma unroll
                for (int ww = 0; ww < NW; ++ww) { Ls += __expf(m[ww] - M) * ((LAS float*)ctl)[C_WSUM + ww]; z += ((LAS float*)ctl)[C_GP + ww]; }
                fw = __expf(mx - M) / Ls;
                gate = rbf(1.0f / (1.0f + __expf(-rbf(z))));
                LGKM0();
            }
            unsigned pf = 0u;
            if (CODE_PF2 && t == CODE_PF2 && w == 0 && lane < 16) pf = code_prefetch(lane);
            const v2u hs = *(const LAS v2u*)(L + HH_OFF + (28 * g + 4 * tt) * 2);
            slot_rows64<4>(myslot, (const LAS unsigned char*)sc + t * 128, sel, c, g, acco, acco1, []() {}, landed);
            asm volatile("s_waitcnt vmcnt(0)" : "+v"(dw[0]), "+v"(dw[1]), "+v"(dw[2]), "+v"(dw[3]), "+v"(pf) :: "memory");
            STAMP(32 + t);
#pragma unroll
            for (int r = 0; r < 4; ++r) accb = mfma(diagA(hs[r >> 1], sel.s[r & 1]), dw[r], accb);
        }
    }
    STAMP(6);
    acco = (acco + acco1) * fw;
    if (g < 2) *(LAS f32x4*)((LAS unsigned char*)sc + 1024 + (8 * c + 4 * g) * 4) = acco;
    arrive(ctl + C_O, lane);
    wait_ge(ctl + C_O, NW);
    v4u of[4];
    {
        float s0 = 0.f, s1 = 0.f;
#pragma unroll
        for (int ww = 0; ww < NW; ++ww) {
            const f32x2 v = *(const LAS f32x2*)(L + SC_OFF + ww * 2048 + 1024 + 8 * lane);
            s0 += v[0]; s1 += v[1];
        }
        ((LAS unsigned*)(L + O_OFF))[lane] = pack2(s0, s1);
        LGKM0();
#pragma unroll
        for (int k = 0; k < 4; ++k) of[k] = *(const LAS v4u*)(L + O_OFF + 64 * g + 16 * k);
    }
    STAMP(7);
    f32x4 acca, ac[4];
#pragma unroll
    for (int k = 0; k < 4; ++k) ac[k] = f32x4{0.f, 0.f, 0.f, 0.f};
#pragma unroll
    for (int t = 0; t < 32; t += 4) {
#pragma unroll
        for (int k = 0; k < 4; ++k) ac[k] = mfma(diagA(of[t >> 3][((t + k) >> 1) & 3], sel.s[k & 1]), wor[t + k], ac[k]);
    }
    acca = (ac[0] + ac[1]) + (ac[2] + ac[3]);
    STAMP(40);
    {
        const float omg = rbf(1.0f - gate);
        {
            const float r0 = gate * (g < 2 ? acca[0] : acca[2]) + omg * (g < 2 ? accb[0] : accb[2]);
            const float r1 = gate * (g < 2 ? acca[1] : acca[3]) + omg * (g < 2 ? accb[1] : accb[3]);
            store_dev(v4u{gran(r0, tag_lo), tag_hi, gran(r1, tag_lo), tag_hi}, rgran, gvo, (int)rfl((unsigned)gso));
        }
        const int l2 = fresh_lane();
        const int vo = ((l2 >> 4) * DM + 2 * (l2 & 15)) * 8, so = ((b * NH + 4 * w) * DM + 32 * h) * 8;
        v4u y, ya, yb;
        auto poll = [&](v4u& d) { asm volatile("buffer_load_dwordx4 %0, %1, %2, %3 offen sc1" : "=v"(d) : "v"(vo), "s"(rgran), "s"(so) : "memory"); };
        auto good = [&](const v4u& d) { return __all(((d[1] ^ tag_hi) | (d[3] ^ tag_hi) | (((d[0] ^ tag_lo) | (d[2] ^ tag_lo)) & 0xffu)) == 0u); };
        poll(ya);
        __builtin_amdgcn_s_sleep(POLL_GAP);
        poll(yb);
        for (unsigned s = 0;; ++s) {
            asm volatile("s_waitcnt vmcnt(1)" : "+v"(ya)::"memory");
            if (good(ya) || s > SPIN_CAP) { y = ya; break; }
            poll(ya);
            asm volatile("s_waitcnt vmcnt(1)" : "+v"(yb)::"memory");
            if (good(yb)) { y = yb; break; }
            poll(yb);
        }
        STAMP(41);
        float v0 = __builtin_bit_cast(float, y[0] & 0xffffff00u), v1 = __builtin_bit_cast(float, y[2] & 0xffffff00u);
        v0 += __shfl_xor(v0, 16); v1 += __shfl_xor(v1, 16);
        v0 += __shfl_xor(v0, 32); v1 += __shfl_xor(v1, 32);
        if (l2 < 16) *(LAS f32x2*)(L + GU_OFF + w * 128 + l2 * 8) = f32x2{v0, v1};
        LGKM0();
        unsigned old = 0u;
        if (l2 == 0) old = __hip_atomic_fetch_add(ctl + C_OUT, 1u, RLX_WG);
        if (rfl(old) == NW - 1) {
            CBAR();
            if (l2 < 16) {
                float s0 = 0.f, s1 = 0.f;
#pragma unroll
                for (int ww = 0; ww < NW; ++ww) { const f32x2 v = *(const LAS f32x2*)(L + GU_OFF + ww * 128 + l2 * 8); s0 += v[0]; s1 += v[1]; }
                __hip_atomic_store((gu32*)(out + ((size_t)b * DM + 32 * h + 2 * l2) * 2), pack2(s0, s1), __ATOMIC_RELAXED, __HIP_MEMORY_SCOPE_SYSTEM);
            }
            if (bh == 0 && l2 == 0) __hip_atomic_store((gu64*)(ws + WS_EPOCH), epoch, RLX_AGENT);
        }
        STAMP(57);
        asm volatile("" ::"v"(ya), "v"(yb));
    }
    SDUMP();
    asm volatile(".Lfdk_end:");
}

}

extern "C" void kernel_launch(void* const* d_in, const int* in_sizes, int n_in, void* d_out, int out_size, void* d_ws, size_t ws_size,
                              hipStream_t stream) {
    (void)in_sizes; (void)n_in; (void)out_size; (void)ws_size;
    (void)hipFuncSetAttribute((const void*)fused_decode_kernel, hipFuncAttributeMaxDynamicSharedMemorySize, LDS_BYTES);
    int pace = PACE, tstart = TSTART, depth = DEPTH;
    fused_decode_kernel<<<dim3(NB * NH), dim3(NTHR), LDS_BYTES, stream>>>(
        (const unsigned char*)d_in[0], (const unsigned char*)d_in[1], (const unsigned char*)d_in[2], (const unsigned char*)d_in[3],
        (const unsigned char*)d_in[4], (const unsigned char*)d_in[6], (const unsigned char*)d_in[7],
        (const unsigned char*)d_in[8], (const unsigned char*)d_in[9], (unsigned char*)d_out, (unsigned char*)d_ws, pace, tstart, depth);
}
```

```cpp
#define PACE 936
#define DEPTH 3
#include <hip/hip_runtime.h>
#include <hip/hip_bf16.h>

#define GAS __attribute__((address_space(1)))
#define LAS __attribute__((address_space(3)))
typedef unsigned v4u __attribute__((ext_vector_type(4)));
typedef unsigned v2u __attribute__((ext_vector_type(2)));
typedef float f32x4 __attribute__((ext_vector_type(4)));
typedef float f32x2 __attribute__((ext_vector_type(2)));
typedef short bf16x8 __attribute__((ext_vector_type(8)));
typedef GAS unsigned gu32;
typedef GAS unsigned long long gu64;
#define RLX_AGENT __ATOMIC_RELAXED, __HIP_MEMORY_SCOPE_AGENT
#define RLX_WG __ATOMIC_RELAXED, __HIP_MEMORY_SCOPE_WORKGROUP

namespace {

constexpr int NB = 8, DM = 1024, NH = 32, SK = 4096, DH = 128, FF = 3584;
constexpr int FS = FF / NH;
constexpr int NW = 8, NTHR = 64 * NW;
constexpr int SLOT = 16384;
#define GUNT " nt"
#define ROTK 0
#define PRE 1
#define GU_T 2
#define WO_AUX 0
#define WD_AUX 0
#define POLL_SLEEP 1
#define QNT " nt"
#define CODE_PF 1
#define CODE_PF2 5
#define POLL_GAP 10
#define HOLE1 65
#define HOLE2 5
#define INVAL 0
#define TSTART 600

constexpr int SC_OFF = NW * SLOT;
constexpr int GU_OFF = SC_OFF + NW * 2048;
constexpr int Q_OFF = GU_OFF + 14336;
constexpr int O_OFF = Q_OFF + 256;
constexpr int HH_OFF = O_OFF + 256;
constexpr int CTL_OFF = HH_OFF + 256;
constexpr int LDS_BYTES = CTL_OFF + 256;
constexpr int C_ISS = 0, C_LAND = 1, C_FFN = 2, C_O = 3, C_QP = 4, C_QR = 5, C_GU = 6, C_OUT = 7, C_TAG = 8, C_POLL = 10, C_SM = 11, C_WMAX = 16, C_WSUM = 24, C_GP = 32;
static_assert(LDS_BYTES <= 160 * 1024, "LDS");

constexpr size_t WS_EPOCH = 0, WS_GRAN = 4096, WS_QGRAN = WS_GRAN + (size_t)NB * NH * DM * 8, WS_GUGRAN = WS_QGRAN + (size_t)NH * NB * NB * DH * 8;

constexpr unsigned SPIN_CAP = 1u << 22;
#define STAMP(i) do {} while (0)
#define SDUMP() do {} while (0)

__device__ __forceinline__ f32x4 mfma(v4u a, v4u b, f32x4 c) {
    return __builtin_amdgcn_mfma_f32_16x16x32_bf16(__builtin_bit_cast(bf16x8, a), __builtin_bit_cast(bf16x8, b), c, 0, 0, 0);
}
__device__ __forceinline__ float bf_lo(unsigned u) { return __builtin_bit_cast(float, u << 16); }
__device__ __forceinline__ float bf_hi(unsigned u) { return __builtin_bit_cast(float, u & 0xffff0000u); }
__device__ __forceinline__ float rbf(float f) { return (float)(__bf16)f; }
__device__ __forceinline__ unsigned bf_bits(float f) { return (unsigned)__builtin_bit_cast(unsigned short, (__bf16)f); }
__device__ __forceinline__ unsigned pack2(float lo, float hi) { return bf_bits(lo) | (bf_bits(hi) << 16); }
__device__ __forceinline__ unsigned gran(float f, unsigned tag_lo) { return ((__builtin_bit_cast(unsigned, f) + 0x80u) & 0xffffff00u) | tag_lo; }
__device__ __forceinline__ void store_dev(v4u d, __amdgpu_buffer_rsrc_t r, int vo, int so) {
    __builtin_amdgcn_raw_buffer_store_b128(d, r, vo, so, 16);
    asm volatile("s_nop 1" ::"v"(d) : "memory");
}
__device__ __forceinline__ unsigned long long rt() { return __builtin_amdgcn_s_memrealtime(); }
__device__ __forceinline__ unsigned rfl(unsigned v) { return (unsigned)__builtin_amdgcn_readfirstlane((int)v); }
#define LGKM0() asm volatile("s_waitcnt lgkmcnt(0)" ::: "memory")
#define VMCNT(n) asm volatile("s_waitcnt vmcnt(%0)" ::"i"(n) : "memory")
#define CBAR() asm volatile("" ::: "memory")
#define PIN2(a, b) do { asm volatile("" : "+v"(a), "+v"(b)); __builtin_amdgcn_sched_barrier(0); } while (0)

__device__ __forceinline__ void glds4(const void* sbase, unsigned v0, unsigned v1, unsigned v2, unsigned v3, unsigned lds) {
    unsigned keep;
    asm volatile("s_mov_b32 %0, m0\n\ts_mov_b32 m0, %6\n\ts_nop 0\n\t"
                 "global_load_lds_dwordx4 %1, %5 nt\n\t"
                 "global_load_lds_dwordx4 %2, %5 offset:1024 nt\n\t"
                 "global_load_lds_dwordx4 %3, %5 offset:2048 nt\n\t"
                 "global_load_lds_dwordx4 %4, %5 offset:3072 nt\n\t"
                 "s_mov_b32 m0, %0"
                 : "=&s"(keep) : "v"(v0), "v"(v1), "v"(v2), "v"(v3), "s"(sbase), "s"(lds) : "memory");
}
__device__ __forceinline__ void fill(const unsigned char* src, unsigned v0, unsigned v1, unsigned v2, unsigned v3, unsigned lds) {
    LGKM0();
#pragma unroll
    for (int q = 0; q < 4; ++q) glds4(src + q * 4096, v0, v1, v2, v3, lds + q * 4096);
}

__device__ __forceinline__ void wait_ge(LAS unsigned* w, unsigned need) {
    for (unsigned s = 0; rfl(*(volatile LAS unsigned*)w) < need && s < SPIN_CAP; ++s) __builtin_amdgcn_s_sleep(0);
    CBAR();
}
__device__ __forceinline__ void arrive(LAS unsigned* w, int lane) {
    LGKM0();
    if (lane == 0) __hip_atomic_fetch_add(w, 1u, RLX_WG);
    CBAR();
}

struct Sel { unsigned s[2][4]; };
__device__ __forceinline__ v4u diagA(unsigned S, const unsigned (&sel)[4]) {
    return v4u{__builtin_amdgcn_perm(0u, S, sel[0]), __builtin_amdgcn_perm(0u, S, sel[1]), __builtin_amdgcn_perm(0u, S, sel[2]), __builtin_amdgcn_perm(0u, S, sel[3])};
}
__device__ __forceinline__ int fresh_lane() {
    int l;
    asm volatile("v_mbcnt_lo_u32_b32 %0, -1, 0\n\tv_mbcnt_hi_u32_b32 %0, -1, %0" : "=v"(l));
    __builtin_assume((unsigned)l < 64u);
    return l;
}
__device__ __forceinline__ Sel make_sel(int c) {
    Sel sel;
    const int dm = (c & 7) >> 1, hf = c & 1;
#pragma unroll
    for (int par = 0; par < 2; ++par)
#pragma unroll
        for (int d = 0; d < 4; ++d) {
            const unsigned lohi = (unsigned)(2 * par) | ((unsigned)(2 * par + 1) << 8);
            const unsigned v = hf ? (0x0c0cu | (lohi << 16)) : (0x0c0c0000u | lohi);
            sel.s[par][d] = (c < 8 && d == dm) ? v : 0x0c0c0c0cu;
        }
    return sel;
}

template <int LATER, typename F, typename G>
__device__ __forceinline__ void slot_rows64(const LAS unsigned char* slot, const LAS unsigned char* vec, const Sel& sel, int c, int g,
                                            f32x4& acc, f32x4& a1, F&& last_wait, G&& all_landed) {
    const LAS unsigned char* base = slot + g * 256 + c * 16;
    const v4u s0 = *(const LAS v4u*)(vec + g * 32), s1 = *(const LAS v4u*)(vec + g * 32 + 16);
#pragma unroll
    for (int q = 0; q < 4; ++q) {
        VMCNT(LATER + 12 - 4 * q);
        v4u bb[4];
#pragma unroll
        for (int t = 0; t < 4; ++t) bb[t] = *(const LAS v4u*)(base + (4 * q + t) * 1024);
        if (q == 3) { last_wait(); all_landed(); }
        __builtin_amdgcn_sched_barrier(0);
#pragma unroll
        for (int u = 0; u < 2; ++u) {
            const unsigned S = (q < 2 ? s0 : s1)[2 * (q & 1) + u];
            acc = mfma(diagA(S, sel.s[0]), bb[2 * u], acc);
            a1 = mfma(diagA(S, sel.s[1]), bb[2 * u + 1], a1);
            PIN2(acc, a1);
        }
    }
}

__device__ __forceinline__ float gelu_tanh(float x) {
    const float y = 0.7978845608028654f * (x + 0.044715f * x * x * x);
    return rbf(x / (1.0f + __expf(-2.0f * y)));
}
__device__ __forceinline__ float wave_max(float v) {
#pragma unroll
    for (int o = 32; o >= 1; o >>= 1) v = fmaxf(v, __shfl_xor(v, o));
    return v;
}
__device__ __forceinline__ float wave_sum(float v) {
#pragma unroll
    for (int o = 32; o >= 1; o >>= 1) v += __shfl_xor(v, o);
    return v;
}

__global__ void __launch_bounds__(NTHR)
fused_decode_kernel(const unsigned char* __restrict__ xin, const unsigned char* __restrict__ kc, const unsigned char* __restrict__ vc,
                    const unsigned char* __restrict__ wq, const unsigned char* __restrict__ wo,
                    const unsigned char* __restrict__ wg, const unsigned char* __restrict__ wu, const unsigned char* __restrict__ wd,
                    const unsigned char* __restrict__ gatew, unsigned char* __restrict__ out, unsigned char* __restrict__ ws,
                    int pace, int tstart, int depth) {
    extern __shared__ __attribute__((aligned(16))) unsigned char smem[];
    LAS unsigned char* L = (LAS unsigned char*)smem;
    LAS unsigned* ctl = (LAS unsigned*)(L + CTL_OFF);
    asm volatile(".Lfdk_begin:");
    const unsigned long long t0 = rt();
    asm volatile("" ::"s"(xin), "s"(kc), "s"(vc), "s"(wq), "s"(wo), "s"(wg), "s"(wu), "s"(wd), "s"(gatew), "s"(out), "s"(ws), "s"(pace), "s"(tstart), "s"(depth));
    const int w = __builtin_amdgcn_readfirstlane((int)threadIdx.x >> 6);
    const int h = (blockIdx.x & 7) * 4 + ((blockIdx.x >> 3) & 3), b = blockIdx.x >> 5;
    const int fs = h, bh = b * NH + h;
    STAMP(0);
    const unsigned myslot_a = (unsigned)(size_t)L + w * SLOT;
    const LAS unsigned char* myslot = L + w * SLOT;
    const unsigned rot = (unsigned)(ROTK) + (w ^ 4);
    auto toff = [&](int t) { return (size_t)(((8u * t + rot) & 63u) * SLOT); };
    int lane, c, g;
    unsigned vk0, vk1, vk2, vk3;
    Sel sel;
    const unsigned char* kb;
    auto setup = [&]() {
        lane = fresh_lane(); c = lane & 15; g = lane >> 4;
        vk0 = g * 256 + ((c ^ g) << 4); vk1 = g * 256 + ((c ^ (4 + g)) << 4); vk2 = g * 256 + ((c ^ (8 + g)) << 4); vk3 = g * 256 + ((c ^ (12 + g)) << 4);
        sel = make_sel(c & 7);
        int bh2 = bh;
        asm volatile("" : "+s"(bh2));
        kb = kc + (size_t)bh2 * (SK * DH * 2);
    };
    auto code_prefetch = [&](int l) {
        unsigned long long pc;
        unsigned off, len, v = 0u;
        asm volatile("s_getpc_b64 %0\n.Lfdk_pc%=:\n\ts_mov_b32 %1, .Lfdk_pc%=-.Lfdk_begin\n\ts_mov_b32 %2, .Lfdk_end-.Lfdk_begin" : "=s"(pc), "=s"(off), "=s"(len));
        const unsigned o = (((blockIdx.x >> 3) & 31u) + 32u * (unsigned)l) * 128u;
        if (o < len) asm volatile("global_load_dword %0, %1, %2" : "+v"(v) : "v"(o), "s"(pc - off) : "memory");
        return v;
    };
    const int u = w ^ 4;
    auto take_turn = [&](int f) {
        const int hole = HOLE1 * min(max(f - 8 * GU_T, 0), 4) + HOLE2 * min(max(f - 56, 0), 8);
        const unsigned long long td = t0 + (unsigned)(tstart + hole + (((f - 8) * pace) >> 4));
        while (rt() < td) __builtin_amdgcn_s_sleep(1);
        for (unsigned s = 0; s < SPIN_CAP; ++s) {
            const v2u st = *(volatile LAS v2u*)(ctl + C_ISS);
            if (rfl(st[0]) >= (unsigned)f && rfl(st[1]) + (unsigned)depth > (unsigned)f) break;
            __builtin_amdgcn_s_sleep(0);
        }
        CBAR();
    };
    auto issued = [&](int f) { if (lane == 0) *(volatile LAS unsigned*)(ctl + C_ISS) = (unsigned)f + 1u; CBAR(); };
    auto landed = [&]() { if (lane == 0) __hip_atomic_fetch_add(ctl + C_LAND, 1u, RLX_WG); CBAR(); };
    LAS float* sc = (LAS float*)(L + SC_OFF + w * 2048);
    unsigned long long epoch;

    if (__builtin_expect(w < 4, 1)) {
        v4u x8[4], wr[8];
        v2u ep;
        const unsigned l0 = threadIdx.x & 63;
        const unsigned char* qs = wq + (size_t)(128 * b + 32 * w) * (NH * DH * 2) + h * 256;
        const unsigned vq = (l0 >> 4) * 8 * (NH * DH * 2) + (l0 & 15) * 16;
#pragma unroll
        for (int t = 0; t < 8; ++t)
            asm volatile("global_load_dwordx4 %0, %1, %2" QNT : "=v"(wr[t]) : "v"(vq), "s"(qs + (size_t)t * (NH * DH * 2)) : "memory");
        __builtin_amdgcn_sched_barrier(0);
        const unsigned char* xs = xin + (128 * b + 32 * w) * 2;
        const unsigned vx = ((l0 >> 3) & 1) * (DM * 2) + (l0 >> 4) * 16;
#pragma unroll
        for (int j = 0; j < 4; ++j)
            asm volatile("global_load_dwordx4 %0, %1, %2" : "=v"(x8[j]) : "v"(vx), "s"(xs + j * (2 * DM * 2)) : "memory");
        asm volatile("global_load_dwordx2 %0, %1, %2 sc1" : "=v"(ep) : "v"(0u), "s"(ws + WS_EPOCH) : "memory");
        const unsigned char* inbox = ws + WS_QGRAN + (size_t)(h * NB + b) * NB * (DH * 8);
        unsigned warm;
        asm volatile("global_load_dword %0, %1, %2 sc1" : "=v"(warm) : "v"(l0 * 16), "s"(inbox) : "memory");
        STAMP(42);
        __builtin_amdgcn_s_barrier();
        setup();
        VMCNT(0);
        asm volatile("" : "+v"(x8[0]), "+v"(x8[1]), "+v"(x8[2]), "+v"(x8[3]), "+v"(wr[0]), "+v"(wr[1]), "+v"(wr[2]), "+v"(wr[3]),
                     "+v"(wr[4]), "+v"(wr[5]), "+v"(wr[6]), "+v"(wr[7]), "+v"(warm), "+v"(ep) :: "memory");
        STAMP(45);
        {
            const unsigned e0 = ep[0], e1 = ep[1];
            epoch = (((unsigned long long)rfl(e1) << 32) | rfl(e0)) + 1ull;
        }
        const unsigned qtag_hi = (unsigned)(epoch >> 8), qtag_lo = (unsigned)epoch & 0xffu;
#pragma unroll
        for (int j = 0; j < 4; ++j) {
            f32x4 a0 = {0.f, 0.f, 0.f, 0.f}, a1 = {0.f, 0.f, 0.f, 0.f};
#pragma unroll
            for (int k = 0; k < 4; ++k) {
                a0 = mfma(diagA(x8[j][k], sel.s[0]), wr[2 * k], a0);
                a1 = mfma(diagA(x8[j][k], sel.s[1]), wr[2 * k + 1], a1);
            }
            a0 += a1;
            *(LAS f32x4*)(L + SC_OFF + ((w * 8 + 2 * j + (g >> 1)) * 128 + 8 * c + 4 * (g & 1)) * 4) = a0;
        }
        if (w == 0 && lane == 0) *(LAS v2u*)(ctl + C_TAG) = v2u{(unsigned)epoch, (unsigned)(epoch >> 32)};
        arrive(ctl + C_QP, lane);
        wait_ge(ctl + C_QP, 4);
        STAMP(1);
        {
            const __amdgpu_buffer_rsrc_t rqg = __builtin_amdgcn_make_buffer_rsrc((void*)(ws + WS_QGRAN), 0, NH * NB * NB * DH * 8, 0x00020000);
#pragma unroll
            for (int r = 0; r < 2; ++r) {
                float s0 = 0.f, s1 = 0.f;
#pragma unroll
                for (int k = 0; k < 4; ++k) { const f32x2 v = *(const LAS f32x2*)(L + SC_OFF + ((k * 8 + 2 * w + r) * 128 + 2 * lane) * 4); s0 += v[0]; s1 += v[1]; }
                store_dev(v4u{gran(s0, qtag_lo), qtag_hi, gran(s1, qtag_lo), qtag_hi}, rqg, lane * 16, ((h * NB + 2 * w + r) * NB + b) * (DH * 8));
            }
        }
        arrive(ctl + C_QP, lane);
        STAMP(46);
        if (w == 3) {
            VMCNT(0);
            STAMP(55);
            const unsigned vo = lane * 16;
            v4u y[NB];
            for (unsigned s = 0;; ++s) {
#pragma unroll
                for (int p = 0; p < NB; ++p)
                    asm volatile("global_load_dwordx4 %0, %1, %2 offset:%3 sc1" : "=v"(y[p]) : "v"(vo), "s"(inbox + (p >> 2) * 4096), "i"((p & 3) * 1024) : "memory");
                if (s == 0) {
                    if (lane == 0) *(volatile LAS unsigned*)(ctl + C_POLL) = 1u;
                    fill(kb + toff(0), vk0, vk1, vk2, vk3, myslot_a);
                    VMCNT(16);
                } else {
                    VMCNT(0);
                }
                asm volatile("" : "+v"(y[0]), "+v"(y[1]), "+v"(y[2]), "+v"(y[3]), "+v"(y[4]), "+v"(y[5]), "+v"(y[6]), "+v"(y[7]) :: "memory");
                unsigned bad = 0u;
#pragma unroll
                for (int p = 0; p < NB; ++p) bad |= (y[p][1] ^ qtag_hi) | (y[p][3] ^ qtag_hi) | (((y[p][0] ^ qtag_lo) | (y[p][2] ^ qtag_lo)) & 0xffu);
                if (__all(bad == 0u) || s > SPIN_CAP) break;
                __builtin_amdgcn_s_sleep(POLL_SLEEP);
            }
            float s0 = 0.f, s1 = 0.f;
#pragma unroll
            for (int p = 0; p < NB; ++p) { s0 += __builtin_bit_cast(float, y[p][0] & 0xffffff00u); s1 += __builtin_bit_cast(float, y[p][2] & 0xffffff00u); }
            ((LAS unsigned*)(L + Q_OFF))[lane] = pack2(s0, s1);
            wait_ge(ctl + C_QP, 8);
            arrive(ctl + C_QR, lane);
        } else {
            if (w >= PRE) wait_ge(ctl + C_POLL, 1);
            fill(kb + toff(0), vk0, vk1, vk2, vk3, myslot_a);
        }
        STAMP(47);
        VMCNT(0);
        landed();
    } else {
        setup();
        if (w == 4 && lane < 16) ctl[lane] = lane == C_ISS ? 8u : 0u;
        unsigned pf = 0u;
        if (CODE_PF && w == 5 && lane < 16) pf = code_prefetch(lane);
        LGKM0();
        __builtin_amdgcn_s_barrier();
        fill(kb + toff(0), vk0, vk1, vk2, vk3, myslot_a);
        STAMP(43);
        asm volatile("s_waitcnt vmcnt(0)" : "+v"(pf)::"memory");
        landed();
        STAMP(2);
        wait_ge(ctl + C_QP, 4);
        const v2u e = *(volatile LAS v2u*)(ctl + C_TAG);
        const unsigned e0 = e[0], e1 = e[1];
        epoch = ((unsigned long long)rfl(e1) << 32) | rfl(e0);
    }
    const unsigned tag_hi = (unsigned)(epoch >> 8), tag_lo = (unsigned)epoch & 0xffu;
    const unsigned char* vb = vc + (size_t)bh * (SK * DH * 2);
    const __amdgpu_buffer_rsrc_t rgran = __builtin_amdgcn_make_buffer_rsrc((void*)(ws + WS_GRAN), 0, NB * NH * DM * 8, 0x00020000);
    const int gvo = (8 * c + 4 * (g & 1) + 2 * (g >> 1)) * 8, gso = bh * (DM * 8) + w * 1024;
    wait_ge(ctl + C_QR, 1);
    v4u qf[4];
#pragma unroll
    for (int s = 0; s < 4; ++s) qf[s] = *(const LAS v4u*)(L + Q_OFF + (32 * s + 8 * g) * 2);
    LGKM0();
    STAMP(3);

    float mx = -INFINITY;
    v4u wor[32];
    const __amdgpu_buffer_rsrc_t ro = __builtin_amdgcn_make_buffer_rsrc((void*)wo, 0, DM * NH * DH * 2, 0x00020000);
    const int soo = h * DH * (DM * 2) + w * 256;
#pragma unroll
    for (int t = 0; t < 8; ++t) {
        if (t > 0) { take_turn(8 * t + u); fill(kb + toff(t), vk0, vk1, vk2, vk3, myslot_a); issued(8 * t + u); }
        STAMP(8 + t);
        v4u xg[4][2], gw[16];
        if (t > 0) {
            const int l2 = fresh_lane();
            const int voo = 32 * (l2 >> 4) * (DM * 2) + (l2 & 15) * 16;
            v4u* wp = wor + 4 * (t - 1);
#pragma unroll
            for (int r = 0; r < 4; ++r) wp[r] = __builtin_amdgcn_raw_buffer_load_b128(ro, voo, soo + (4 * (t - 1) + r) * (DM * 2), WO_AUX);
            if (t == 1) {
                const __amdgpu_buffer_rsrc_t rx = __builtin_amdgcn_make_buffer_rsrc((void*)xin, 0, NB * DM * 2, 0x00020000);
                const __amdgpu_buffer_rsrc_t rw = __builtin_amdgcn_make_buffer_rsrc((void*)gatew, 0, DM * 2, 0x00020000);
                v4u xv = __builtin_amdgcn_raw_buffer_load_b128(rx, (l2 & 15) * 16, b * (DM * 2) + w * 256, 0);
                v4u gv = __builtin_amdgcn_raw_buffer_load_b128(rw, (l2 & 15) * 16, w * 256, 0);
                asm volatile("s_waitcnt vmcnt(0)" : "+v"(wp[0]), "+v"(wp[1]), "+v"(wp[2]), "+v"(wp[3]), "+v"(xv), "+v"(gv) :: "memory");
                landed();
                if (INVAL) store_dev(v4u{tag_lo ^ 1u, ~tag_hi, tag_lo ^ 1u, ~tag_hi}, rgran, gvo, gso);
                float s = 0.f;
#pragma unroll
                for (int k = 0; k < 4; ++k) s += rbf(bf_lo(xv[k]) * bf_lo(gv[k])) + rbf(bf_hi(xv[k]) * bf_hi(gv[k]));
                s = wave_sum(l2 < 16 ? s : 0.f);
                if (l2 == 0) ((LAS float*)ctl)[C_GP + w] = s;
            } else if (t == GU_T && w >= 4) {
                const int m = (w >> 1) & 1, hf = w & 1;
                const unsigned char* gs = (m ? wu : wg) + (size_t)(128 * b + 64 * hf) * (FF * 2) + fs * (FS * 2);
                const unsigned vog = 16 * (l2 >> 4) * (FF * 2) + ((l2 & 15) < 14 ? (l2 & 15) : 13) * 16;
#pragma unroll
                for (int k = 0; k < 16; ++k)
                    asm volatile("global_load_dwordx4 %0, %1, %2" GUNT : "=v"(gw[k]) : "v"(vog), "s"(gs + (size_t)k * (FF * 2)) : "memory");
                const unsigned char* xs = xin + (128 * b + 64 * hf) * 2;
                const unsigned vox = ((l2 & 15) >> 3) * (DM * 2) + (l2 >> 4) * 32;
#pragma unroll
                for (int j = 0; j < 4; ++j) {
                    asm volatile("global_load_dwordx4 %0, %1, %2" : "=v"(xg[j][0]) : "v"(vox), "s"(xs + j * (2 * DM * 2)) : "memory");
                    asm volatile("global_load_dwordx4 %0, %1, %2 offset:16" : "=v"(xg[j][1]) : "v"(vox), "s"(xs + j * (2 * DM * 2)) : "memory");
                }
                VMCNT(24);
            } else if (t == GU_T + 1 && w >= 4) {
                asm volatile("s_waitcnt vmcnt(0)" : "+v"(wp[0]), "+v"(wp[1]), "+v"(wp[2]), "+v"(wp[3]) :: "memory");
                landed();
                wait_ge(ctl + C_GU, 4);
                const __amdgpu_buffer_rsrc_t rgg = __builtin_amdgcn_make_buffer_rsrc((void*)(ws + WS_GUGRAN), 0, NH * NB * NB * 2048, 0x00020000);
#pragma unroll
                for (int r = 0; r < 2; ++r)
#pragma unroll
                    for (int q = 0; q < 2; ++q) {
                        const int p = q == 0 ? l2 : (l2 < 48 ? 64 + l2 : 111);
                        const int mm = p >= 56 ? 1 : 0, col = 2 * (p - 56 * mm), cons = 2 * (w - 4) + r;
                        const f32x2 v0 = *(const LAS f32x2*)(L + GU_OFF + (((2 * mm) * 8 + cons) * FS + col) * 4);
                        const f32x2 v1 = *(const LAS f32x2*)(L + GU_OFF + (((2 * mm + 1) * 8 + cons) * FS + col) * 4);
                        const float s0 = v0[0] + v1[0], s1 = v0[1] + v1[1];
                        if (q == 0 || l2 < 48)
                            store_dev(v4u{gran(s0, tag_lo), tag_hi, gran(s1, tag_lo), tag_hi}, rgg, p * 16, ((h * NB + cons) * NB + b) * 2048);
                    }
                arrive(ctl + C_GU, l2);
            } else if (t == 7) {
                const __amdgpu_buffer_rsrc_t rgg = __builtin_amdgcn_make_buffer_rsrc((void*)(ws + WS_GUGRAN), 0, NH * NB * NB * 2048, 0x00020000);
                const int sgg = ((h * NB + b) * NB + w) * 2048, v1o = 1024 + (l2 < 48 ? l2 : 47) * 16;
                v4u y0, y1;
                for (unsigned s = 0;; ++s) {
                    y0 = __builtin_amdgcn_raw_buffer_load_b128(rgg, l2 * 16, sgg, 16);
                    y1 = __builtin_amdgcn_raw_buffer_load_b128(rgg, v1o, sgg, 16);
                    asm volatile("s_waitcnt vmcnt(0)" : "+v"(wp[0]), "+v"(wp[1]), "+v"(wp[2]), "+v"(wp[3]), "+v"(y0), "+v"(y1) :: "memory");
                    const unsigned bad = (y0[1] ^ tag_hi) | (y0[3] ^ tag_hi) | (y1[1] ^ tag_hi) | (y1[3] ^ tag_hi) | (((y0[0] ^ tag_lo) | (y0[2] ^ tag_lo) | (y1[0] ^ tag_lo) | (y1[2] ^ tag_lo)) & 0xffu);
                    if (s == 0) landed();
                    if (__all(bad == 0u) || s > SPIN_CAP) break;
                    __builtin_amdgcn_s_sleep(1);
                }
                wait_ge(ctl + C_GU, 8);
                *(LAS v2u*)(L + GU_OFF + w * 896 + l2 * 8) = v2u{y0[0] & 0xffffff00u, y0[2] & 0xffffff00u};
                if (l2 < 48) *(LAS v2u*)(L + GU_OFF + w * 896 + 512 + l2 * 8) = v2u{y1[0] & 0xffffff00u, y1[2] & 0xffffff00u};
                arrive(ctl + C_FFN, l2);
            } else {
                asm volatile("s_waitcnt vmcnt(0)" : "+v"(wp[0]), "+v"(wp[1]), "+v"(wp[2]), "+v"(wp[3]) :: "memory");
            }
            if (t != 7 && t != 1 && !(t == GU_T + 1 && w >= 4)) landed();
        }
        STAMP(16 + t);
        f32x4 s4[4];
#pragma unroll
        for (int kg = 0; kg < 4; ++kg) {
            f32x4 a = {0.f, 0.f, 0.f, 0.f};
#pragma unroll
            for (int s = 0; s < 4; ++s) {
                const v4u kf = *(const LAS v4u*)(myslot + (16 * kg + c) * 256 + (((4 * s + g) ^ c) << 4));
                a = mfma(kf, qf[s], a);
            }
            s4[kg] = a;
        }
        f32x4 v;
#pragma unroll
        for (int i = 0; i < 4; ++i) {
            const float lo = (c & 1) ? s4[1][i] : s4[0][i], hi = (c & 1) ? s4[3][i] : s4[2][i];
            v[i] = rbf(rbf((c & 2) ? hi : lo) * 0.08838834764831845f);
            mx = fmaxf(mx, v[i]);
        }
        if (c < 4) *(LAS f32x4*)(sc + t * 64 + 16 * c + 4 * g) = v;
        __builtin_amdgcn_sched_barrier(0);
        if (t == GU_T && w >= 4) {
            v4u* wp = wor + 4 * (t - 1);
            asm volatile("s_waitcnt vmcnt(0)" : "+v"(gw[0]), "+v"(gw[1]), "+v"(gw[2]), "+v"(gw[3]), "+v"(gw[4]), "+v"(gw[5]), "+v"(gw[6]), "+v"(gw[7]),
                         "+v"(gw[8]), "+v"(gw[9]), "+v"(gw[10]), "+v"(gw[11]), "+v"(gw[12]), "+v"(gw[13]), "+v"(gw[14]), "+v"(gw[15]),
                         "+v"(xg[0][0]), "+v"(xg[0][1]), "+v"(xg[1][0]), "+v"(xg[1][1]), "+v"(xg[2][0]), "+v"(xg[2][1]), "+v"(xg[3][0]), "+v"(xg[3][1]) :: "memory");
            asm volatile("" : "+v"(wp[0]), "+v"(wp[1]), "+v"(wp[2]), "+v"(wp[3]));
            STAMP(44);
            f32x4 a[4];
#pragma unroll
            for (int j = 0; j < 4; ++j) a[j] = f32x4{0.f, 0.f, 0.f, 0.f};
#pragma unroll
            for (int k = 0; k < 16; ++k) {
#pragma unroll
                for (int j = 0; j < 4; ++j) a[j] = mfma(diagA(xg[j][k >> 3][(k >> 1) & 3], sel.s[k & 1]), gw[k], a[j]);
                PIN2(a[0], a[1]); PIN2(a[2], a[3]);
            }
            if (c < 14) {
#pragma unroll
                for (int j = 0; j < 4; ++j)
                    *(LAS f32x4*)(L + GU_OFF + ((((w - 4) * 8 + 2 * j + (g >> 1)) * FS) + 8 * c + 4 * (g & 1)) * 4) = a[j];
            }
            arrive(ctl + C_GU, lane);
            STAMP(56);
        }
    }
    STAMP(4);
    float lsum;
    mx = wave_max(mx);
    {
        const f32x4 sa = *(const LAS f32x4*)(sc + 8 * lane), sb = *(const LAS f32x4*)(sc + 8 * lane + 4);
        float e[8];
#pragma unroll
        for (int i = 0; i < 4; ++i) { e[i] = __expf(sa[i] - mx); e[4 + i] = __expf(sb[i] - mx); }
        lsum = wave_sum(((e[0] + e[1]) + (e[2] + e[3])) + ((e[4] + e[5]) + (e[6] + e[7])));
        unsigned pk[4];
#pragma unroll
        for (int i = 0; i < 4; ++i) pk[i] = pack2(e[i], e[4 + i]);
        LGKM0();
#pragma unroll
        for (int i = 0; i < 4; ++i) *(LAS unsigned*)((LAS unsigned char*)sc + (lane >> 3) * 128 + i * 32 + (lane & 7) * 4) = pk[i];
    }
    if (lane == 0) { ((LAS float*)ctl)[C_WMAX + w] = mx; ((LAS float*)ctl)[C_WSUM + w] = lsum; }
    arrive(ctl + C_SM, lane);
    STAMP(5);

    f32x4 acco = {0.f, 0.f, 0.f, 0.f}, acco1 = {0.f, 0.f, 0.f, 0.f}, accb = {0.f, 0.f, 0.f, 0.f};
    float fw = 0.f, gate = 0.f;
    {
        const unsigned vlin = lane * 16;
        {
            take_turn(64 + u);
            fill(vb + toff(0), vlin, vlin, vlin, vlin, myslot_a);
            issued(64 + u);
            STAMP(24);
            const int voo = 32 * g * (DM * 2) + c * 16;
#pragma unroll
            for (int r = 0; r < 4; ++r) wor[28 + r] = __builtin_amdgcn_raw_buffer_load_b128(ro, voo, soo + (28 + r) * (DM * 2), WO_AUX);
            slot_rows64<4>(myslot, (const LAS unsigned char*)sc, sel, c, g, acco, acco1, []() {}, landed);
            asm volatile("s_waitcnt vmcnt(0)" : "+v"(wor[28]), "+v"(wor[29]), "+v"(wor[30]), "+v"(wor[31]) :: "memory");
            STAMP(32);
        }
        const __amdgpu_buffer_rsrc_t rd = __builtin_amdgcn_make_buffer_rsrc((void*)wd, 0, FF * DM * 2, 0x00020000);
        const int vod = 28 * g * (DM * 2) + c * 16;
#pragma unroll 1
        for (int t = 1; t < 8; ++t) {
            take_turn(64 + 8 * t + u);
            fill(vb + toff(t), vlin, vlin, vlin, vlin, myslot_a);
            issued(64 + 8 * t + u);
            STAMP(24 + t);
            const int tt = t - 1;
            const int sod = (fs * FS + 4 * tt) * (DM * 2) + w * 256;
            v4u dw[4];
#pragma unroll
            for (int r = 0; r < 4; ++r) dw[r] = __builtin_amdgcn_raw_buffer_load_b128(rd, vod, sod + r * (DM * 2), WD_AUX);
            if (t == 1) {
                wait_ge(ctl + C_FFN, NW);
                const int l2 = fresh_lane();
                if (l2 < FS / 2) {
                    float g0 = 0.f, g1 = 0.f, u0 = 0.f, u1 = 0.f;
#pragma unroll
                    for (int ww = 0; ww < NW; ++ww) {
                        const f32x2 a = *(const LAS f32x2*)(L + GU_OFF + ww * 896 + 8 * l2), bb = *(const LAS f32x2*)(L + GU_OFF + ww * 896 + 448 + 8 * l2);
                        g0 += a[0]; g1 += a[1]; u0 += bb[0]; u1 += bb[1];
                    }
                    ((LAS unsigned*)(L + HH_OFF))[l2] = pack2(gelu_tanh(rbf(g0)) * rbf(u0), gelu_tanh(rbf(g1)) * rbf(u1));
                }
                wait_ge(ctl + C_SM, NW);
                float m[NW], M = -INFINITY, Ls = 0.f, z = 0.f;
#pragma unroll
                for (int ww = 0; ww < NW; ++ww) { m[ww] = ((LAS float*)ctl)[C_WMAX + ww]; M = fmaxf(M, m[ww]); }
#pragma unroll
                for (int ww = 0; ww < NW; ++ww) { Ls += __expf(m[ww] - M) * ((LAS float*)ctl)[C_WSUM + ww]; z += ((LAS float*)ctl)[C_GP + ww]; }
                fw = __expf(mx - M) / Ls;
                gate = rbf(1.0f / (1.0f + __expf(-rbf(z))));
                LGKM0();
            }
            unsigned pf = 0u;
            if (CODE_PF2 && t == CODE_PF2 && w == 0 && lane < 16) pf = code_prefetch(lane);
            const v2u hs = *(const LAS v2u*)(L + HH_OFF + (28 * g + 4 * tt) * 2);
            slot_rows64<4>(myslot, (const LAS unsigned char*)sc + t * 128, sel, c, g, acco, acco1, []() {}, landed);
            asm volatile("s_waitcnt vmcnt(0)" : "+v"(dw[0]), "+v"(dw[1]), "+v"(dw[2]), "+v"(dw[3]), "+v"(pf) :: "memory");
            STAMP(32 + t);
#pragma unroll
            for (int r = 0; r < 4; ++r) accb = mfma(diagA(hs[r >> 1], sel.s[r & 1]), dw[r], accb);
        }
    }
    STAMP(6);
    acco = (acco + acco1) * fw;
    if (g < 2) *(LAS f32x4*)((LAS unsigned char*)sc + 1024 + (8 * c + 4 * g) * 4) = acco;
    arrive(ctl + C_O, lane);
    wait_ge(ctl + C_O, NW);
    v4u of[4];
    {
        float s0 = 0.f, s1 = 0.f;
#pragma unroll
        for (int ww = 0; ww < NW; ++ww) {
            const f32x2 v = *(const LAS f32x2*)(L + SC_OFF + ww * 2048 + 1024 + 8 * lane);
            s0 += v[0]; s1 += v[1];
        }
        ((LAS unsigned*)(L + O_OFF))[lane] = pack2(s0, s1);
        LGKM0();
#pragma unroll
        for (int k = 0; k < 4; ++k) of[k] = *(const LAS v4u*)(L + O_OFF + 64 * g + 16 * k);
    }
    STAMP(7);
    f32x4 acca, ac[4];
#pragma unroll
    for (int k = 0; k < 4; ++k) ac[k] = f32x4{0.f, 0.f, 0.f, 0.f};
#pragma unroll
    for (int t = 0; t < 32; t += 4) {
#pragma unroll
        for (int k = 0; k < 4; ++k) ac[k] = mfma(diagA(of[t >> 3][((t + k) >> 1) & 3], sel.s[k & 1]), wor[t + k], ac[k]);
    }
    acca = (ac[0] + ac[1]) + (ac[2] + ac[3]);
    STAMP(40);
    {
        const float omg = rbf(1.0f - gate);
        {
            const float r0 = gate * (g < 2 ? acca[0] : acca[2]) + omg * (g < 2 ? accb[0] : accb[2]);
            const float r1 = gate * (g < 2 ? acca[1] : acca[3]) + omg * (g < 2 ? accb[1] : accb[3]);
            store_dev(v4u{gran(r0, tag_lo), tag_hi, gran(r1, tag_lo), tag_hi}, rgran, gvo, (int)rfl((unsigned)gso));
        }
        const int l2 = fresh_lane();
        const int vo = ((l2 >> 4) * DM + 2 * (l2 & 15)) * 8, so = ((b * NH + 4 * w) * DM + 32 * h) * 8;
        v4u y, ya, yb;
        auto poll = [&](v4u& d) { asm volatile("buffer_load_dwordx4 %0, %1, %2, %3 offen sc1" : "=v"(d) : "v"(vo), "s"(rgran), "s"(so) : "memory"); };
        auto good = [&](const v4u& d) { return __all(((d[1] ^ tag_hi) | (d[3] ^ tag_hi) | (((d[0] ^ tag_lo) | (d[2] ^ tag_lo)) & 0xffu)) == 0u); };
        poll(ya);
        __builtin_amdgcn_s_sleep(POLL_GAP);
        poll(yb);
        for (unsigned s = 0;; ++s) {
            asm volatile("s_waitcnt vmcnt(1)" : "+v"(ya)::"memory");
            if (good(ya) || s > SPIN_CAP) { y = ya; break; }
            poll(ya);
            asm volatile("s_waitcnt vmcnt(1)" : "+v"(yb)::"memory");
            if (good(yb)) { y = yb; break; }
            poll(yb);
        }
        STAMP(41);
        float v0 = __builtin_bit_cast(float, y[0] & 0xffffff00u), v1 = __builtin_bit_cast(float, y[2] & 0xffffff00u);
        v0 += __shfl_xor(v0, 16); v1 += __shfl_xor(v1, 16);
        v0 += __shfl_xor(v0, 32); v1 += __shfl_xor(v1, 32);
        if (l2 < 16) *(LAS f32x2*)(L + GU_OFF + w * 128 + l2 * 8) = f32x2{v0, v1};
        LGKM0();
        unsigned old = 0u;
        if (l2 == 0) old = __hip_atomic_fetch_add(ctl + C_OUT, 1u, RLX_WG);
        if (rfl(old) == NW - 1) {
            CBAR();
            if (l2 < 16) {
                float s0 = 0.f, s1 = 0.f;
#pragma unroll
                for (int ww = 0; ww < NW; ++ww) { const f32x2 v = *(const LAS f32x2*)(L + GU_OFF + ww * 128 + l2 * 8); s0 += v[0]; s1 += v[1]; }
                __hip_atomic_store((gu32*)(out + ((size_t)b * DM + 32 * h + 2 * l2) * 2), pack2(s0, s1), __ATOMIC_RELAXED, __HIP_MEMORY_SCOPE_SYSTEM);
            }
            if (bh == 0 && l2 == 0) __hip_atomic_store((gu64*)(ws + WS_EPOCH), epoch, RLX_AGENT);
        }
        STAMP(57);
        asm volatile("" ::"v"(ya), "v"(yb));
    }
    SDUMP();
    asm volatile(".Lfdk_end:");
}

}

extern "C" void kernel_launch(void* const* d_in, const int* in_sizes, int n_in, void* d_out, int out_size, void* d_ws, size_t ws_size,
                              hipStream_t stream) {
    (void)in_sizes; (void)n_in; (void)out_size; (void)ws_size;
    (void)hipFuncSetAttribute((const void*)fused_decode_kernel, hipFuncAttributeMaxDynamicSharedMemorySize, LDS_BYTES);
    int pace = PACE, tstart = TSTART, depth = DEPTH;
    fused_decode_kernel<<<dim3(NB * NH), dim3(NTHR), LDS_BYTES, stream>>>(
        (const unsigned char*)d_in[0], (const unsigned char*)d_in[1], (const unsigned char*)d_in[2], (const unsigned char*)d_in[3],
        (const unsigned char*)d_in[4], (const unsigned char*)d_in[6], (const unsigned char*)d_in[7],
        (const unsigned char*)d_in[8], (const unsigned char*)d_in[9], (unsigned char*)d_out, (unsigned char*)d_ws, pace, tstart, depth);
}
```

```cpp
#define F_SW 40
#define PACE 968
#include <hip/hip_runtime.h>
#include <hip/hip_bf16.h>

#define GAS __attribute__((address_space(1)))
#define LAS __attribute__((address_space(3)))
typedef unsigned v4u __attribute__((ext_vector_type(4)));
typedef unsigned v2u __attribute__((ext_vector_type(2)));
typedef float f32x4 __attribute__((ext_vector_type(4)));
typedef float f32x2 __attribute__((ext_vector_type(2)));
typedef short bf16x8 __attribute__((ext_vector_type(8)));
typedef GAS unsigned gu32;
typedef GAS unsigned long long gu64;
#define RLX_AGENT __ATOMIC_RELAXED, __HIP_MEMORY_SCOPE_AGENT
#define RLX_WG __ATOMIC_RELAXED, __HIP_MEMORY_SCOPE_WORKGROUP

namespace {

constexpr int NB = 8, DM = 1024, NH = 32, SK = 4096, DH = 128, FF = 3584;
constexpr int FS = FF / NH;
constexpr int NW = 8, NTHR = 64 * NW;
constexpr int SLOT = 16384;
#define GUNT " nt"
#define ROTK 0
#define PRE 1
#define GU_T 2
#define WO_AUX 0
#define WD_AUX 0
#define POLL_SLEEP 1
#define QNT " nt"
#define CODE_PF 1
#define CODE_PF2 5
#define POLL_GAP 10
#define HOLE1 65
#define HOLE2 5
#define PACEV 928
#define INVAL 0
#define TSTART 600
#define DEPTH 4

constexpr int SC_OFF = NW * SLOT;
constexpr int GU_OFF = SC_OFF + NW * 2048;
constexpr int Q_OFF = GU_OFF + 14336;
constexpr int O_OFF = Q_OFF + 256;
constexpr int HH_OFF = O_OFF + 256;
constexpr int CTL_OFF = HH_OFF + 256;
constexpr int LDS_BYTES = CTL_OFF + 256;
constexpr int C_ISS = 0, C_LAND = 1, C_FFN = 2, C_O = 3, C_QP = 4, C_QR = 5, C_GU = 6, C_OUT = 7, C_TAG = 8, C_POLL = 10, C_SM = 11, C_WMAX = 16, C_WSUM = 24, C_GP = 32;
static_assert(LDS_BYTES <= 160 * 1024, "LDS");

constexpr size_t WS_EPOCH = 0, WS_GRAN = 4096, WS_QGRAN = WS_GRAN + (size_t)NB * NH * DM * 8, WS_GUGRAN = WS_QGRAN + (size_t)NH * NB * NB * DH * 8;

constexpr unsigned SPIN_CAP = 1u << 22;
#define STAMP(i) do {} while (0)
#define SDUMP() do {} while (0)

__device__ __forceinline__ f32x4 mfma(v4u a, v4u b, f32x4 c) {
    return __builtin_amdgcn_mfma_f32_16x16x32_bf16(__builtin_bit_cast(bf16x8, a), __builtin_bit_cast(bf16x8, b), c, 0, 0, 0);
}
__device__ __forceinline__ float bf_lo(unsigned u) { return __builtin_bit_cast(float, u << 16); }
__device__ __forceinline__ float bf_hi(unsigned u) { return __builtin_bit_cast(float, u & 0xffff0000u); }
__device__ __forceinline__ float rbf(float f) { return (float)(__bf16)f; }
__device__ __forceinline__ unsigned bf_bits(float f) { return (unsigned)__builtin_bit_cast(unsigned short, (__bf16)f); }
__device__ __forceinline__ unsigned pack2(float lo, float hi) { return bf_bits(lo) | (bf_bits(hi) << 16); }
__device__ __forceinline__ unsigned gran(float f, unsigned tag_lo) { return ((__builtin_bit_cast(unsigned, f) + 0x80u) & 0xffffff00u) | tag_lo; }
__device__ __forceinline__ void store_dev(v4u d, __amdgpu_buffer_rsrc_t r, int vo, int so) {
    __builtin_amdgcn_raw_buffer_store_b128(d, r, vo, so, 16);
    asm volatile("s_nop 1" ::"v"(d) : "memory");
}
__device__ __forceinline__ unsigned long long rt() { return __builtin_amdgcn_s_memrealtime(); }
__device__ __forceinline__ unsigned rfl(unsigned v) { return (unsigned)__builtin_amdgcn_readfirstlane((int)v); }
#define LGKM0() asm volatile("s_waitcnt lgkmcnt(0)" ::: "memory")
#define VMCNT(n) asm volatile("s_waitcnt vmcnt(%0)" ::"i"(n) : "memory")
#define CBAR() asm volatile("" ::: "memory")
#define PIN2(a, b) do { asm volatile("" : "+v"(a), "+v"(b)); __builtin_amdgcn_sched_barrier(0); } while (0)

__device__ __forceinline__ void glds4(const void* sbase, unsigned v0, unsigned v1, unsigned v2, unsigned v3, unsigned lds) {
    unsigned keep;
    asm volatile("s_mov_b32 %0, m0\n\ts_mov_b32 m0, %6\n\ts_nop 0\n\t"
                 "global_load_lds_dwordx4 %1, %5 nt\n\t"
                 "global_load_lds_dwordx4 %2, %5 offset:1024 nt\n\t"
                 "global_load_lds_dwordx4 %3, %5 offset:2048 nt\n\t"
                 "global_load_lds_dwordx4 %4, %5 offset:3072 nt\n\t"
                 "s_mov_b32 m0, %0"
                 : "=&s"(keep) : "v"(v0), "v"(v1), "v"(v2), "v"(v3), "s"(sbase), "s"(lds) : "memory");
}
__device__ __forceinline__ void fill(const unsigned char* src, unsigned v0, unsigned v1, unsigned v2, unsigned v3, unsigned lds) {
    LGKM0();
#pragma unroll
    for (int q = 0; q < 4; ++q) glds4(src + q * 4096, v0, v1, v2, v3, lds + q * 4096);
}

__device__ __forceinline__ void wait_ge(LAS unsigned* w, unsigned need) {
    for (unsigned s = 0; rfl(*(volatile LAS unsigned*)w) < need && s < SPIN_CAP; ++s) __builtin_amdgcn_s_sleep(0);
    CBAR();
}
__device__ __forceinline__ void arrive(LAS unsigned* w, int lane) {
    LGKM0();
    if (lane == 0) __hip_atomic_fetch_add(w, 1u, RLX_WG);
    CBAR();
}

struct Sel { unsigned s[2][4]; };
__device__ __forceinline__ v4u diagA(unsigned S, const unsigned (&sel)[4]) {
    return v4u{__builtin_amdgcn_perm(0u, S, sel[0]), __builtin_amdgcn_perm(0u, S, sel[1]), __builtin_amdgcn_perm(0u, S, sel[2]), __builtin_amdgcn_perm(0u, S, sel[3])};
}
__device__ __forceinline__ int fresh_lane() {
    int l;
    asm volatile("v_mbcnt_lo_u32_b32 %0, -1, 0\n\tv_mbcnt_hi_u32_b32 %0, -1, %0" : "=v"(l));
    __builtin_assume((unsigned)l < 64u);
    return l;
}
__device__ __forceinline__ Sel make_sel(int c) {
    Sel sel;
    const int dm = (c & 7) >> 1, hf = c & 1;
#pragma unroll
    for (int par = 0; par < 2; ++par)
#pragma unroll
        for (int d = 0; d < 4; ++d) {
            const unsigned lohi = (unsigned)(2 * par) | ((unsigned)(2 * par + 1) << 8);
            const unsigned v = hf ? (0x0c0cu | (lohi << 16)) : (0x0c0c0000u | lohi);
            sel.s[par][d] = (c < 8 && d == dm) ? v : 0x0c0c0c0cu;
        }
    return sel;
}

template <int LATER, typename F, typename G>
__device__ __forceinline__ void slot_rows64(const LAS unsigned char* slot, const LAS unsigned char* vec, const Sel& sel, int c, int g,
                                            f32x4& acc, f32x4& a1, F&& last_wait, G&& all_landed) {
    const LAS unsigned char* base = slot + g * 256 + c * 16;
    const v4u s0 = *(const LAS v4u*)(vec + g * 32), s1 = *(const LAS v4u*)(vec + g * 32 + 16);
#pragma unroll
    for (int q = 0; q < 4; ++q) {
        VMCNT(LATER + 12 - 4 * q);
        v4u bb[4];
#pragma unroll
        for (int t = 0; t < 4; ++t) bb[t] = *(const LAS v4u*)(base + (4 * q + t) * 1024);
        if (q == 3) { last_wait(); all_landed(); }
        __builtin_amdgcn_sched_barrier(0);
#pragma unroll
        for (int u = 0; u < 2; ++u) {
            const unsigned S = (q < 2 ? s0 : s1)[2 * (q & 1) + u];
            acc = mfma(diagA(S, sel.s[0]), bb[2 * u], acc);
            a1 = mfma(diagA(S, sel.s[1]), bb[2 * u + 1], a1);
            PIN2(acc, a1);
        }
    }
}

__device__ __forceinline__ float gelu_tanh(float x) {
    const float y = 0.7978845608028654f * (x + 0.044715f * x * x * x);
    return rbf(x / (1.0f + __expf(-2.0f * y)));
}
__device__ __forceinline__ float wave_max(float v) {
#pragma unroll
    for (int o = 32; o >= 1; o >>= 1) v = fmaxf(v, __shfl_xor(v, o));
    return v;
}
__device__ __forceinline__ float wave_sum(float v) {
#pragma unroll
    for (int o = 32; o >= 1; o >>= 1) v += __shfl_xor(v, o);
    return v;
}

__global__ void __launch_bounds__(NTHR)
fused_decode_kernel(const unsigned char* __restrict__ xin, const unsigned char* __restrict__ kc, const unsigned char* __restrict__ vc,
                    const unsigned char* __restrict__ wq, const unsigned char* __restrict__ wo,
                    const unsigned char* __restrict__ wg, const unsigned char* __restrict__ wu, const unsigned char* __restrict__ wd,
                    const unsigned char* __restrict__ gatew, unsigned char* __restrict__ out, unsigned char* __restrict__ ws,
                    int pace, int tstart, int depth) {
    extern __shared__ __attribute__((aligned(16))) unsigned char smem[];
    LAS unsigned char* L = (LAS unsigned char*)smem;
    LAS unsigned* ctl = (LAS unsigned*)(L + CTL_OFF);
    asm volatile(".Lfdk_begin:");
    const unsigned long long t0 = rt();
    asm volatile("" ::"s"(xin), "s"(kc), "s"(vc), "s"(wq), "s"(wo), "s"(wg), "s"(wu), "s"(wd), "s"(gatew), "s"(out), "s"(ws), "s"(pace), "s"(tstart), "s"(depth));
    const int w = __builtin_amdgcn_readfirstlane((int)threadIdx.x >> 6);
    const int h = (blockIdx.x & 7) * 4 + ((blockIdx.x >> 3) & 3), b = blockIdx.x >> 5;
    const int fs = h, bh = b * NH + h;
    STAMP(0);
    const unsigned myslot_a = (unsigned)(size_t)L + w * SLOT;
    const LAS unsigned char* myslot = L + w * SLOT;
    const unsigned rot = (unsigned)(ROTK) + (w ^ 4);
    auto toff = [&](int t) { return (size_t)(((8u * t + rot) & 63u) * SLOT); };
    int lane, c, g;
    unsigned vk0, vk1, vk2, vk3;
    Sel sel;
    const unsigned char* kb;
    auto setup = [&]() {
        lane = fresh_lane(); c = lane & 15; g = lane >> 4;
        vk0 = g * 256 + ((c ^ g) << 4); vk1 = g * 256 + ((c ^ (4 + g)) << 4); vk2 = g * 256 + ((c ^ (8 + g)) << 4); vk3 = g * 256 + ((c ^ (12 + g)) << 4);
        sel = make_sel(c & 7);
        int bh2 = bh;
        asm volatile("" : "+s"(bh2));
        kb = kc + (size_t)bh2 * (SK * DH * 2);
    };
    auto code_prefetch = [&](int l) {
        unsigned long long pc;
        unsigned off, len, v = 0u;
        asm volatile("s_getpc_b64 %0\n.Lfdk_pc%=:\n\ts_mov_b32 %1, .Lfdk_pc%=-.Lfdk_begin\n\ts_mov_b32 %2, .Lfdk_end-.Lfdk_begin" : "=s"(pc), "=s"(off), "=s"(len));
        const unsigned o = (((blockIdx.x >> 3) & 31u) + 32u * (unsigned)l) * 128u;
        if (o < len) asm volatile("global_load_dword %0, %1, %2" : "+v"(v) : "v"(o), "s"(pc - off) : "memory");
        return v;
    };
    const int u = w ^ 4;
    auto take_turn = [&](int f) {
        const int hole = HOLE1 * min(max(f - 8 * GU_T, 0), 4) + HOLE2 * min(max(f - 56, 0), 8);
        const unsigned long long td = t0 + (unsigned)(tstart + hole + (((min(f, F_SW) - 8) * pace + max(f - F_SW, 0) * PACEV) >> 4));
        while (rt() < td) __builtin_amdgcn_s_sleep(1);
        for (unsigned s = 0; s < SPIN_CAP; ++s) {
            const v2u st = *(volatile LAS v2u*)(ctl + C_ISS);
            if (rfl(st[0]) >= (unsigned)f && rfl(st[1]) + (unsigned)depth > (unsigned)f) break;
            __builtin_amdgcn_s_sleep(0);
        }
        CBAR();
    };
    auto issued = [&](int f) { if (lane == 0) *(volatile LAS unsigned*)(ctl + C_ISS) = (unsigned)f + 1u; CBAR(); };
    auto landed = [&]() { if (lane == 0) __hip_atomic_fetch_add(ctl + C_LAND, 1u, RLX_WG); CBAR(); };
    LAS float* sc = (LAS float*)(L + SC_OFF + w * 2048);
    unsigned long long epoch;

    if (__builtin_expect(w < 4, 1)) {
        v4u x8[4], wr[8];
        v2u ep;
        const unsigned l0 = threadIdx.x & 63;
        const unsigned char* qs = wq + (size_t)(128 * b + 32 * w) * (NH * DH * 2) + h * 256;
        const unsigned vq = (l0 >> 4) * 8 * (NH * DH * 2) + (l0 & 15) * 16;
#pragma unroll
        for (int t = 0; t < 8; ++t)
            asm volatile("global_load_dwordx4 %0, %1, %2" QNT : "=v"(wr[t]) : "v"(vq), "s"(qs + (size_t)t * (NH * DH * 2)) : "memory");
        __builtin_amdgcn_sched_barrier(0);
        const unsigned char* xs = xin + (128 * b + 32 * w) * 2;
        const unsigned vx = ((l0 >> 3) & 1) * (DM * 2) + (l0 >> 4) * 16;
#pragma unroll
        for (int j = 0; j < 4; ++j)
            asm volatile("global_load_dwordx4 %0, %1, %2" : "=v"(x8[j]) : "v"(vx), "s"(xs + j * (2 * DM * 2)) : "memory");
        asm volatile("global_load_dwordx2 %0, %1, %2 sc1" : "=v"(ep) : "v"(0u), "s"(ws + WS_EPOCH) : "memory");
        const unsigned char* inbox = ws + WS_QGRAN + (size_t)(h * NB + b) * NB * (DH * 8);
        unsigned warm;
        asm volatile("global_load_dword %0, %1, %2 sc1" : "=v"(warm) : "v"(l0 * 16), "s"(inbox) : "memory");
        STAMP(42);
        __builtin_amdgcn_s_barrier();
        setup();
        VMCNT(0);
        asm volatile("" : "+v"(x8[0]), "+v"(x8[1]), "+v"(x8[2]), "+v"(x8[3]), "+v"(wr[0]), "+v"(wr[1]), "+v"(wr[2]), "+v"(wr[3]),
                     "+v"(wr[4]), "+v"(wr[5]), "+v"(wr[6]), "+v"(wr[7]), "+v"(warm), "+v"(ep) :: "memory");
        STAMP(45);
        {
            const unsigned e0 = ep[0], e1 = ep[1];
            epoch = (((unsigned long long)rfl(e1) << 32) | rfl(e0)) + 1ull;
        }
        const unsigned qtag_hi = (unsigned)(epoch >> 8), qtag_lo = (unsigned)epoch & 0xffu;
#pragma unroll
        for (int j = 0; j < 4; ++j) {
            f32x4 a0 = {0.f, 0.f, 0.f, 0.f}, a1 = {0.f, 0.f, 0.f, 0.f};
#pragma unroll
            for (int k = 0; k < 4; ++k) {
                a0 = mfma(diagA(x8[j][k], sel.s[0]), wr[2 * k], a0);
                a1 = mfma(diagA(x8[j][k], sel.s[1]), wr[2 * k + 1], a1);
            }
            a0 += a1;
            *(LAS f32x4*)(L + SC_OFF + ((w * 8 + 2 * j + (g >> 1)) * 128 + 8 * c + 4 * (g & 1)) * 4) = a0;
        }
        if (w == 0 && lane == 0) *(LAS v2u*)(ctl + C_TAG) = v2u{(unsigned)epoch, (unsigned)(epoch >> 32)};
        arrive(ctl + C_QP, lane);
        wait_ge(ctl + C_QP, 4);
        STAMP(1);
        {
            const __amdgpu_buffer_rsrc_t rqg = __builtin_amdgcn_make_buffer_rsrc((void*)(ws + WS_QGRAN), 0, NH * NB * NB * DH * 8, 0x00020000);
#pragma unroll
            for (int r = 0; r < 2; ++r) {
                float s0 = 0.f, s1 = 0.f;
#pragma unroll
                for (int k = 0; k < 4; ++k) { const f32x2 v = *(const LAS f32x2*)(L + SC_OFF + ((k * 8 + 2 * w + r) * 128 + 2 * lane) * 4); s0 += v[0]; s1 += v[1]; }
                store_dev(v4u{gran(s0, qtag_lo), qtag_hi, gran(s1, qtag_lo), qtag_hi}, rqg, lane * 16, ((h * NB + 2 * w + r) * NB + b) * (DH * 8));
            }
        }
        arrive(ctl + C_QP, lane);
        STAMP(46);
        if (w == 3) {
            VMCNT(0);
            STAMP(55);
            const unsigned vo = lane * 16;
            v4u y[NB];
            for (unsigned s = 0;; ++s) {
#pragma unroll
                for (int p = 0; p < NB; ++p)
                    asm volatile("global_load_dwordx4 %0, %1, %2 offset:%3 sc1" : "=v"(y[p]) : "v"(vo), "s"(inbox + (p >> 2) * 4096), "i"((p & 3) * 1024) : "memory");
                if (s == 0) {
                    if (lane == 0) *(volatile LAS unsigned*)(ctl + C_POLL) = 1u;
                    fill(kb + toff(0), vk0, vk1, vk2, vk3, myslot_a);
                    VMCNT(16);
                } else {
                    VMCNT(0);
                }
                asm volatile("" : "+v"(y[0]), "+v"(y[1]), "+v"(y[2]), "+v"(y[3]), "+v"(y[4]), "+v"(y[5]), "+v"(y[6]), "+v"(y[7]) :: "memory");
                unsigned bad = 0u;
#pragma unroll
                for (int p = 0; p < NB; ++p) bad |= (y[p][1] ^ qtag_hi) | (y[p][3] ^ qtag_hi) | (((y[p][0] ^ qtag_lo) | (y[p][2] ^ qtag_lo)) & 0xffu);
                if (__all(bad == 0u) || s > SPIN_CAP) break;
                __builtin_amdgcn_s_sleep(POLL_SLEEP);
            }
            float s0 = 0.f, s1 = 0.f;
#pragma unroll
            for (int p = 0; p < NB; ++p) { s0 += __builtin_bit_cast(float, y[p][0] & 0xffffff00u); s1 += __builtin_bit_cast(float, y[p][2] & 0xffffff00u); }
            ((LAS unsigned*)(L + Q_OFF))[lane] = pack2(s0, s1);
            wait_ge(ctl + C_QP, 8);
            arrive(ctl + C_QR, lane);
        } else {
            if (w >= PRE) wait_ge(ctl + C_POLL, 1);
            fill(kb + toff(0), vk0, vk1, vk2, vk3, myslot_a);
        }
        STAMP(47);
        VMCNT(0);
        landed();
    } else {
        setup();
        if (w == 4 && lane < 16) ctl[lane] = lane == C_ISS ? 8u : 0u;
        unsigned pf = 0u;
        if (CODE_PF && w == 5 && lane < 16) pf = code_prefetch(lane);
        LGKM0();
        __builtin_amdgcn_s_barrier();
        fill(kb + toff(0), vk0, vk1, vk2, vk3, myslot_a);
        STAMP(43);
        asm volatile("s_waitcnt vmcnt(0)" : "+v"(pf)::"memory");
        landed();
        STAMP(2);
        wait_ge(ctl + C_QP, 4);
        const v2u e = *(volatile LAS v2u*)(ctl + C_TAG);
        const unsigned e0 = e[0], e1 = e[1];
        epoch = ((unsigned long long)rfl(e1) << 32) | rfl(e0);
    }
    const unsigned tag_hi = (unsigned)(epoch >> 8), tag_lo = (unsigned)epoch & 0xffu;
    const unsigned char* vb = vc + (size_t)bh * (SK * DH * 2);
    const __amdgpu_buffer_rsrc_t rgran = __builtin_amdgcn_make_buffer_rsrc((void*)(ws + WS_GRAN), 0, NB * NH * DM * 8, 0x00020000);
    const int gvo = (8 * c + 4 * (g & 1) + 2 * (g >> 1)) * 8, gso = bh * (DM * 8) + w * 1024;
    wait_ge(ctl + C_QR, 1);
    v4u qf[4];
#pragma unroll
    for (int s = 0; s < 4; ++s) qf[s] = *(const LAS v4u*)(L + Q_OFF + (32 * s + 8 * g) * 2);
    LGKM0();
    STAMP(3);

    float mx = -INFINITY;
    v4u wor[32];
    const __amdgpu_buffer_rsrc_t ro = __builtin_amdgcn_make_buffer_rsrc((void*)wo, 0, DM * NH * DH * 2, 0x00020000);
    const int soo = h * DH * (DM * 2) + w * 256;
#pragma unroll
    for (int t = 0; t < 8; ++t) {
        if (t > 0) { take_turn(8 * t + u); fill(kb + toff(t), vk0, vk1, vk2, vk3, myslot_a); issued(8 * t + u); }
        STAMP(8 + t);
        v4u xg[4][2], gw[16];
        if (t > 0) {
            const int l2 = fresh_lane();
            const int voo = 32 * (l2 >> 4) * (DM * 2) + (l2 & 15) * 16;
            v4u* wp = wor + 4 * (t - 1);
#pragma unroll
            for (int r = 0; r < 4; ++r) wp[r] = __builtin_amdgcn_raw_buffer_load_b128(ro, voo, soo + (4 * (t - 1) + r) * (DM * 2), WO_AUX);
            if (t == 1) {
                const __amdgpu_buffer_rsrc_t rx = __builtin_amdgcn_make_buffer_rsrc((void*)xin, 0, NB * DM * 2, 0x00020000);
                const __amdgpu_buffer_rsrc_t rw = __builtin_amdgcn_make_buffer_rsrc((void*)gatew, 0, DM * 2, 0x00020000);
                v4u xv = __builtin_amdgcn_raw_buffer_load_b128(rx, (l2 & 15) * 16, b * (DM * 2) + w * 256, 0);
                v4u gv = __builtin_amdgcn_raw_buffer_load_b128(rw, (l2 & 15) * 16, w * 256, 0);
                asm volatile("s_waitcnt vmcnt(0)" : "+v"(wp[0]), "+v"(wp[1]), "+v"(wp[2]), "+v"(wp[3]), "+v"(xv), "+v"(gv) :: "memory");
                landed();
                if (INVAL) store_dev(v4u{tag_lo ^ 1u, ~tag_hi, tag_lo ^ 1u, ~tag_hi}, rgran, gvo, gso);
                float s = 0.f;
#pragma unroll
                for (int k = 0; k < 4; ++k) s += rbf(bf_lo(xv[k]) * bf_lo(gv[k])) + rbf(bf_hi(xv[k]) * bf_hi(gv[k]));
                s = wave_sum(l2 < 16 ? s : 0.f);
                if (l2 == 0) ((LAS float*)ctl)[C_GP + w] = s;
            } else if (t == GU_T && w >= 4) {
                const int m = (w >> 1) & 1, hf = w & 1;
                const unsigned char* gs = (m ? wu : wg) + (size_t)(128 * b + 64 * hf) * (FF * 2) + fs * (FS * 2);
                const unsigned vog = 16 * (l2 >> 4) * (FF * 2) + ((l2 & 15) < 14 ? (l2 & 15) : 13) * 16;
#pragma unroll
                for (int k = 0; k < 16; ++k)
                    asm volatile("global_load_dwordx4 %0, %1, %2" GUNT : "=v"(gw[k]) : "v"(vog), "s"(gs + (size_t)k * (FF * 2)) : "memory");
                const unsigned char* xs = xin + (128 * b + 64 * hf) * 2;
                const unsigned vox = ((l2 & 15) >> 3) * (DM * 2) + (l2 >> 4) * 32;
#pragma unroll
                for (int j = 0; j < 4; ++j) {
                    asm volatile("global_load_dwordx4 %0, %1, %2" : "=v"(xg[j][0]) : "v"(vox), "s"(xs + j * (2 * DM * 2)) : "memory");
                    asm volatile("global_load_dwordx4 %0, %1, %2 offset:16" : "=v"(xg[j][1]) : "v"(vox), "s"(xs + j * (2 * DM * 2)) : "memory");
                }
                VMCNT(24);
            } else if (t == GU_T + 1 && w >= 4) {
                asm volatile("s_waitcnt vmcnt(0)" : "+v"(wp[0]), "+v"(wp[1]), "+v"(wp[2]), "+v"(wp[3]) :: "memory");
                landed();
                wait_ge(ctl + C_GU, 4);
                const __amdgpu_buffer_rsrc_t rgg = __builtin_amdgcn_make_buffer_rsrc((void*)(ws + WS_GUGRAN), 0, NH * NB * NB * 2048, 0x00020000);
#pragma unroll
                for (int r = 0; r < 2; ++r)
#pragma unroll
                    for (int q = 0; q < 2; ++q) {
                        const int p = q == 0 ? l2 : (l2 < 48 ? 64 + l2 : 111);
                        const int mm = p >= 56 ? 1 : 0, col = 2 * (p - 56 * mm), cons = 2 * (w - 4) + r;
                        const f32x2 v0 = *(const LAS f32x2*)(L + GU_OFF + (((2 * mm) * 8 + cons) * FS + col) * 4);
                        const f32x2 v1 = *(const LAS f32x2*)(L + GU_OFF + (((2 * mm + 1) * 8 + cons) * FS + col) * 4);
                        const float s0 = v0[0] + v1[0], s1 = v0[1] + v1[1];
                        if (q == 0 || l2 < 48)
                            store_dev(v4u{gran(s0, tag_lo), tag_hi, gran(s1, tag_lo), tag_hi}, rgg, p * 16, ((h * NB + cons) * NB + b) * 2048);
                    }
                arrive(ctl + C_GU, l2);
            } else if (t == 7) {
                const __amdgpu_buffer_rsrc_t rgg = __builtin_amdgcn_make_buffer_rsrc((void*)(ws + WS_GUGRAN), 0, NH * NB * NB * 2048, 0x00020000);
                const int sgg = ((h * NB + b) * NB + w) * 2048, v1o = 1024 + (l2 < 48 ? l2 : 47) * 16;
                v4u y0, y1;
                for (unsigned s = 0;; ++s) {
                    y0 = __builtin_amdgcn_raw_buffer_load_b128(rgg, l2 * 16, sgg, 16);
                    y1 = __builtin_amdgcn_raw_buffer_load_b128(rgg, v1o, sgg, 16);
                    asm volatile("s_waitcnt vmcnt(0)" : "+v"(wp[0]), "+v"(wp[1]), "+v"(wp[2]), "+v"(wp[3]), "+v"(y0), "+v"(y1) :: "memory");
                    const unsigned bad = (y0[1] ^ tag_hi) | (y0[3] ^ tag_hi) | (y1[1] ^ tag_hi) | (y1[3] ^ tag_hi) | (((y0[0] ^ tag_lo) | (y0[2] ^ tag_lo) | (y1[0] ^ tag_lo) | (y1[2] ^ tag_lo)) & 0xffu);
                    if (s == 0) landed();
                    if (__all(bad == 0u) || s > SPIN_CAP) break;
                    __builtin_amdgcn_s_sleep(1);
                }
                wait_ge(ctl + C_GU, 8);
                *(LAS v2u*)(L + GU_OFF + w * 896 + l2 * 8) = v2u{y0[0] & 0xffffff00u, y0[2] & 0xffffff00u};
                if (l2 < 48) *(LAS v2u*)(L + GU_OFF + w * 896 + 512 + l2 * 8) = v2u{y1[0] & 0xffffff00u, y1[2] & 0xffffff00u};
                arrive(ctl + C_FFN, l2);
            } else {
                asm volatile("s_waitcnt vmcnt(0)" : "+v"(wp[0]), "+v"(wp[1]), "+v"(wp[2]), "+v"(wp[3]) :: "memory");
            }
            if (t != 7 && t != 1 && !(t == GU_T + 1 && w >= 4)) landed();
        }
        STAMP(16 + t);
        f32x4 s4[4];
#pragma unroll
        for (int kg = 0; kg < 4; ++kg) {
            f32x4 a = {0.f, 0.f, 0.f, 0.f};
#pragma unroll
            for (int s = 0; s < 4; ++s) {
                const v4u kf = *(const LAS v4u*)(myslot + (16 * kg + c) * 256 + (((4 * s + g) ^ c) << 4));
                a = mfma(kf, qf[s], a);
            }
            s4[kg] = a;
        }
        f32x4 v;
#pragma unroll
        for (int i = 0; i < 4; ++i) {
            const float lo = (c & 1) ? s4[1][i] : s4[0][i], hi = (c & 1) ? s4[3][i] : s4[2][i];
            v[i] = rbf(rbf((c & 2) ? hi : lo) * 0.08838834764831845f);
            mx = fmaxf(mx, v[i]);
        }
        if (c < 4) *(LAS f32x4*)(sc + t * 64 + 16 * c + 4 * g) = v;
        __builtin_amdgcn_sched_barrier(0);
        if (t == GU_T && w >= 4) {
            v4u* wp = wor + 4 * (t - 1);
            asm volatile("s_waitcnt vmcnt(0)" : "+v"(gw[0]), "+v"(gw[1]), "+v"(gw[2]), "+v"(gw[3]), "+v"(gw[4]), "+v"(gw[5]), "+v"(gw[6]), "+v"(gw[7]),
                         "+v"(gw[8]), "+v"(gw[9]), "+v"(gw[10]), "+v"(gw[11]), "+v"(gw[12]), "+v"(gw[13]), "+v"(gw[14]), "+v"(gw[15]),
                         "+v"(xg[0][0]), "+v"(xg[0][1]), "+v"(xg[1][0]), "+v"(xg[1][1]), "+v"(xg[2][0]), "+v"(xg[2][1]), "+v"(xg[3][0]), "+v"(xg[3][1]) :: "memory");
            asm volatile("" : "+v"(wp[0]), "+v"(wp[1]), "+v"(wp[2]), "+v"(wp[3]));
            STAMP(44);
            f32x4 a[4];
#pragma unroll
            for (int j = 0; j < 4; ++j) a[j] = f32x4{0.f, 0.f, 0.f, 0.f};
#pragma unroll
            for (int k = 0; k < 16; ++k) {
#pragma unroll
                for (int j = 0; j < 4; ++j) a[j] = mfma(diagA(xg[j][k >> 3][(k >> 1) & 3], sel.s[k & 1]), gw[k], a[j]);
                PIN2(a[0], a[1]); PIN2(a[2], a[3]);
            }
            if (c < 14) {
#pragma unroll
                for (int j = 0; j < 4; ++j)
                    *(LAS f32x4*)(L + GU_OFF + ((((w - 4) * 8 + 2 * j + (g >> 1)) * FS) + 8 * c + 4 * (g & 1)) * 4) = a[j];
            }
            arrive(ctl + C_GU, lane);
            STAMP(56);
        }
    }
    STAMP(4);
    float lsum;
    mx = wave_max(mx);
    {
        const f32x4 sa = *(const LAS f32x4*)(sc + 8 * lane), sb = *(const LAS f32x4*)(sc + 8 * lane + 4);
        float e[8];
#pragma unroll
        for (int i = 0; i < 4; ++i) { e[i] = __expf(sa[i] - mx); e[4 + i] = __expf(sb[i] - mx); }
        lsum = wave_sum(((e[0] + e[1]) + (e[2] + e[3])) + ((e[4] + e[5]) + (e[6] + e[7])));
        unsigned pk[4];
#pragma unroll
        for (int i = 0; i < 4; ++i) pk[i] = pack2(e[i], e[4 + i]);
        LGKM0();
#pragma unroll
        for (int i = 0; i < 4; ++i) *(LAS unsigned*)((LAS unsigned char*)sc + (lane >> 3) * 128 + i * 32 + (lane & 7) * 4) = pk[i];
    }
    if (lane == 0) { ((LAS float*)ctl)[C_WMAX + w] = mx; ((LAS float*)ctl)[C_WSUM + w] = lsum; }
    arrive(ctl + C_SM, lane);
    STAMP(5);

    f32x4 acco = {0.f, 0.f, 0.f, 0.f}, acco1 = {0.f, 0.f, 0.f, 0.f}, accb = {0.f, 0.f, 0.f, 0.f};
    float fw = 0.f, gate = 0.f;
    {
        const unsigned vlin = lane * 16;
        {
            take_turn(64 + u);
            fill(vb + toff(0), vlin, vlin, vlin, vlin, myslot_a);
            issued(64 + u);
            STAMP(24);
            const int voo = 32 * g * (DM * 2) + c * 16;
#pragma unroll
            for (int r = 0; r < 4; ++r) wor[28 + r] = __builtin_amdgcn_raw_buffer_load_b128(ro, voo, soo + (28 + r) * (DM * 2), WO_AUX);
            slot_rows64<4>(myslot, (const LAS unsigned char*)sc, sel, c, g, acco, acco1, []() {}, landed);
            asm volatile("s_waitcnt vmcnt(0)" : "+v"(wor[28]), "+v"(wor[29]), "+v"(wor[30]), "+v"(wor[31]) :: "memory");
            STAMP(32);
        }
        const __amdgpu_buffer_rsrc_t rd = __builtin_amdgcn_make_buffer_rsrc((void*)wd, 0, FF * DM * 2, 0x00020000);
        const int vod = 28 * g * (DM * 2) + c * 16;
#pragma unroll 1
        for (int t = 1; t < 8; ++t) {
            take_turn(64 + 8 * t + u);
            fill(vb + toff(t), vlin, vlin, vlin, vlin, myslot_a);
            issued(64 + 8 * t + u);
            STAMP(24 + t);
            const int tt = t - 1;
            const int sod = (fs * FS + 4 * tt) * (DM * 2) + w * 256;
            v4u dw[4];
#pragma unroll
            for (int r = 0; r < 4; ++r) dw[r] = __builtin_amdgcn_raw_buffer_load_b128(rd, vod, sod + r * (DM * 2), WD_AUX);
            if (t == 1) {
                wait_ge(ctl + C_FFN, NW);
                const int l2 = fresh_lane();
                if (l2 < FS / 2) {
                    float g0 = 0.f, g1 = 0.f, u0 = 0.f, u1 = 0.f;
#pragma unroll
                    for (int ww = 0; ww < NW; ++ww) {
                        const f32x2 a = *(const LAS f32x2*)(L + GU_OFF + ww * 896 + 8 * l2), bb = *(const LAS f32x2*)(L + GU_OFF + ww * 896 + 448 + 8 * l2);
                        g0 += a[0]; g1 += a[1]; u0 += bb[0]; u1 += bb[1];
                    }
                    ((LAS unsigned*)(L + HH_OFF))[l2] = pack2(gelu_tanh(rbf(g0)) * rbf(u0), gelu_tanh(rbf(g1)) * rbf(u1));
                }
                wait_ge(ctl + C_SM, NW);
                float m[NW], M = -INFINITY, Ls = 0.f, z = 0.f;
#pragma unroll
                for (int ww = 0; ww < NW; ++ww) { m[ww] = ((LAS float*)ctl)[C_WMAX + ww]; M = fmaxf(M, m[ww]); }
#pragma unroll
                for (int ww = 0; ww < NW; ++ww) { Ls += __expf(m[ww] - M) * ((LAS float*)ctl)[C_WSUM + ww]; z += ((LAS float*)ctl)[C_GP + ww]; }
                fw = __expf(mx - M) / Ls;
                gate = rbf(1.0f / (1.0f + __expf(-rbf(z))));
                LGKM0();
            }
            unsigned pf = 0u;
            if (CODE_PF2 && t == CODE_PF2 && w == 0 && lane < 16) pf = code_prefetch(lane);
            const v2u hs = *(const LAS v2u*)(L + HH_OFF + (28 * g + 4 * tt) * 2);
            slot_rows64<4>(myslot, (const LAS unsigned char*)sc + t * 128, sel, c, g, acco, acco1, []() {}, landed);
            asm volatile("s_waitcnt vmcnt(0)" : "+v"(dw[0]), "+v"(dw[1]), "+v"(dw[2]), "+v"(dw[3]), "+v"(pf) :: "memory");
            STAMP(32 + t);
#pragma unroll
            for (int r = 0; r < 4; ++r) accb = mfma(diagA(hs[r >> 1], sel.s[r & 1]), dw[r], accb);
        }
    }
    STAMP(6);
    acco = (acco + acco1) * fw;
    if (g < 2) *(LAS f32x4*)((LAS unsigned char*)sc + 1024 + (8 * c + 4 * g) * 4) = acco;
    arrive(ctl + C_O, lane);
    wait_ge(ctl + C_O, NW);
    v4u of[4];
    {
        float s0 = 0.f, s1 = 0.f;
#pragma unroll
        for (int ww = 0; ww < NW; ++ww) {
            const f32x2 v = *(const LAS f32x2*)(L + SC_OFF + ww * 2048 + 1024 + 8 * lane);
            s0 += v[0]; s1 += v[1];
        }
        ((LAS unsigned*)(L + O_OFF))[lane] = pack2(s0, s1);
        LGKM0();
#pragma unroll
        for (int k = 0; k < 4; ++k) of[k] = *(const LAS v4u*)(L + O_OFF + 64 * g + 16 * k);
    }
    STAMP(7);
    f32x4 acca, ac[4];
#pragma unroll
    for (int k = 0; k < 4; ++k) ac[k] = f32x4{0.f, 0.f, 0.f, 0.f};
#pragma unroll
    for (int t = 0; t < 32; t += 4) {
#pragma unroll
        for (int k = 0; k < 4; ++k) ac[k] = mfma(diagA(of[t >> 3][((t + k) >> 1) & 3], sel.s[k & 1]), wor[t + k], ac[k]);
    }
    acca = (ac[0] + ac[1]) + (ac[2] + ac[3]);
    STAMP(40);
    {
        const float omg = rbf(1.0f - gate);
        {
            const float r0 = gate * (g < 2 ? acca[0] : acca[2]) + omg * (g < 2 ? accb[0] : accb[2]);
            const float r1 = gate * (g < 2 ? acca[1] : acca[3]) + omg * (g < 2 ? accb[1] : accb[3]);
            store_dev(v4u{gran(r0, tag_lo), tag_hi, gran(r1, tag_lo), tag_hi}, rgran, gvo, (int)rfl((unsigned)gso));
        }
        const int l2 = fresh_lane();
        const int vo = ((l2 >> 4) * DM + 2 * (l2 & 15)) * 8, so = ((b * NH + 4 * w) * DM + 32 * h) * 8;
        v4u y, ya, yb;
        auto poll = [&](v4u& d) { asm volatile("buffer_load_dwordx4 %0, %1, %2, %3 offen sc1" : "=v"(d) : "v"(vo), "s"(rgran), "s"(so) : "memory"); };
        auto good = [&](const v4u& d) { return __all(((d[1] ^ tag_hi) | (d[3] ^ tag_hi) | (((d[0] ^ tag_lo) | (d[2] ^ tag_lo)) & 0xffu)) == 0u); };
        poll(ya);
        __builtin_amdgcn_s_sleep(POLL_GAP);
        poll(yb);
        for (unsigned s = 0;; ++s) {
            asm volatile("s_waitcnt vmcnt(1)" : "+v"(ya)::"memory");
            if (good(ya) || s > SPIN_CAP) { y = ya; break; }
            poll(ya);
            asm volatile("s_waitcnt vmcnt(1)" : "+v"(yb)::"memory");
            if (good(yb)) { y = yb; break; }
            poll(yb);
        }
        STAMP(41);
        float v0 = __builtin_bit_cast(float, y[0] & 0xffffff00u), v1 = __builtin_bit_cast(float, y[2] & 0xffffff00u);
        v0 += __shfl_xor(v0, 16); v1 += __shfl_xor(v1, 16);
        v0 += __shfl_xor(v0, 32); v1 += __shfl_xor(v1, 32);
        if (l2 < 16) *(LAS f32x2*)(L + GU_OFF + w * 128 + l2 * 8) = f32x2{v0, v1};
        LGKM0();
        unsigned old = 0u;
        if (l2 == 0) old = __hip_atomic_fetch_add(ctl + C_OUT, 1u, RLX_WG);
        if (rfl(old) == NW - 1) {
            CBAR();
            if (l2 < 16) {
                float s0 = 0.f, s1 = 0.f;
#pragma unroll
                for (int ww = 0; ww < NW; ++ww) { const f32x2 v = *(const LAS f32x2*)(L + GU_OFF + ww * 128 + l2 * 8); s0 += v[0]; s1 += v[1]; }
                __hip_atomic_store((gu32*)(out + ((size_t)b * DM + 32 * h + 2 * l2) * 2), pack2(s0, s1), __ATOMIC_RELAXED, __HIP_MEMORY_SCOPE_SYSTEM);
            }
            if (bh == 0 && l2 == 0) __hip_atomic_store((gu64*)(ws + WS_EPOCH), epoch, RLX_AGENT);
        }
        STAMP(57);
        asm volatile("" ::"v"(ya), "v"(yb));
    }
    SDUMP();
    asm volatile(".Lfdk_end:");
}

}

extern "C" void kernel_launch(void* const* d_in, const int* in_sizes, int n_in, void* d_out, int out_size, void* d_ws, size_t ws_size,
                              hipStream_t stream) {
    (void)in_sizes; (void)n_in; (void)out_size; (void)ws_size;
    (void)hipFuncSetAttribute((const void*)fused_decode_kernel, hipFuncAttributeMaxDynamicSharedMemorySize, LDS_BYTES);
    int pace = PACE, tstart = TSTART, depth = DEPTH;
    fused_decode_kernel<<<dim3(NB * NH), dim3(NTHR), LDS_BYTES, stream>>>(
        (const unsigned char*)d_in[0], (const unsigned char*)d_in[1], (const unsigned char*)d_in[2], (const unsigned char*)d_in[3],
        (const unsigned char*)d_in[4], (const unsigned char*)d_in[6], (const unsigned char*)d_in[7],
        (const unsigned char*)d_in[8], (const unsigned char*)d_in[9], (unsigned char*)d_out, (unsigned char*)d_ws, pace, tstart, depth);
}
```

```cpp
#include <hip/hip_runtime.h>
#include <hip/hip_bf16.h>

#define GAS __attribute__((address_space(1)))
#define LAS __attribute__((address_space(3)))
typedef unsigned v4u __attribute__((ext_vector_type(4)));
typedef unsigned v2u __attribute__((ext_vector_type(2)));
typedef float f32x4 __attribute__((ext_vector_type(4)));
typedef float f32x2 __attribute__((ext_vector_type(2)));
typedef short bf16x8 __attribute__((ext_vector_type(8)));
typedef GAS unsigned gu32;
typedef GAS unsigned long long gu64;
#define RLX_AGENT __ATOMIC_RELAXED, __HIP_MEMORY_SCOPE_AGENT
#define RLX_WG __ATOMIC_RELAXED, __HIP_MEMORY_SCOPE_WORKGROUP

namespace {

constexpr int NB = 8, DM = 1024, NH = 32, SK = 4096, DH = 128, FF = 3584;
constexpr int FS = FF / NH;
constexpr int NW = 8, NTHR = 64 * NW;
constexpr int SLOT = 16384;
#define GUNT " nt"
#define ROTK 0
#define PRE 1
#define GU_T 2
#define WO_AUX 0
#define WD_AUX 0
#define POLL_SLEEP 1
#define QNT " nt"
#define CODE_PF 1
#define CODE_PF2 5
#define POLL_GAP 10
#define HOLE1 65
#define HOLE2 5
#define PACEV 928
#define INVAL 0
#define PACE 960
#define TSTART 600
#define DEPTH 4

constexpr int SC_OFF = NW * SLOT;
constexpr int GU_OFF = SC_OFF + NW * 2048;
constexpr int Q_OFF = GU_OFF + 14336;
constexpr int O_OFF = Q_OFF + 256;
constexpr int HH_OFF = O_OFF + 256;
constexpr int CTL_OFF = HH_OFF + 256;
constexpr int LDS_BYTES = CTL_OFF + 256;
constexpr int C_ISS = 0, C_LAND = 1, C_FFN = 2, C_O = 3, C_QP = 4, C_QR = 5, C_GU = 6, C_OUT = 7, C_TAG = 8, C_POLL = 10, C_SM = 11, C_WMAX = 16, C_WSUM = 24, C_GP = 32;
static_assert(LDS_BYTES <= 160 * 1024, "LDS");

constexpr size_t WS_EPOCH = 0, WS_GRAN = 4096, WS_QGRAN = WS_GRAN + (size_t)NB * NH * DM * 8, WS_GUGRAN = WS_QGRAN + (size_t)NH * NB * NB * DH * 8;

constexpr unsigned SPIN_CAP = 1u << 22;

__device__ __forceinline__ f32x4 mfma(v4u a, v4u b, f32x4 c) {
    return __builtin_amdgcn_mfma_f32_16x16x32_bf16(__builtin_bit_cast(bf16x8, a), __builtin_bit_cast(bf16x8, b), c, 0, 0, 0);
}
__device__ __forceinline__ float bf_lo(unsigned u) { return __builtin_bit_cast(float, u << 16); }
__device__ __forceinline__ float bf_hi(unsigned u) { return __builtin_bit_cast(float, u & 0xffff0000u); }
__device__ __forceinline__ float rbf(float f) { return (float)(__bf16)f; }
__device__ __forceinline__ unsigned bf_bits(float f) { return (unsigned)__builtin_bit_cast(unsigned short, (__bf16)f); }
__device__ __forceinline__ unsigned pack2(float lo, float hi) { return bf_bits(lo) | (bf_bits(hi) << 16); }
__device__ __forceinline__ unsigned gran(float f, unsigned tag_lo) { return ((__builtin_bit_cast(unsigned, f) + 0x80u) & 0xffffff00u) | tag_lo; }
__device__ __forceinline__ void store_dev(v4u d, __amdgpu_buffer_rsrc_t r, int vo, int so) {
    __builtin_amdgcn_raw_buffer_store_b128(d, r, vo, so, 16);
    asm volatile("s_nop 1" ::"v"(d) : "memory");
}
__device__ __forceinline__ unsigned long long rt() { return __builtin_amdgcn_s_memrealtime(); }
__device__ __forceinline__ unsigned rfl(unsigned v) { return (unsigned)__builtin_amdgcn_readfirstlane((int)v); }
#define LGKM0() asm volatile("s_waitcnt lgkmcnt(0)" ::: "memory")
#define VMCNT(n) asm volatile("s_waitcnt vmcnt(%0)" ::"i"(n) : "memory")
#define CBAR() asm volatile("" ::: "memory")
#define PIN2(a, b) do { asm volatile("" : "+v"(a), "+v"(b)); __builtin_amdgcn_sched_barrier(0); } while (0)

__device__ __forceinline__ void glds4(const void* sbase, unsigned v0, unsigned v1, unsigned v2, unsigned v3, unsigned lds) {
    unsigned keep;
    asm volatile("s_mov_b32 %0, m0\n\ts_mov_b32 m0, %6\n\ts_nop 0\n\t"
                 "global_load_lds_dwordx4 %1, %5 nt\n\t"
                 "global_load_lds_dwordx4 %2, %5 offset:1024 nt\n\t"
                 "global_load_lds_dwordx4 %3, %5 offset:2048 nt\n\t"
                 "global_load_lds_dwordx4 %4, %5 offset:3072 nt\n\t"
                 "s_mov_b32 m0, %0"
                 : "=&s"(keep) : "v"(v0), "v"(v1), "v"(v2), "v"(v3), "s"(sbase), "s"(lds) : "memory");
}
__device__ __forceinline__ void fill(const unsigned char* src, unsigned v0, unsigned v1, unsigned v2, unsigned v3, unsigned lds) {
    LGKM0();
#pragma unroll
    for (int q = 0; q < 4; ++q) glds4(src + q * 4096, v0, v1, v2, v3, lds + q * 4096);
}

__device__ __forceinline__ void wait_ge(LAS unsigned* w, unsigned need) {
    for (unsigned s = 0; rfl(*(volatile LAS unsigned*)w) < need && s < SPIN_CAP; ++s) __builtin_amdgcn_s_sleep(0);
    CBAR();
}
__device__ __forceinline__ void arrive(LAS unsigned* w, int lane) {
    LGKM0();
    if (lane == 0) __hip_atomic_fetch_add(w, 1u, RLX_WG);
    CBAR();
}

struct Sel { unsigned s[2][4]; };
__device__ __forceinline__ v4u diagA(unsigned S, const unsigned (&sel)[4]) {
    return v4u{__builtin_amdgcn_perm(0u, S, sel[0]), __builtin_amdgcn_perm(0u, S, sel[1]), __builtin_amdgcn_perm(0u, S, sel[2]), __builtin_amdgcn_perm(0u, S, sel[3])};
}
__device__ __forceinline__ int fresh_lane() {
    int l;
    asm volatile("v_mbcnt_lo_u32_b32 %0, -1, 0\n\tv_mbcnt_hi_u32_b32 %0, -1, %0" : "=v"(l));
    __builtin_assume((unsigned)l < 64u);
    return l;
}
__device__ __forceinline__ Sel make_sel(int c) {
    Sel sel;
    const int dm = (c & 7) >> 1, hf = c & 1;
#pragma unroll
    for (int par = 0; par < 2; ++par)
#pragma unroll
        for (int d = 0; d < 4; ++d) {
            const unsigned lohi = (unsigned)(2 * par) | ((unsigned)(2 * par + 1) << 8);
            const unsigned v = hf ? (0x0c0cu | (lohi << 16)) : (0x0c0c0000u | lohi);
            sel.s[par][d] = (c < 8 && d == dm) ? v : 0x0c0c0c0cu;
        }
    return sel;
}

template <int LATER, typename F, typename G>
__device__ __forceinline__ void slot_rows64(const LAS unsigned char* slot, const LAS unsigned char* vec, const Sel& sel, int c, int g,
                                            f32x4& acc, f32x4& a1, F&& last_wait, G&& all_landed) {
    const LAS unsigned char* base = slot + g * 256 + c * 16;
    const v4u s0 = *(const LAS v4u*)(vec + g * 32), s1 = *(const LAS v4u*)(vec + g * 32 + 16);
#pragma unroll
    for (int q = 0; q < 4; ++q) {
        VMCNT(LATER + 12 - 4 * q);
        v4u bb[4];
#pragma unroll
        for (int t = 0; t < 4; ++t) bb[t] = *(const LAS v4u*)(base + (4 * q + t) * 1024);
        if (q == 3) { last_wait(); all_landed(); }
        __builtin_amdgcn_sched_barrier(0);
#pragma unroll
        for (int u = 0; u < 2; ++u) {
            const unsigned S = (q < 2 ? s0 : s1)[2 * (q & 1) + u];
            acc = mfma(diagA(S, sel.s[0]), bb[2 * u], acc);
            a1 = mfma(diagA(S, sel.s[1]), bb[2 * u + 1], a1);
            PIN2(acc, a1);
        }
    }
}

__device__ __forceinline__ float gelu_tanh(float x) {
    const float y = 0.7978845608028654f * (x + 0.044715f * x * x * x);
    return rbf(x / (1.0f + __expf(-2.0f * y)));
}
__device__ __forceinline__ float wave_max(float v) {
#pragma unroll
    for (int o = 32; o >= 1; o >>= 1) v = fmaxf(v, __shfl_xor(v, o));
    return v;
}
__device__ __forceinline__ float wave_sum(float v) {
#pragma unroll
    for (int o = 32; o >= 1; o >>= 1) v += __shfl_xor(v, o);
    return v;
}

__global__ void __launch_bounds__(NTHR)
fused_decode_kernel(const unsigned char* __restrict__ xin, const unsigned char* __restrict__ kc, const unsigned char* __restrict__ vc,
                    const unsigned char* __restrict__ wq, const unsigned char* __restrict__ wo,
                    const unsigned char* __restrict__ wg, const unsigned char* __restrict__ wu, const unsigned char* __restrict__ wd,
                    const unsigned char* __restrict__ gatew, unsigned char* __restrict__ out, unsigned char* __restrict__ ws,
                    int pace, int tstart, int depth) {
    extern __shared__ __attribute__((aligned(16))) unsigned char smem[];
    LAS unsigned char* L = (LAS unsigned char*)smem;
    LAS unsigned* ctl = (LAS unsigned*)(L + CTL_OFF);
    asm volatile(".Lfdk_begin:");
    const unsigned long long t0 = rt();
    asm volatile("" ::"s"(xin), "s"(kc), "s"(vc), "s"(wq), "s"(wo), "s"(wg), "s"(wu), "s"(wd), "s"(gatew), "s"(out), "s"(ws), "s"(pace), "s"(tstart), "s"(depth));
    const int w = __builtin_amdgcn_readfirstlane((int)threadIdx.x >> 6);
    const int h = (blockIdx.x & 7) * 4 + ((blockIdx.x >> 3) & 3), b = blockIdx.x >> 5;
    const int fs = h, bh = b * NH + h;
    const unsigned myslot_a = (unsigned)(size_t)L + w * SLOT;
    const LAS unsigned char* myslot = L + w * SLOT;
    const unsigned rot = (unsigned)(ROTK) + (w ^ 4);
    auto toff = [&](int t) { return (size_t)(((8u * t + rot) & 63u) * SLOT); };
    int lane, c, g;
    unsigned vk0, vk1, vk2, vk3;
    Sel sel;
    const unsigned char* kb;
    auto setup = [&]() {
        lane = fresh_lane(); c = lane & 15; g = lane >> 4;
        vk0 = g * 256 + ((c ^ g) << 4); vk1 = g * 256 + ((c ^ (4 + g)) << 4); vk2 = g * 256 + ((c ^ (8 + g)) << 4); vk3 = g * 256 + ((c ^ (12 + g)) << 4);
        sel = make_sel(c & 7);
        int bh2 = bh;
        asm volatile("" : "+s"(bh2));
        kb = kc + (size_t)bh2 * (SK * DH * 2);
    };
    auto code_prefetch = [&](int l) {
        unsigned long long pc;
        unsigned off, len, v = 0u;
        asm volatile("s_getpc_b64 %0\n.Lfdk_pc%=:\n\ts_mov_b32 %1, .Lfdk_pc%=-.Lfdk_begin\n\ts_mov_b32 %2, .Lfdk_end-.Lfdk_begin" : "=s"(pc), "=s"(off), "=s"(len));
        const unsigned o = (((blockIdx.x >> 3) & 31u) + 32u * (unsigned)l) * 128u;
        if (o < len) asm volatile("global_load_dword %0, %1, %2" : "+v"(v) : "v"(o), "s"(pc - off) : "memory");
        return v;
    };
    const int u = w ^ 4;
    auto take_turn = [&](int f) {
        const int hole = HOLE1 * min(max(f - 8 * GU_T, 0), 4) + HOLE2 * min(max(f - 56, 0), 8);
        const unsigned long long td = t0 + (unsigned)(tstart + hole + (((min(f, 64) - 8) * pace + max(f - 64, 0) * PACEV) >> 4));
        while (rt() < td) __builtin_amdgcn_s_sleep(1);
        for (unsigned s = 0; s < SPIN_CAP; ++s) {
            const v2u st = *(volatile LAS v2u*)(ctl + C_ISS);
            if (rfl(st[0]) >= (unsigned)f && rfl(st[1]) + (unsigned)depth > (unsigned)f) break;
            __builtin_amdgcn_s_sleep(0);
        }
        CBAR();
    };
    auto issued = [&](int f) { if (lane == 0) *(volatile LAS unsigned*)(ctl + C_ISS) = (unsigned)f + 1u; CBAR(); };
    auto landed = [&]() { if (lane == 0) __hip_atomic_fetch_add(ctl + C_LAND, 1u, RLX_WG); CBAR(); };
    LAS float* sc = (LAS float*)(L + SC_OFF + w * 2048);
    unsigned long long epoch;

    if (__builtin_expect(w < 4, 1)) {
        v4u x8[4], wr[8];
        v2u ep;
        const unsigned l0 = threadIdx.x & 63;
        const unsigned char* qs = wq + (size_t)(128 * b + 32 * w) * (NH * DH * 2) + h * 256;
        const unsigned vq = (l0 >> 4) * 8 * (NH * DH * 2) + (l0 & 15) * 16;
#pragma unroll
        for (int t = 0; t < 8; ++t)
            asm volatile("global_load_dwordx4 %0, %1, %2" QNT : "=v"(wr[t]) : "v"(vq), "s"(qs + (size_t)t * (NH * DH * 2)) : "memory");
        __builtin_amdgcn_sched_barrier(0);
        const unsigned char* xs = xin + (128 * b + 32 * w) * 2;
        const unsigned vx = ((l0 >> 3) & 1) * (DM * 2) + (l0 >> 4) * 16;
#pragma unroll
        for (int j = 0; j < 4; ++j)
            asm volatile("global_load_dwordx4 %0, %1, %2" : "=v"(x8[j]) : "v"(vx), "s"(xs + j * (2 * DM * 2)) : "memory");
        asm volatile("global_load_dwordx2 %0, %1, %2 sc1" : "=v"(ep) : "v"(0u), "s"(ws + WS_EPOCH) : "memory");
        const unsigned char* inbox = ws + WS_QGRAN + (size_t)(h * NB + b) * NB * (DH * 8);
        unsigned warm;
        asm volatile("global_load_dword %0, %1, %2 sc1" : "=v"(warm) : "v"(l0 * 16), "s"(inbox) : "memory");
        __builtin_amdgcn_s_barrier();
        setup();
        VMCNT(0);
        asm volatile("" : "+v"(x8[0]), "+v"(x8[1]), "+v"(x8[2]), "+v"(x8[3]), "+v"(wr[0]), "+v"(wr[1]), "+v"(wr[2]), "+v"(wr[3]),
                     "+v"(wr[4]), "+v"(wr[5]), "+v"(wr[6]), "+v"(wr[7]), "+v"(warm), "+v"(ep) :: "memory");
        {
            const unsigned e0 = ep[0], e1 = ep[1];
            epoch = (((unsigned long long)rfl(e1) << 32) | rfl(e0)) + 1ull;
        }
        const unsigned qtag_hi = (unsigned)(epoch >> 8), qtag_lo = (unsigned)epoch & 0xffu;
#pragma unroll
        for (int j = 0; j < 4; ++j) {
            f32x4 a0 = {0.f, 0.f, 0.f, 0.f}, a1 = {0.f, 0.f, 0.f, 0.f};
#pragma unroll
            for (int k = 0; k < 4; ++k) {
                a0 = mfma(diagA(x8[j][k], sel.s[0]), wr[2 * k], a0);
                a1 = mfma(diagA(x8[j][k], sel.s[1]), wr[2 * k + 1], a1);
            }
            a0 += a1;
            *(LAS f32x4*)(L + SC_OFF + ((w * 8 + 2 * j + (g >> 1)) * 128 + 8 * c + 4 * (g & 1)) * 4) = a0;
        }
        if (w == 0 && lane == 0) *(LAS v2u*)(ctl + C_TAG) = v2u{(unsigned)epoch, (unsigned)(epoch >> 32)};
        arrive(ctl + C_QP, lane);
        wait_ge(ctl + C_QP, 4);
        {
            const __amdgpu_buffer_rsrc_t rqg = __builtin_amdgcn_make_buffer_rsrc((void*)(ws + WS_QGRAN), 0, NH * NB * NB * DH * 8, 0x00020000);
#pragma unroll
            for (int r = 0; r < 2; ++r) {
                float s0 = 0.f, s1 = 0.f;
#pragma unroll
                for (int k = 0; k < 4; ++k) { const f32x2 v = *(const LAS f32x2*)(L + SC_OFF + ((k * 8 + 2 * w + r) * 128 + 2 * lane) * 4); s0 += v[0]; s1 += v[1]; }
                store_dev(v4u{gran(s0, qtag_lo), qtag_hi, gran(s1, qtag_lo), qtag_hi}, rqg, lane * 16, ((h * NB + 2 * w + r) * NB + b) * (DH * 8));
            }
        }
        arrive(ctl + C_QP, lane);
        if (w == 3) {
            VMCNT(0);
            const unsigned vo = lane * 16;
            v4u y[NB];
            for (unsigned s = 0;; ++s) {
#pragma unroll
                for (int p = 0; p < NB; ++p)
                    asm volatile("global_load_dwordx4 %0, %1, %2 offset:%3 sc1" : "=v"(y[p]) : "v"(vo), "s"(inbox + (p >> 2) * 4096), "i"((p & 3) * 1024) : "memory");
                if (s == 0) {
                    if (lane == 0) *(volatile LAS unsigned*)(ctl + C_POLL) = 1u;
                    fill(kb + toff(0), vk0, vk1, vk2, vk3, myslot_a);
                    VMCNT(16);
                } else {
                    VMCNT(0);
                }
                asm volatile("" : "+v"(y[0]), "+v"(y[1]), "+v"(y[2]), "+v"(y[3]), "+v"(y[4]), "+v"(y[5]), "+v"(y[6]), "+v"(y[7]) :: "memory");
                unsigned bad = 0u;
#pragma unroll
                for (int p = 0; p < NB; ++p) bad |= (y[p][1] ^ qtag_hi) | (y[p][3] ^ qtag_hi) | (((y[p][0] ^ qtag_lo) | (y[p][2] ^ qtag_lo)) & 0xffu);
                if (__all(bad == 0u) || s > SPIN_CAP) break;
                __builtin_amdgcn_s_sleep(POLL_SLEEP);
            }
            float s0 = 0.f, s1 = 0.f;
#pragma unroll
            for (int p = 0; p < NB; ++p) { s0 += __builtin_bit_cast(float, y[p][0] & 0xffffff00u); s1 += __builtin_bit_cast(float, y[p][2] & 0xffffff00u); }
            ((LAS unsigned*)(L + Q_OFF))[lane] = pack2(s0, s1);
            wait_ge(ctl + C_QP, 8);
            arrive(ctl + C_QR, lane);
        } else {
            if (w >= PRE) wait_ge(ctl + C_POLL, 1);
            fill(kb + toff(0), vk0, vk1, vk2, vk3, myslot_a);
        }
        VMCNT(0);
        landed();
    } else {
        setup();
        if (w == 4 && lane < 16) ctl[lane] = lane == C_ISS ? 8u : 0u;
        unsigned pf = 0u;
        if (CODE_PF && w == 5 && lane < 16) pf = code_prefetch(lane);
        LGKM0();
        __builtin_amdgcn_s_barrier();
        fill(kb + toff(0), vk0, vk1, vk2, vk3, myslot_a);
        asm volatile("s_waitcnt vmcnt(0)" : "+v"(pf)::"memory");
        landed();
        wait_ge(ctl + C_QP, 4);
        const v2u e = *(volatile LAS v2u*)(ctl + C_TAG);
        const unsigned e0 = e[0], e1 = e[1];
        epoch = ((unsigned long long)rfl(e1) << 32) | rfl(e0);
    }
    const unsigned tag_hi = (unsigned)(epoch >> 8), tag_lo = (unsigned)epoch & 0xffu;
    const unsigned char* vb = vc + (size_t)bh * (SK * DH * 2);
    const __amdgpu_buffer_rsrc_t rgran = __builtin_amdgcn_make_buffer_rsrc((void*)(ws + WS_GRAN), 0, NB * NH * DM * 8, 0x00020000);
    const int gvo = (8 * c + 4 * (g & 1) + 2 * (g >> 1)) * 8, gso = bh * (DM * 8) + w * 1024;
    wait_ge(ctl + C_QR, 1);
    v4u qf[4];
#pragma unroll
    for (int s = 0; s < 4; ++s) qf[s] = *(const LAS v4u*)(L + Q_OFF + (32 * s + 8 * g) * 2);
    LGKM0();
    float mx = -INFINITY;
    v4u wor[32];
    const __amdgpu_buffer_rsrc_t ro = __builtin_amdgcn_make_buffer_rsrc((void*)wo, 0, DM * NH * DH * 2, 0x00020000);
    const int soo = h * DH * (DM * 2) + w * 256;
#pragma unroll
    for (int t = 0; t < 8; ++t) {
        if (t > 0) { take_turn(8 * t + u); fill(kb + toff(t), vk0, vk1, vk2, vk3, myslot_a); issued(8 * t + u); }
        v4u xg[4][2], gw[16];
        if (t > 0) {
            const int l2 = fresh_lane();
            const int voo = 32 * (l2 >> 4) * (DM * 2) + (l2 & 15) * 16;
            v4u* wp = wor + 4 * (t - 1);
#pragma unroll
            for (int r = 0; r < 4; ++r) wp[r] = __builtin_amdgcn_raw_buffer_load_b128(ro, voo, soo + (4 * (t - 1) + r) * (DM * 2), WO_AUX);
            if (t == 1) {
                const __amdgpu_buffer_rsrc_t rx = __builtin_amdgcn_make_buffer_rsrc((void*)xin, 0, NB * DM * 2, 0x00020000);
                const __amdgpu_buffer_rsrc_t rw = __builtin_amdgcn_make_buffer_rsrc((void*)gatew, 0, DM * 2, 0x00020000);
                v4u xv = __builtin_amdgcn_raw_buffer_load_b128(rx, (l2 & 15) * 16, b * (DM * 2) + w * 256, 0);
                v4u gv = __builtin_amdgcn_raw_buffer_load_b128(rw, (l2 & 15) * 16, w * 256, 0);
                asm volatile("s_waitcnt vmcnt(0)" : "+v"(wp[0]), "+v"(wp[1]), "+v"(wp[2]), "+v"(wp[3]), "+v"(xv), "+v"(gv) :: "memory");
                landed();
                if (INVAL) store_dev(v4u{tag_lo ^ 1u, ~tag_hi, tag_lo ^ 1u, ~tag_hi}, rgran, gvo, gso);
                float s = 0.f;
#pragma unroll
                for (int k = 0; k < 4; ++k) s += rbf(bf_lo(xv[k]) * bf_lo(gv[k])) + rbf(bf_hi(xv[k]) * bf_hi(gv[k]));
                s = wave_sum(l2 < 16 ? s : 0.f);
                if (l2 == 0) ((LAS float*)ctl)[C_GP + w] = s;
            } else if (t == GU_T && w >= 4) {
                const int m = (w >> 1) & 1, hf = w & 1;
                const unsigned char* gs = (m ? wu : wg) + (size_t)(128 * b + 64 * hf) * (FF * 2) + fs * (FS * 2);
                const unsigned vog = 16 * (l2 >> 4) * (FF * 2) + ((l2 & 15) < 14 ? (l2 & 15) : 13) * 16;
#pragma unroll
                for (int k = 0; k < 16; ++k)
                    asm volatile("global_load_dwordx4 %0, %1, %2" GUNT : "=v"(gw[k]) : "v"(vog), "s"(gs + (size_t)k * (FF * 2)) : "memory");
                const unsigned char* xs = xin + (128 * b + 64 * hf) * 2;
                const unsigned vox = ((l2 & 15) >> 3) * (DM * 2) + (l2 >> 4) * 32;
#pragma unroll
                for (int j = 0; j < 4; ++j) {
                    asm volatile("global_load_dwordx4 %0, %1, %2" : "=v"(xg[j][0]) : "v"(vox), "s"(xs + j * (2 * DM * 2)) : "memory");
                    asm volatile("global_load_dwordx4 %0, %1, %2 offset:16" : "=v"(xg[j][1]) : "v"(vox), "s"(xs + j * (2 * DM * 2)) : "memory");
                }
                VMCNT(24);
            } else if (t == GU_T + 1 && w >= 4) {
                asm volatile("s_waitcnt vmcnt(0)" : "+v"(wp[0]), "+v"(wp[1]), "+v"(wp[2]), "+v"(wp[3]) :: "memory");
                landed();
                wait_ge(ctl + C_GU, 4);
                const __amdgpu_buffer_rsrc_t rgg = __builtin_amdgcn_make_buffer_rsrc((void*)(ws + WS_GUGRAN), 0, NH * NB * NB * 2048, 0x00020000);
#pragma unroll
                for (int r = 0; r < 2; ++r)
#pragma unroll
                    for (int q = 0; q < 2; ++q) {
                        const int p = q == 0 ? l2 : (l2 < 48 ? 64 + l2 : 111);
                        const int mm = p >= 56 ? 1 : 0, col = 2 * (p - 56 * mm), cons = 2 * (w - 4) + r;
                        const f32x2 v0 = *(const LAS f32x2*)(L + GU_OFF + (((2 * mm) * 8 + cons) * FS + col) * 4);
                        const f32x2 v1 = *(const LAS f32x2*)(L + GU_OFF + (((2 * mm + 1) * 8 + cons) * FS + col) * 4);
                        const float s0 = v0[0] + v1[0], s1 = v0[1] + v1[1];
                        if (q == 0 || l2 < 48)
                            store_dev(v4u{gran(s0, tag_lo), tag_hi, gran(s1, tag_lo), tag_hi}, rgg, p * 16, ((h * NB + cons) * NB + b) * 2048);
                    }
                arrive(ctl + C_GU, l2);
            } else if (t == 7) {
                const __amdgpu_buffer_rsrc_t rgg = __builtin_amdgcn_make_buffer_rsrc((void*)(ws + WS_GUGRAN), 0, NH * NB * NB * 2048, 0x00020000);
                const int sgg = ((h * NB + b) * NB + w) * 2048, v1o = 1024 + (l2 < 48 ? l2 : 47) * 16;
                v4u y0, y1;
                for (unsigned s = 0;; ++s) {
                    y0 = __builtin_amdgcn_raw_buffer_load_b128(rgg, l2 * 16, sgg, 16);
                    y1 = __builtin_amdgcn_raw_buffer_load_b128(rgg, v1o, sgg, 16);
                    asm volatile("s_waitcnt vmcnt(0)" : "+v"(wp[0]), "+v"(wp[1]), "+v"(wp[2]), "+v"(wp[3]), "+v"(y0), "+v"(y1) :: "memory");
                    const unsigned bad = (y0[1] ^ tag_hi) | (y0[3] ^ tag_hi) | (y1[1] ^ tag_hi) | (y1[3] ^ tag_hi) | (((y0[0] ^ tag_lo) | (y0[2] ^ tag_lo) | (y1[0] ^ tag_lo) | (y1[2] ^ tag_lo)) & 0xffu);
                    if (s == 0) landed();
                    if (__all(bad == 0u) || s > SPIN_CAP) break;
                    __builtin_amdgcn_s_sleep(1);
                }
                wait_ge(ctl + C_GU, 8);
                *(LAS v2u*)(L + GU_OFF + w * 896 + l2 * 8) = v2u{y0[0] & 0xffffff00u, y0[2] & 0xffffff00u};
                if (l2 < 48) *(LAS v2u*)(L + GU_OFF + w * 896 + 512 + l2 * 8) = v2u{y1[0] & 0xffffff00u, y1[2] & 0xffffff00u};
                arrive(ctl + C_FFN, l2);
            } else {
                asm volatile("s_waitcnt vmcnt(0)" : "+v"(wp[0]), "+v"(wp[1]), "+v"(wp[2]), "+v"(wp[3]) :: "memory");
            }
            if (t != 7 && t != 1 && !(t == GU_T + 1 && w >= 4)) landed();
        }
        f32x4 s4[4];
#pragma unroll
        for (int kg = 0; kg < 4; ++kg) {
            f32x4 a = {0.f, 0.f, 0.f, 0.f};
#pragma unroll
            for (int s = 0; s < 4; ++s) {
                const v4u kf = *(const LAS v4u*)(myslot + (16 * kg + c) * 256 + (((4 * s + g) ^ c) << 4));
                a = mfma(kf, qf[s], a);
            }
            s4[kg] = a;
        }
        f32x4 v;
#pragma unroll
        for (int i = 0; i < 4; ++i) {
            const float lo = (c & 1) ? s4[1][i] : s4[0][i], hi = (c & 1) ? s4[3][i] : s4[2][i];
            v[i] = rbf(rbf((c & 2) ? hi : lo) * 0.08838834764831845f);
            mx = fmaxf(mx, v[i]);
        }
        if (c < 4) *(LAS f32x4*)(sc + t * 64 + 16 * c + 4 * g) = v;
        __builtin_amdgcn_sched_barrier(0);
        if (t == GU_T && w >= 4) {
            v4u* wp = wor + 4 * (t - 1);
            asm volatile("s_waitcnt vmcnt(0)" : "+v"(gw[0]), "+v"(gw[1]), "+v"(gw[2]), "+v"(gw[3]), "+v"(gw[4]), "+v"(gw[5]), "+v"(gw[6]), "+v"(gw[7]),
                         "+v"(gw[8]), "+v"(gw[9]), "+v"(gw[10]), "+v"(gw[11]), "+v"(gw[12]), "+v"(gw[13]), "+v"(gw[14]), "+v"(gw[15]),
                         "+v"(xg[0][0]), "+v"(xg[0][1]), "+v"(xg[1][0]), "+v"(xg[1][1]), "+v"(xg[2][0]), "+v"(xg[2][1]), "+v"(xg[3][0]), "+v"(xg[3][1]) :: "memory");
            asm volatile("" : "+v"(wp[0]), "+v"(wp[1]), "+v"(wp[2]), "+v"(wp[3]));
            f32x4 a[4];
#pragma unroll
            for (int j = 0; j < 4; ++j) a[j] = f32x4{0.f, 0.f, 0.f, 0.f};
#pragma unroll
            for (int k = 0; k < 16; ++k) {
#pragma unroll
                for (int j = 0; j < 4; ++j) a[j] = mfma(diagA(xg[j][k >> 3][(k >> 1) & 3], sel.s[k & 1]), gw[k], a[j]);
                PIN2(a[0], a[1]); PIN2(a[2], a[3]);
            }
            if (c < 14) {
#pragma unroll
                for (int j = 0; j < 4; ++j)
                    *(LAS f32x4*)(L + GU_OFF + ((((w - 4) * 8 + 2 * j + (g >> 1)) * FS) + 8 * c + 4 * (g & 1)) * 4) = a[j];
            }
            arrive(ctl + C_GU, lane);
        }
    }
    float lsum;
    mx = wave_max(mx);
    {
        const f32x4 sa = *(const LAS f32x4*)(sc + 8 * lane), sb = *(const LAS f32x4*)(sc + 8 * lane + 4);
        float e[8];
#pragma unroll
        for (int i = 0; i < 4; ++i) { e[i] = __expf(sa[i] - mx); e[4 + i] = __expf(sb[i] - mx); }
        lsum = wave_sum(((e[0] + e[1]) + (e[2] + e[3])) + ((e[4] + e[5]) + (e[6] + e[7])));
        unsigned pk[4];
#pragma unroll
        for (int i = 0; i < 4; ++i) pk[i] = pack2(e[i], e[4 + i]);
        LGKM0();
#pragma unroll
        for (int i = 0; i < 4; ++i) *(LAS unsigned*)((LAS unsigned char*)sc + (lane >> 3) * 128 + i * 32 + (lane & 7) * 4) = pk[i];
    }
    if (lane == 0) { ((LAS float*)ctl)[C_WMAX + w] = mx; ((LAS float*)ctl)[C_WSUM + w] = lsum; }
    arrive(ctl + C_SM, lane);
    f32x4 acco = {0.f, 0.f, 0.f, 0.f}, acco1 = {0.f, 0.f, 0.f, 0.f}, accb = {0.f, 0.f, 0.f, 0.f};
    float fw = 0.f, gate = 0.f;
    {
        const unsigned vlin = lane * 16;
        {
            take_turn(64 + u);
            fill(vb + toff(0), vlin, vlin, vlin, vlin, myslot_a);
            issued(64 + u);
            const int voo = 32 * g * (DM * 2) + c * 16;
#pragma unroll
            for (int r = 0; r < 4; ++r) wor[28 + r] = __builtin_amdgcn_raw_buffer_load_b128(ro, voo, soo + (28 + r) * (DM * 2), WO_AUX);
            slot_rows64<4>(myslot, (const LAS unsigned char*)sc, sel, c, g, acco, acco1, []() {}, landed);
            asm volatile("s_waitcnt vmcnt(0)" : "+v"(wor[28]), "+v"(wor[29]), "+v"(wor[30]), "+v"(wor[31]) :: "memory");
        }
        const __amdgpu_buffer_rsrc_t rd = __builtin_amdgcn_make_buffer_rsrc((void*)wd, 0, FF * DM * 2, 0x00020000);
        const int vod = 28 * g * (DM * 2) + c * 16;
#pragma unroll 1
        for (int t = 1; t < 8; ++t) {
            take_turn(64 + 8 * t + u);
            fill(vb + toff(t), vlin, vlin, vlin, vlin, myslot_a);
            issued(64 + 8 * t + u);
            const int tt = t - 1;
            const int sod = (fs * FS + 4 * tt) * (DM * 2) + w * 256;
            v4u dw[4];
#pragma unroll
            for (int r = 0; r < 4; ++r) dw[r] = __builtin_amdgcn_raw_buffer_load_b128(rd, vod, sod + r * (DM * 2), WD_AUX);
            if (t == 1) {
                wait_ge(ctl + C_FFN, NW);
                const int l2 = fresh_lane();
                if (l2 < FS / 2) {
                    float g0 = 0.f, g1 = 0.f, u0 = 0.f, u1 = 0.f;
#pragma unroll
                    for (int ww = 0; ww < NW; ++ww) {
                        const f32x2 a = *(const LAS f32x2*)(L + GU_OFF + ww * 896 + 8 * l2), bb = *(const LAS f32x2*)(L + GU_OFF + ww * 896 + 448 + 8 * l2);
                        g0 += a[0]; g1 += a[1]; u0 += bb[0]; u1 += bb[1];
                    }
                    ((LAS unsigned*)(L + HH_OFF))[l2] = pack2(gelu_tanh(rbf(g0)) * rbf(u0), gelu_tanh(rbf(g1)) * rbf(u1));
                }
                wait_ge(ctl + C_SM, NW);
                float m[NW], M = -INFINITY, Ls = 0.f, z = 0.f;
#pragma unroll
                for (int ww = 0; ww < NW; ++ww) { m[ww] = ((LAS float*)ctl)[C_WMAX + ww]; M = fmaxf(M, m[ww]); }
#pragma unroll
                for (int ww = 0; ww < NW; ++ww) { Ls += __expf(m[ww] - M) * ((LAS float*)ctl)[C_WSUM + ww]; z += ((LAS float*)ctl)[C_GP + ww]; }
                fw = __expf(mx - M) / Ls;
                gate = rbf(1.0f / (1.0f + __expf(-rbf(z))));
                LGKM0();
            }
            unsigned pf = 0u;
            if (CODE_PF2 && t == CODE_PF2 && w == 0 && lane < 16) pf = code_prefetch(lane);
            const v2u hs = *(const LAS v2u*)(L + HH_OFF + (28 * g + 4 * tt) * 2);
            slot_rows64<4>(myslot, (const LAS unsigned char*)sc + t * 128, sel, c, g, acco, acco1, []() {}, landed);
            asm volatile("s_waitcnt vmcnt(0)" : "+v"(dw[0]), "+v"(dw[1]), "+v"(dw[2]), "+v"(dw[3]), "+v"(pf) :: "memory");
#pragma unroll
            for (int r = 0; r < 4; ++r) accb = mfma(diagA(hs[r >> 1], sel.s[r & 1]), dw[r], accb);
        }
    }
    acco = (acco + acco1) * fw;
    if (g < 2) *(LAS f32x4*)((LAS unsigned char*)sc + 1024 + (8 * c + 4 * g) * 4) = acco;
    arrive(ctl + C_O, lane);
    wait_ge(ctl + C_O, NW);
    v4u of[4];
    {
        float s0 = 0.f, s1 = 0.f;
#pragma unroll
        for (int ww = 0; ww < NW; ++ww) {
            const f32x2 v = *(const LAS f32x2*)(L + SC_OFF + ww * 2048 + 1024 + 8 * lane);
            s0 += v[0]; s1 += v[1];
        }
        ((LAS unsigned*)(L + O_OFF))[lane] = pack2(s0, s1);
        LGKM0();
#pragma unroll
        for (int k = 0; k < 4; ++k) of[k] = *(const LAS v4u*)(L + O_OFF + 64 * g + 16 * k);
    }
    f32x4 acca, ac[4];
#pragma unroll
    for (int k = 0; k < 4; ++k) ac[k] = f32x4{0.f, 0.f, 0.f, 0.f};
#pragma unroll
    for (int t = 0; t < 32; t += 4) {
#pragma unroll
        for (int k = 0; k < 4; ++k) ac[k] = mfma(diagA(of[t >> 3][((t + k) >> 1) & 3], sel.s[k & 1]), wor[t + k], ac[k]);
    }
    acca = (ac[0] + ac[1]) + (ac[2] + ac[3]);
    {
        const float omg = rbf(1.0f - gate);
        {
            const float r0 = gate * (g < 2 ? acca[0] : acca[2]) + omg * (g < 2 ? accb[0] : accb[2]);
            const float r1 = gate * (g < 2 ? acca[1] : acca[3]) + omg * (g < 2 ? accb[1] : accb[3]);
            store_dev(v4u{gran(r0, tag_lo), tag_hi, gran(r1, tag_lo), tag_hi}, rgran, gvo, (int)rfl((unsigned)gso));
        }
        const int l2 = fresh_lane();
        const int vo = ((l2 >> 4) * DM + 2 * (l2 & 15)) * 8, so = ((b * NH + 4 * w) * DM + 32 * h) * 8;
        v4u y, ya, yb;
        auto poll = [&](v4u& d) { asm volatile("buffer_load_dwordx4 %0, %1, %2, %3 offen sc1" : "=v"(d) : "v"(vo), "s"(rgran), "s"(so) : "memory"); };
        auto good = [&](const v4u& d) { return __all(((d[1] ^ tag_hi) | (d[3] ^ tag_hi) | (((d[0] ^ tag_lo) | (d[2] ^ tag_lo)) & 0xffu)) == 0u); };
        poll(ya);
        __builtin_amdgcn_s_sleep(POLL_GAP);
        poll(yb);
        for (unsigned s = 0;; ++s) {
            asm volatile("s_waitcnt vmcnt(1)" : "+v"(ya)::"memory");
            if (good(ya) || s > SPIN_CAP) { y = ya; break; }
            poll(ya);
            asm volatile("s_waitcnt vmcnt(1)" : "+v"(yb)::"memory");
            if (good(yb)) { y = yb; break; }
            poll(yb);
        }
        float v0 = __builtin_bit_cast(float, y[0] & 0xffffff00u), v1 = __builtin_bit_cast(float, y[2] & 0xffffff00u);
        v0 += __shfl_xor(v0, 16); v1 += __shfl_xor(v1, 16);
        v0 += __shfl_xor(v0, 32); v1 += __shfl_xor(v1, 32);
        if (l2 < 16) *(LAS f32x2*)(L + GU_OFF + w * 128 + l2 * 8) = f32x2{v0, v1};
        LGKM0();
        unsigned old = 0u;
        if (l2 == 0) old = __hip_atomic_fetch_add(ctl + C_OUT, 1u, RLX_WG);
        if (rfl(old) == NW - 1) {
            CBAR();
            if (l2 < 16) {
                float s0 = 0.f, s1 = 0.f;
#pragma unroll
                for (int ww = 0; ww < NW; ++ww) { const f32x2 v = *(const LAS f32x2*)(L + GU_OFF + ww * 128 + l2 * 8); s0 += v[0]; s1 += v[1]; }
                __hip_atomic_store((gu32*)(out + ((size_t)b * DM + 32 * h + 2 * l2) * 2), pack2(s0, s1), __ATOMIC_RELAXED, __HIP_MEMORY_SCOPE_SYSTEM);
            }
            if (bh == 0 && l2 == 0) __hip_atomic_store((gu64*)(ws + WS_EPOCH), epoch, RLX_AGENT);
        }
        asm volatile("" ::"v"(ya), "v"(yb));
    }
    asm volatile(".Lfdk_end:");
}

}

extern "C" void kernel_launch(void* const* d_in, const int* in_sizes, int n_in, void* d_out, int out_size, void* d_ws, size_t ws_size,
                              hipStream_t stream) {
    (void)in_sizes; (void)n_in; (void)out_size; (void)ws_size;
    (void)hipFuncSetAttribute((const void*)fused_decode_kernel, hipFuncAttributeMaxDynamicSharedMemorySize, LDS_BYTES);
    int pace = PACE, tstart = TSTART, depth = DEPTH;
    fused_decode_kernel<<<dim3(NB * NH), dim3(NTHR), LDS_BYTES, stream>>>(
        (const unsigned char*)d_in[0], (const unsigned char*)d_in[1], (const unsigned char*)d_in[2], (const unsigned char*)d_in[3],
        (const unsigned char*)d_in[4], (const unsigned char*)d_in[6], (const unsigned char*)d_in[7],
        (const unsigned char*)d_in[8], (const unsigned char*)d_in[9], (unsigned char*)d_out, (unsigned char*)d_ws, pace, tstart, depth);
}
```
